```python
import torch

def _softmax(x, axis=-1):
    return torch.softmax(x, dim=axis)

def _gelu(x):
    return torch.nn.functional.gelu(x, approximate='tanh')

def _silu(x):
    return torch.nn.functional.silu(x)

def _sigmoid(x):
    return torch.sigmoid(x)

def _layernorm(x, axis=-1, eps=1e-05):
    m = x.mean(dim=axis, keepdim=True)
    v = x.var(dim=axis, keepdim=True, unbiased=False)
    return (x - m) / torch.sqrt(v + eps)

def _scatter_add(tgt, idx, src, axis=0):
    return tgt.clone().index_add_(axis, idx, src)

def _segment_sum(src, seg, n):
    out = torch.zeros((n,) + src.shape[1:], dtype=src.dtype)
    return out.index_add_(0, seg, src)

def _pad(x, pw):
    flat = tuple((v for pair in reversed(pw) for v in pair))
    return torch.nn.functional.pad(x, flat)
kernel_name = 'parallel_sum__compressed_kv_cla__mixer_gmlp__prompt'

def setup_inputs():

    _w = lambda a: (lambda t: t.to(torch.bfloat16) if t.is_floating_point() else t)(torch.as_tensor(a))

    def _A_setup():
        B, H, Sq, Sk, D, M = (1, 32, 2048, 2048, 128, 512)
        Dc = 4 * D
        return {'x': _w((lambda *s: torch.randn(*s, dtype=torch.bfloat16) * 0.05)(B, Sq, M)), 'c_kv': _w((lambda *s: torch.randn(*s, dtype=torch.bfloat16) * 1.6)(B, Sk, Dc)), 'w_q': _w((lambda *s: torch.randn(*s, dtype=torch.bfloat16) * 1.6)(M, H, D) * M ** (-0.5)), 'w_uk': _w((lambda *s: torch.randn(*s, dtype=torch.bfloat16) * 1.6)(Dc, H, D) * Dc ** (-0.5)), 'w_uv': _w((lambda *s: torch.randn(*s, dtype=torch.bfloat16) * 1.6)(Dc, H, D) * Dc ** (-0.5)), 'w_o': _w((lambda *s: torch.randn(*s, dtype=torch.bfloat16) * 1.6)(H, D, M) * (H * D) ** (-0.5))}
    _A_in = _A_setup()

    def _B_setup():
        B, S, D, F = (1, 2048, 512, 1376)
        Fh = F // 2
        return {'x': _w((lambda *s: torch.randn(*s, dtype=torch.bfloat16) * 0.05)(B, S, D)), 'w_in': _w((lambda *s: torch.randn(*s, dtype=torch.bfloat16) * 0.4)(D, 2 * Fh)), 'w_sp': _w(0.01 * (lambda *s: torch.randn(*s, dtype=torch.bfloat16) * 0.4)(S, S)), 'b_sp': _w(1.0 + 0.01 * (lambda *s: torch.randn(*s, dtype=torch.bfloat16) * 0.05)(S)), 'w_out': _w((lambda *s: torch.randn(*s, dtype=torch.bfloat16) * 0.05)(Fh, D))}
    _B_in = _B_setup()
    return {'A_x': _A_in['x'], 'A_c_kv': _A_in['c_kv'], 'A_w_q': _A_in['w_q'], 'A_w_uk': _A_in['w_uk'], 'A_w_uv': _A_in['w_uv'], 'A_w_o': _A_in['w_o'], 'B_x': _B_in['x'], 'B_w_in': _B_in['w_in'], 'B_w_sp': _B_in['w_sp'], 'B_b_sp': _B_in['b_sp'], 'B_w_out': _B_in['w_out']}

def reference(A_x, A_c_kv, A_w_q, A_w_uk, A_w_uv, A_w_o, B_x, B_w_in, B_w_sp, B_b_sp, B_w_out):

    def _A_ref(x, c_kv, w_q, w_uk, w_uv, w_o):
        B, Sq, M = (x.shape[0], x.shape[1], x.shape[2])
        Sk = c_kv.shape[1]
        H, D = (w_q.shape[1], w_q.shape[2])
        q = torch.einsum('bqm,mhd->bhqd', x, w_q)
        k = torch.einsum('bkc,chd->bhkd', c_kv, w_uk)
        v = torch.einsum('bkc,chd->bhkd', c_kv, w_uv)
        scores = torch.einsum('bhqd,bhkd->bhqk', q, k) * D ** (-0.5)
        off = Sk - Sq
        qi = torch.arange(Sq)[:, None]
        kj = torch.arange(Sk)[None, :]
        cmask = kj <= qi + off
        scores = torch.where(cmask[None, None], scores, -1000000000.0)
        p = _softmax(scores, axis=-1)
        ctx = torch.einsum('bhqk,bhkd->bhqd', p, v)
        return torch.einsum('bhqd,hdm->bqm', ctx, w_o)

    def _B_ref(x, w_in, w_sp, b_sp, w_out):
        B, S, D = x.shape
        F = w_in.shape[-1]
        Fh = F // 2
        z = _gelu(torch.einsum('bsd,df->bsf', x, w_in))
        z1 = z[..., :Fh].clone()
        z2 = _layernorm(z[..., Fh:], axis=-1)
        g = torch.einsum('ts,bsf->btf', w_sp, z2) + b_sp[None, :, None]
        return torch.einsum('bsf,fd->bsd', z1 * g, w_out)
    x = A_x
    xn = _layernorm(x, axis=-1)
    return x + _A_ref(xn, A_c_kv, A_w_q, A_w_uk, A_w_uv, A_w_o) + _B_ref(xn, B_w_in, B_w_sp, B_b_sp, B_w_out)
```

```cpp
#include <hip/hip_runtime.h>
#include <hip/hip_bf16.h>
#include <stdint.h>

#define LAS __attribute__((address_space(3)))
#define DI __device__ __forceinline__
typedef unsigned short bf16_t;
typedef short bf16x8 __attribute__((ext_vector_type(8)));
typedef short s16x4 __attribute__((ext_vector_type(4)));
typedef __bf16 bf16x2_t __attribute__((ext_vector_type(2)));
typedef float f32x16 __attribute__((ext_vector_type(16)));
typedef float f32x4 __attribute__((ext_vector_type(4)));
typedef float f32x2 __attribute__((ext_vector_type(2)));
typedef unsigned u32x4 __attribute__((ext_vector_type(4)));
typedef unsigned u32x2 __attribute__((ext_vector_type(2)));
typedef LAS unsigned char* lds_p;
typedef const LAS unsigned char* lds_cp;

constexpr int SEQ = 2048, DM = 512, NH = 32, FH = 688, FF = 1376;
constexpr int NKS = 299;
constexpr size_t WS_CNT = 0;
constexpr size_t WS_XN  = 4096;
constexpr size_t WS_QF  = WS_XN + (size_t)SEQ * DM * 2;
constexpr size_t WS_KVF = WS_QF + (size_t)NH * 64 * 8192;
constexpr size_t WS_Z1F = WS_KVF + (size_t)NH * 64 * 16384;
constexpr size_t WS_Z2R = WS_Z1F + (size_t)64 * 44 * 1024;
constexpr size_t WS_Z2N = WS_Z2R + (size_t)SEQ * 704 * 2;
constexpr size_t WS_GP  = WS_Z2N + (size_t)SEQ * 768 * 2;
constexpr size_t WS_AF  = WS_GP + (size_t)4 * 64 * 22 * 4096;
constexpr size_t WS_WOF = WS_AF + (size_t)304 * 64 * 1024;
constexpr size_t WS_END = WS_WOF + (size_t)304 * 16 * 1024;

#define MFMA32(a, b, c) __builtin_amdgcn_mfma_f32_32x32x16_bf16((a), (b), (c), 0, 0, 0)
#define SBAR() __builtin_amdgcn_sched_barrier(0)
#define PIN(x) asm volatile("" : "+v"(x))
#define WAIT_V(n) asm volatile("s_waitcnt vmcnt(" #n ")" ::: "memory")
#define WAIT_L(n) asm volatile("s_waitcnt lgkmcnt(" #n ")" ::: "memory")
#define BAR() __builtin_amdgcn_s_barrier()

DI unsigned cvtpk(float lo, float hi) { f32x2 v = {lo, hi}; bf16x2_t b = __builtin_convertvector(v, bf16x2_t); return __builtin_bit_cast(unsigned, b); }
DI float bflo(unsigned w) { return __uint_as_float(w << 16); }
DI float bfhi(unsigned w) { return __uint_as_float(w & 0xffff0000u); }
DI u32x4 pack8(const f32x16& a, int s, float sc) {
    u32x4 w;
    w[0] = cvtpk(a[8 * s + 0] * sc, a[8 * s + 1] * sc); w[1] = cvtpk(a[8 * s + 2] * sc, a[8 * s + 3] * sc);
    w[2] = cvtpk(a[8 * s + 4] * sc, a[8 * s + 5] * sc); w[3] = cvtpk(a[8 * s + 6] * sc, a[8 * s + 7] * sc);
    return w;
}
DI bf16x8 ld128(lds_cp p) { return *(const LAS bf16x8*)p; }
typedef short v4i16_t __attribute__((ext_vector_type(4)));
DI s16x4 ldtr(lds_cp p) { return __builtin_bit_cast(s16x4, __builtin_amdgcn_ds_read_tr16_b64_v4i16((LAS v4i16_t*)p)); }
DI bf16x8 ldtr2(lds_cp p, int step) { const s16x4 lo = ldtr(p), hi = ldtr(p + step); return __builtin_shufflevector(lo, hi, 0, 1, 2, 3, 4, 5, 6, 7); }
DI void glds16(const void* g, lds_p dst) { __builtin_amdgcn_global_load_lds((const unsigned*)g, (LAS unsigned*)dst, 16, 0, 0); }
DI float swap_add(float v) { auto r = __builtin_amdgcn_permlane32_swap(__float_as_uint(v), __float_as_uint(v), false, false); return __uint_as_float(r[0]) + __uint_as_float(r[1]); }
DI float swap_max(float v) { auto r = __builtin_amdgcn_permlane32_swap(__float_as_uint(v), __float_as_uint(v), false, false); return __builtin_fmaxf(__uint_as_float(r[0]), __uint_as_float(r[1])); }
DI float gelu_tanh(float v) {
    const float t = v * (-2.302208198f) * __builtin_fmaf(v * v, 0.044715f, 1.0f);
    return v * __builtin_amdgcn_rcpf(1.0f + __builtin_amdgcn_exp2f(t));
}

__global__ __launch_bounds__(768) void k0_z(const bf16_t* __restrict__ x, const bf16_t* __restrict__ w_in, unsigned char* __restrict__ ws) {
    __shared__ __attribute__((aligned(16))) unsigned char smem[163840];
    const lds_p lds = (lds_p)smem;
    const int tid = threadIdx.x, w = __builtin_amdgcn_readfirstlane(tid >> 6), lane = tid & 63;
    const int bid = blockIdx.x, x8 = bid & 7, kk = bid >> 3, cgrp = kk & 7, half = cgrp >> 2, cg = cgrp & 3, rgrp = x8 * 4 + (kk >> 3), rot = ((kk >> 3) & 1) * 4;
    if (bid == 0 && tid == 0) *(unsigned*)(ws + WS_CNT) = 0u;
    const int sub = w >> 2, rp = (2 * w) & 7;
    int gcol = half * FH + cg * 192 + sub * 64 + (((lane & 7) ^ (((lane >> 4) & 1) << 2)) << 3);
    gcol = gcol > FF - 8 ? FF - 8 : gcol;
    const unsigned bvoff = (unsigned)(((8 * rp + (lane >> 3)) * FF + gcol) * 2);
    const lds_p bdst = lds + 65536 + sub * 8192 + rp * 1024;
#define K0_STAGE(j) do { const char* g_ = (const char*)w_in + (size_t)((((j) + rot) & 7) * 64) * (FF * 2) + bvoff; \
        glds16(g_, bdst + ((j) & 3) * 24576); glds16(g_ + 8 * FF * 2, bdst + ((j) & 3) * 24576 + 1024); } while (0)
    K0_STAGE(0); K0_STAGE(1); K0_STAGE(2); K0_STAGE(3);
    if (w < 8) {
        const int row = tid >> 3, seg = tid & 7;
        const u32x4* src = (const u32x4*)(x + (size_t)(rgrp * 64 + row) * DM) + seg;
        u32x4 v[8];
#pragma unroll
        for (int j = 0; j < 8; ++j) v[j] = src[j * 8];
        float s = 0.f;
#pragma unroll
        for (int j = 0; j < 8; ++j)
#pragma unroll
            for (int e = 0; e < 4; ++e) s += bflo(v[j][e]) + bfhi(v[j][e]);
        s += __shfl_xor(s, 1); s += __shfl_xor(s, 2); s += __shfl_xor(s, 4);
        const float mean = s * (1.0f / DM);
        float q = 0.f;
#pragma unroll
        for (int j = 0; j < 8; ++j)
#pragma unroll
            for (int e = 0; e < 4; ++e) { const float a = bflo(v[j][e]) - mean, b = bfhi(v[j][e]) - mean; q += a * a + b * b; }
        q += __shfl_xor(q, 1); q += __shfl_xor(q, 2); q += __shfl_xor(q, 4);
        const float rstd = 1.0f / sqrtf(q * (1.0f / DM) + 1e-5f);
        u32x4* xo = (u32x4*)(ws + WS_XN + (size_t)(rgrp * 64 + row) * DM * 2) + seg;
#pragma unroll
        for (int j = 0; j < 8; ++j) {
            u32x4 o;
#pragma unroll
            for (int e = 0; e < 4; ++e) o[e] = cvtpk((bflo(v[j][e]) - mean) * rstd, (bfhi(v[j][e]) - mean) * rstd);
            *(LAS u32x4*)(lds + j * 8192 + row * 128 + ((seg ^ ((row >> 1) & 7)) << 4)) = o;
            if (cgrp == 0) xo[j * 8] = o;
        }
    }
    const int rb = w & 1, cb = w >> 1, r = lane & 31, hh = lane >> 5, blk = (lane >> 4) & 1, qq = (lane & 15) >> 2, p = lane & 3;
    const lds_cp abase = lds + (rb * 32 + r) * 128;
    const int asw = (r >> 1) & 7;
    const lds_cp bbase = lds + 65536 + (cb >> 1) * 8192 + (8 * hh + qq) * 128 + (((cb & 1) ^ (qq >> 1)) << 6) + blk * 32 + p * 8;
    f32x16 acc = {};
    asm volatile("s_waitcnt vmcnt(0) lgkmcnt(0)\n\ts_barrier" ::: "memory");
#pragma unroll
    for (int j = 0; j < 8; ++j) {
        const int kt = (j + rot) & 7;
        bf16x8 af[4], bf[4];
#pragma unroll
        for (int ks = 0; ks < 4; ++ks) {
            af[ks] = ld128(abase + kt * 8192 + (((2 * ks + hh) ^ asw) << 4));
            bf[ks] = ldtr2(bbase + (j & 3) * 24576 + ks * 2048, 512);
        }
#pragma unroll
        for (int ks = 0; ks < 4; ++ks) acc = MFMA32(bf[ks], af[ks], acc);
        if (j < 3) asm volatile("s_waitcnt lgkmcnt(0)\n\ts_barrier" ::: "memory");
        else if (j == 3 || j == 4) asm volatile("s_waitcnt vmcnt(4) lgkmcnt(0)\n\ts_barrier" ::: "memory");
        else if (j == 5) asm volatile("s_waitcnt vmcnt(2) lgkmcnt(0)\n\ts_barrier" ::: "memory");
        else if (j == 6) asm volatile("s_waitcnt vmcnt(0) lgkmcnt(0)\n\ts_barrier" ::: "memory");
        if (j < 4) K0_STAGE(j + 4);
    }
#undef K0_STAGE
#pragma unroll
    for (int e = 0; e < 16; ++e) acc[e] = gelu_tanh(acc[e]);
    const int fb = cg * 6 + cb, tb = rgrp * 2 + rb;
    if (half == 0) {
#pragma unroll
        for (int s = 0; s < 2; ++s)
            if (2 * fb + s < 43) *(u32x4*)(ws + WS_Z1F + (size_t)(tb * 44 + 2 * fb + s) * 1024 + lane * 16) = pack8(acc, s, 1.0f);
    } else {
        bf16_t* zr = (bf16_t*)(ws + WS_Z2R) + (size_t)(tb * 32 + r) * 704 + 32 * fb + 4 * hh;
#pragma unroll
        for (int g = 0; g < 4; ++g)
            if (32 * fb + 8 * g < FH) { u32x2 o = {cvtpk(acc[4 * g], acc[4 * g + 1]), cvtpk(acc[4 * g + 2], acc[4 * g + 3])}; *(u32x2*)(zr + 8 * g) = o; }
    }
}

struct Unit { const char* A; const char* B; unsigned lda2, ldb2; int type, pm, pn, aux; };
DI Unit k1_unit(int round, int bid, const bf16_t* ckv, const bf16_t* wq, const bf16_t* wk, const bf16_t* wv, const bf16_t* wsp, const unsigned char* ws) {
    const int x8 = bid & 7, k = bid >> 3; Unit u;
    if (round == 0) {
        u.type = k >> 4; u.pn = 2 * x8 + ((k >> 3) & 1); u.pm = k & 7; u.aux = 0; u.lda2 = DM * 2; u.ldb2 = 4096 * 2;
        u.A = (const char*)ckv + (size_t)u.pm * 256 * DM * 2; u.B = (const char*)(u.type ? wv : wk) + u.pn * 512;
    } else if (k < 16) {
        u.type = 2; u.pn = 2 * x8 + (k >> 3); u.pm = k & 7; u.aux = 0; u.lda2 = DM * 2; u.ldb2 = 4096 * 2;
        u.A = (const char*)ws + WS_XN + (size_t)u.pm * 256 * DM * 2; u.B = (const char*)wq + u.pn * 512;
    } else if (k < 28) {
        const int j = k - 16; u.type = 3; u.aux = x8 & 3; u.pm = (x8 >> 2) * 4 + (j & 3); u.pn = j >> 2; u.lda2 = SEQ * 2; u.ldb2 = 768 * 2;
        u.A = (const char*)wsp + (size_t)u.pm * 256 * SEQ * 2 + u.aux * 1024; u.B = (const char*)ws + WS_Z2N + (size_t)u.aux * 512 * 768 * 2 + u.pn * 512;
    } else { u.type = -1; u.pm = u.pn = u.aux = 0; u.lda2 = DM * 2; u.ldb2 = 4096 * 2; u.A = u.B = nullptr; }
    return u;
}
constexpr int HTB = 16384;
#define G_SA(b, h) (((b) * 2 + (h)) * HTB)
#define G_SB(b, h) ((4 + (b) * 2 + (h)) * HTB)
#define G_STAGE(bufoff, gb, voff, step2) do { glds16((gb) + (voff), lds + (bufoff) + ldsw); glds16((gb) + (step2) + (voff), lds + (bufoff) + ldsw + 8192); } while (0)
#define G_LDA(dst, b, h) do { _Pragma("unroll") for (int m_ = 0; m_ < 2; ++m_) _Pragma("unroll") for (int k_ = 0; k_ < 4; ++k_) dst[m_][k_] = ld128(lds + G_SA(b, h) + aoff[k_] + m_ * 4096); } while (0)
#define G_LDB(dst, b, h) do { _Pragma("unroll") for (int k_ = 0; k_ < 4; ++k_) dst[k_] = ldtr2(lds + G_SB(b, h) + boff + k_ * 4096, 1024); } while (0)
#define G_MMA(ai, bj, At, Bt) do { __builtin_amdgcn_s_setprio(1); _Pragma("unroll") for (int k_ = 0; k_ < 4; ++k_) _Pragma("unroll") for (int m_ = 0; m_ < 2; ++m_) \
        acc[ai][bj][m_] = SWAP ? MFMA32(At[m_][k_], Bt[k_], acc[ai][bj][m_]) : MFMA32(Bt[k_], At[m_][k_], acc[ai][bj][m_]); __builtin_amdgcn_s_setprio(0); } while (0)

template <bool SWAP>
DI void k1_kloop(const lds_p lds, f32x16 (&acc)[2][2][2], const Unit& cur, const Unit& nxt, const unsigned vAc, const unsigned vBc, const unsigned vAn, const unsigned vBn,
                 const int (&aoff)[4], const int boff, const unsigned ldsw, const bool first, const bool poll, unsigned* cnt, const int lane, const int wid) {
    bf16x8 At[2][4], B0[4], B1[4];
    for (int t = 0; t < 8; t += 2) {
        const bool last = (t == 6);
        const char* a1 = cur.A + (t + 1) * 128;
        const char* a2 = last ? nxt.A : cur.A + (t + 2) * 128;
        const char* b2 = last ? nxt.B : cur.B + (size_t)(t + 2) * 64 * cur.ldb2;
        const unsigned la = last ? nxt.lda2 : cur.lda2, lb = last ? nxt.ldb2 : cur.ldb2;
        const unsigned vA = last ? vAn : vAc, vB = last ? vBn : vBc;
        const char* a3 = a2 + 128; const char* b3 = b2 + (size_t)64 * lb;
        if (first && t == 2) {
            if (lane == 0) __hip_atomic_fetch_add(cnt, 1u, __ATOMIC_RELAXED, __HIP_MEMORY_SCOPE_AGENT);
        }
        if (last && poll) {
            if (wid == 0) {
                for (unsigned spins = 0; spins < (1u << 22); ++spins) {
                    if ((unsigned)__builtin_amdgcn_readfirstlane(__hip_atomic_load(cnt, __ATOMIC_RELAXED, __HIP_MEMORY_SCOPE_AGENT)) >= 2048u) break;
                    __builtin_amdgcn_s_sleep(2);
                }
                __builtin_amdgcn_fence(__ATOMIC_ACQUIRE, "agent");
                WAIT_V(0);
            }
            asm volatile("" ::: "memory"); BAR(); asm volatile("" ::: "memory");
        }
        G_LDB(B0, 0, 0); SBAR(); G_LDA(At, 0, 0); G_STAGE(G_SA(1, 1), a1 + (size_t)128 * cur.lda2, vAc, (size_t)64 * cur.lda2);
        WAIT_L(8); BAR(); WAIT_L(0); G_MMA(0, 0, At, B0); BAR(); SBAR();
        G_LDB(B1, 0, 1); G_STAGE(G_SB(0, 0), b2, vB, (size_t)32 * lb);
        BAR(); WAIT_L(0); G_MMA(0, 1, At, B1); BAR();
        G_LDA(At, 0, 1); G_STAGE(G_SA(0, 0), a2, vA, (size_t)64 * la);
        BAR(); WAIT_L(0); G_MMA(1, 0, At, B0); BAR(); SBAR();
        G_STAGE(G_SB(0, 1), b2 + 256, vB, (size_t)32 * lb);
        WAIT_V(6); BAR(); G_MMA(1, 1, At, B1); BAR();
        G_LDB(B0, 1, 0); SBAR(); G_LDA(At, 1, 0); G_STAGE(G_SA(0, 1), a2 + (size_t)128 * la, vA, (size_t)64 * la);
        WAIT_L(8); BAR(); WAIT_L(0); G_MMA(0, 0, At, B0); BAR(); SBAR();
        G_LDB(B1, 1, 1); G_STAGE(G_SB(1, 0), b3, vB, (size_t)32 * lb);
        BAR(); WAIT_L(0); G_MMA(0, 1, At, B1); BAR();
        G_LDA(At, 1, 1); G_STAGE(G_SA(1, 0), a3, vA, (size_t)64 * la);
        BAR(); WAIT_L(0); G_MMA(1, 0, At, B0); BAR(); SBAR();
        G_STAGE(G_SB(1, 1), b3 + 256, vB, (size_t)32 * lb);
        WAIT_V(6); BAR(); G_MMA(1, 1, At, B1); BAR();
    }
}

DI void k1_epilogue(const f32x16 (&acc)[2][2][2], const Unit& u, unsigned char* ws, int wr, int wc, int lane) {
    constexpr float QS = 0.08838834764831845f * 1.4426950408889634f;
    if (u.type == 3) {
#pragma unroll
        for (int ai = 0; ai < 2; ++ai)
#pragma unroll
            for (int bj = 0; bj < 2; ++bj)
#pragma unroll
                for (int m = 0; m < 2; ++m) {
                    const int tb = u.pm * 8 + ai * 4 + wr * 2 + m, fb = u.pn * 8 + bj * 4 + wc;
                    if (fb < 22) {
                        unsigned char* dst = ws + WS_GP + (size_t)((u.aux * 64 + tb) * 22 + fb) * 4096 + lane * 16;
#pragma unroll
                        for (int g = 0; g < 4; ++g) *(f32x4*)(dst + g * 1024) = (f32x4){acc[ai][bj][m][4 * g], acc[ai][bj][m][4 * g + 1], acc[ai][bj][m][4 * g + 2], acc[ai][bj][m][4 * g + 3]};
                    }
                }
    } else {
        const float sc = u.type == 2 ? QS : 1.0f;
        unsigned char* base = u.type == 2 ? ws + WS_QF : ws + WS_KVF + (u.type == 1 ? 8192 : 0);
        const int hstride = u.type == 2 ? 8192 : 16384;
#pragma unroll
        for (int ai = 0; ai < 2; ++ai)
#pragma unroll
            for (int bj = 0; bj < 2; ++bj)
#pragma unroll
                for (int m = 0; m < 2; ++m) {
                    const int tb = u.pm * 8 + ai * 4 + wr * 2 + m, head = 2 * u.pn + bj;
                    unsigned char* dst = base + (size_t)(head * 64 + tb) * hstride + wc * 2048 + lane * 16;
                    *(u32x4*)dst = pack8(acc[ai][bj][m], 0, sc);
                    *(u32x4*)(dst + 1024) = pack8(acc[ai][bj][m], 1, sc);
                }
    }
}

__global__ __launch_bounds__(512, 2) void k1_gemm(const bf16_t* __restrict__ ckv, const bf16_t* __restrict__ wq, const bf16_t* __restrict__ wk, const bf16_t* __restrict__ wv,
                                                  const bf16_t* __restrict__ wsp, const bf16_t* __restrict__ wo, const bf16_t* __restrict__ wout, unsigned char* ws) {
    __shared__ __attribute__((aligned(16))) unsigned char smem[131072];
    const lds_p lds = (lds_p)smem;
    const int tid = threadIdx.x, wid = __builtin_amdgcn_readfirstlane(tid >> 6), lane = tid & 63, wr = wid >> 2, wc = wid & 3;
    const int bid = blockIdx.x;
    const Unit u0 = k1_unit(0, bid, ckv, wq, wk, wv, wsp, ws);
    Unit u1 = k1_unit(1, bid, ckv, wq, wk, wv, wsp, ws);
    const bool has2 = u1.type >= 0;
    if (!has2) { u1.A = u0.A; u1.B = u0.B; }
    const unsigned arow = 8 * wid + (lane >> 3), achk = ((lane & 7) ^ (((wid & 1) << 2) + (lane >> 4))) << 4;
    const unsigned brow = 4 * wid + (lane >> 4), bchk = ((lane & 15) ^ (((lane >> 4) & 3) << 2)) << 4;
    const unsigned vA0 = arow * u0.lda2 + achk, vB0 = brow * u0.ldb2 + bchk, vA1 = arow * u1.lda2 + achk, vB1 = brow * u1.ldb2 + bchk;
    const unsigned ldsw = (unsigned)wid * 1024u;
    const int r = lane & 31, hh = lane >> 5, blk = (lane >> 4) & 1, qq = (lane & 15) >> 2, p = lane & 3;
    int aoff[4];
#pragma unroll
    for (int k = 0; k < 4; ++k) aoff[k] = (wr * 64 + r) * 128 + (((2 * k + hh) ^ ((r >> 1) & 7)) << 4);
    const int boff = (8 * hh + qq) * 256 + ((wc ^ qq) << 6) + blk * 32 + p * 8;
    unsigned* cnt = (unsigned*)(ws + WS_CNT);
    G_STAGE(G_SB(0, 0), u0.B, vB0, (size_t)32 * u0.ldb2); G_STAGE(G_SA(0, 0), u0.A, vA0, (size_t)64 * u0.lda2);
    G_STAGE(G_SB(0, 1), u0.B + 256, vB0, (size_t)32 * u0.ldb2); G_STAGE(G_SA(0, 1), u0.A + (size_t)128 * u0.lda2, vA0, (size_t)64 * u0.lda2);
    G_STAGE(G_SB(1, 0), u0.B + (size_t)64 * u0.ldb2, vB0, (size_t)32 * u0.ldb2); G_STAGE(G_SA(1, 0), u0.A + 128, vA0, (size_t)64 * u0.lda2);
    G_STAGE(G_SB(1, 1), u0.B + (size_t)64 * u0.ldb2 + 256, vB0, (size_t)32 * u0.ldb2);
    {
        const int tok = bid * 8 + wid;
        const u32x4* src = (const u32x4*)(ws + WS_Z2R + (size_t)tok * 704 * 2);
        const u32x4 v0 = src[lane];
        u32x4 v1 = {0u, 0u, 0u, 0u};
        const bool has1 = lane < 22;
        if (has1) v1 = src[64 + lane];
        float s = 0.f;
#pragma unroll
        for (int e = 0; e < 4; ++e) s += (bflo(v0[e]) + bfhi(v0[e])) + (bflo(v1[e]) + bfhi(v1[e]));
#pragma unroll
        for (int o = 32; o >= 1; o >>= 1) s += __shfl_xor(s, o);
        const float mean = s * (1.0f / FH);
        float q = 0.f;
#pragma unroll
        for (int e = 0; e < 4; ++e) { const float a = bflo(v0[e]) - mean, b = bfhi(v0[e]) - mean, c = bflo(v1[e]) - mean, d = bfhi(v1[e]) - mean; q += a * a + b * b; if (has1) q += c * c + d * d; }
#pragma unroll
        for (int o = 32; o >= 1; o >>= 1) q += __shfl_xor(q, o);
        const float rstd = 1.0f / sqrtf(q * (1.0f / FH) + 1e-5f);
        u32x4 o0, o1;
#pragma unroll
        for (int e = 0; e < 4; ++e) { o0[e] = cvtpk((bflo(v0[e]) - mean) * rstd, (bfhi(v0[e]) - mean) * rstd); o1[e] = has1 ? cvtpk((bflo(v1[e]) - mean) * rstd, (bfhi(v1[e]) - mean) * rstd) : 0u; }
        WAIT_V(0);
        const __amdgpu_buffer_rsrc_t rs = __builtin_amdgcn_make_buffer_rsrc((void*)(ws + WS_Z2N), (short)0, SEQ * 768 * 2, 0x00020000);
        __builtin_amdgcn_raw_buffer_store_b128(o0, rs, tok * 1536 + lane * 16, 0, 16);
        if (lane < 32) __builtin_amdgcn_raw_buffer_store_b128(o1, rs, tok * 1536 + 1024 + lane * 16, 0, 16);
    }
    if (wr == 1) BAR();
    BAR(); BAR();
    f32x16 acc[2][2][2];
#pragma unroll
    for (int i = 0; i < 8; ++i) acc[i >> 2][(i >> 1) & 1][i & 1] = f32x16{};
    if (u0.type == 1) k1_kloop<true>(lds, acc, u0, u1, vA0, vB0, vA1, vB1, aoff, boff, ldsw, true, u1.type == 3, cnt, lane, wid);
    else              k1_kloop<false>(lds, acc, u0, u1, vA0, vB0, vA1, vB1, aoff, boff, ldsw, true, u1.type == 3, cnt, lane, wid);
    k1_epilogue(acc, u0, ws, wr, wc, lane);
    if (has2) {
#pragma unroll
        for (int i = 0; i < 8; ++i) acc[i >> 2][(i >> 1) & 1][i & 1] = f32x16{};
        k1_kloop<false>(lds, acc, u1, u1, vA1, vB1, vA1, vB1, aoff, boff, ldsw, false, false, cnt, lane, wid);
        k1_epilogue(acc, u1, ws, wr, wc, lane);
        WAIT_V(0);
        if (wr == 0) BAR();
    } else {
        WAIT_V(0);
        if (wr == 0) BAR();
        BAR();
        const int gw = ((bid & 7) * 4 + ((bid >> 3) - 28)) * 8 + wid;
        const lds_p scr = lds + wid * 16384;
        for (int ks = gw; ks < NKS; ks += 256) {
            const char* src = (ks < 256 ? (const char*)wo + (size_t)ks * 16 * 1024 : (const char*)wout + (size_t)(ks - 256) * 16 * 1024) + lane * 16;
#pragma unroll
            for (int i = 0; i < 16; ++i) glds16(src + i * 1024, scr + i * 1024);
            WAIT_V(0);
            unsigned char* dst = ws + WS_WOF + (size_t)ks * 16384 + lane * 16;
            const lds_cp rd = scr + (4 * hh + qq) * 1024 + blk * 32 + p * 8;
#pragma unroll
            for (int nb = 0; nb < 16; ++nb) *(bf16x8*)(dst + nb * 1024) = ldtr2(rd + nb * 64, 8192);
            asm volatile("s_waitcnt lgkmcnt(0)" ::: "memory");
        }
    }
}

constexpr float ATT_THR = 32.0f;
DI void dma2(unsigned voff, const unsigned char* sbase, unsigned ldsdst) {
    asm volatile("s_mov_b32 m0, %2\n\ts_nop 0\n\tglobal_load_lds_dwordx4 %0, %1\n\tglobal_load_lds_dwordx4 %0, %1 offset:1024" :: "v"(voff), "s"(sbase), "s"(ldsdst) : "memory");
}
#define EX2(v) __builtin_amdgcn_exp2f(v)
#define MX3(a, b, c) __builtin_fmaxf(__builtin_fmaxf((a), (b)), (c))
DI void att_mask(f32x16& s, int dq) {
#pragma unroll
    for (int e = 0; e < 16; ++e) if ((e & 3) + 8 * (e >> 2) > dq) s[e] = -__builtin_inff();
}

__global__ __launch_bounds__(512, 2) void k2_attn(const bf16_t* __restrict__ bsp, unsigned char* __restrict__ ws) {
    __shared__ __attribute__((aligned(16))) unsigned char smem[163840];
    const lds_p lds = (lds_p)smem;
    const int tid = threadIdx.x, wid = __builtin_amdgcn_readfirstlane(tid >> 6), lane = tid & 63, kh = wid >> 2, rg = wid & 3, r = lane & 31, hh = lane >> 5;
    const int bid = blockIdx.x, head = 4 * (bid & 7) + (bid >> 6), pr = (bid >> 3) & 7;
    const int i1 = 15 - pr, i2 = pr, G1 = 2 * (i1 + 1);
    const unsigned lds0 = (unsigned)(uintptr_t)smem;
    const unsigned char* kvh = ws + WS_KVF + (size_t)head * 64 * 16384;
    const unsigned dvoff = (unsigned)(rg * 2048 + lane * 16);
    const unsigned ddst = lds0 + kh * 65536 + rg * 2048;
#define A_TB(g) (4 * (((g) >= G1 ? (g) - G1 : (g)) >> 1) + 2 * kh + ((g) & 1))
#define A_DMAK(g) do { if ((g) < 34) dma2(dvoff, kvh + (size_t)A_TB(g) * 16384, ddst + ((g) & 3) * 8192); } while (0)
#define A_DMAV(g) do { if ((g) < 34) dma2(dvoff, kvh + (size_t)A_TB(g) * 16384 + 8192, ddst + 32768 + ((g) & 3) * 8192); } while (0)
    A_DMAK(0); A_DMAK(1); A_DMAV(0); A_DMAK(2); A_DMAV(1);
    bf16x8 q[8];
    {
        const bf16x8* qs = (const bf16x8*)(ws + WS_QF + (size_t)(head * 64 + 4 * i1 + rg) * 8192) + lane;
#pragma unroll
        for (int d = 0; d < 8; ++d) q[d] = qs[d * 64];
    }
    for (int j = wid; j < 11; j += 8) {
        const int f = bid * 11 + j, tb = f / 44, fs = f - tb * 44;
        if (fs < 43) {
            const unsigned char* gp = ws + WS_GP + (size_t)(tb * 22 + (fs >> 1)) * 4096 + (fs & 1) * 2048 + lane * 16;
            f32x4 a = {0.f, 0.f, 0.f, 0.f}, b = {0.f, 0.f, 0.f, 0.f};
#pragma unroll
            for (int sp = 0; sp < 4; ++sp) { a += *(const f32x4*)(gp + (size_t)sp * 64 * 22 * 4096); b += *(const f32x4*)(gp + (size_t)sp * 64 * 22 * 4096 + 1024); }
            const u32x4 z = *(const u32x4*)(ws + WS_Z1F + (size_t)f * 1024 + lane * 16);
            const float bias = __uint_as_float((unsigned)bsp[tb * 32 + r] << 16);
            u32x4 o;
            o[0] = cvtpk(bflo(z[0]) * (a[0] + bias), bfhi(z[0]) * (a[1] + bias)); o[1] = cvtpk(bflo(z[1]) * (a[2] + bias), bfhi(z[1]) * (a[3] + bias));
            o[2] = cvtpk(bflo(z[2]) * (b[0] + bias), bfhi(z[2]) * (b[1] + bias)); o[3] = cvtpk(bflo(z[3]) * (b[2] + bias), bfhi(z[3]) * (b[3] + bias));
            *(u32x4*)(ws + WS_AF + ((size_t)(256 + fs) * 64 + tb) * 1024 + lane * 16) = o;
        }
    }
    asm volatile("s_waitcnt vmcnt(0)\n\ts_barrier" ::: "memory");
    const lds_cp rbase = lds + kh * 65536 + lane * 16;
    int so = 0;
    int g0 = 0;
#pragma unroll 1
    for (int blkno = 0; blkno < 2; ++blkno) {
        const int ib = blkno ? i2 : i1;
        const int qrow = 128 * ib + 32 * rg + r - 4 * hh - 64 * kh;
        f32x16 o[4], sA, sB, negm;
        bf16x8 fr[8];
        u32x4 pw0, pw1;
        float mref, l0 = 0.f, l1 = 0.f, resc = 1.0f;
        bool dead = false, pend = false;
        lds_cp pc = rbase + so, pn = rbase + (so ^ 16384);
#pragma unroll
        for (int d = 0; d < 8; ++d) fr[d] = ld128(pc + d * 1024);
        sA = f32x16{};
#pragma unroll
        for (int d = 0; d < 8; ++d) sA = MFMA32(fr[d], q[d], sA);
        asm volatile("s_waitcnt lgkmcnt(0)\n\ts_barrier" ::: "memory");
        A_DMAK(g0 + 3); A_DMAV(g0 + 2); A_DMAK(g0 + 4); A_DMAV(g0 + 3);
#pragma unroll
        for (int d = 0; d < 4; ++d) fr[d] = ld128(pc + 8192 + d * 1024);
#pragma unroll
        for (int d = 0; d < 4; ++d) o[d] = f32x16{};
        if (ib == 0) att_mask(sA, qrow);
        {
            float m = MX3(sA[0], sA[1], sA[2]);
#pragma unroll
            for (int e = 3; e < 15; e += 2) m = MX3(m, sA[e], sA[e + 1]);
            m = swap_max(__builtin_fmaxf(m, sA[15]));
            dead = m == -__builtin_inff();
            mref = dead ? 0.f : m;
#pragma unroll
            for (int e = 0; e < 16; ++e) { sA[e] -= mref; negm[e] = -mref; }
            PIN(negm);
#pragma unroll
            for (int e = 0; e < 8; ++e) sA[e] = EX2(sA[e]);
            l0 = sA[0] + sA[2]; l1 = sA[1] + sA[3];
        }
#define A_RD(j, ptr) do { fr[j] = ld128(ptr); } while (0)
#define A_P1(SC, SN, KP, VP, Q) do { \
        A_RD(4, (KP) + 4096); SBAR(); if (Q) SN = MFMA32(fr[0], q[0], negm); SC[8] = EX2(SC[8]); SC[9] = EX2(SC[9]); pw0[0] = cvtpk(SC[0], SC[1]); PIN(SC); PIN(pw0); SBAR(); \
        A_RD(5, (KP) + 5120); SBAR(); if (Q) SN = MFMA32(fr[1], q[1], SN); SC[10] = EX2(SC[10]); SC[11] = EX2(SC[11]); pw0[1] = cvtpk(SC[2], SC[3]); PIN(SC); PIN(pw0); SBAR(); \
        A_RD(6, (KP) + 6144); SBAR(); if (Q) SN = MFMA32(fr[2], q[2], SN); SC[12] = EX2(SC[12]); SC[13] = EX2(SC[13]); pw0[2] = cvtpk(SC[4], SC[5]); PIN(SC); PIN(pw0); SBAR(); \
        A_RD(7, (KP) + 7168); SBAR(); if (Q) SN = MFMA32(fr[3], q[3], SN); SC[14] = EX2(SC[14]); SC[15] = EX2(SC[15]); pw0[3] = cvtpk(SC[6], SC[7]); PIN(SC); PIN(pw0); SBAR(); \
        A_RD(0, (VP));        SBAR(); if (Q) SN = MFMA32(fr[4], q[4], SN); l0 += SC[4]; l1 += SC[5]; l0 += SC[6]; l1 += SC[7]; PIN(l0); PIN(l1); SBAR(); \
        A_RD(1, (VP) + 2048); SBAR(); if (Q) SN = MFMA32(fr[5], q[5], SN); l0 += SC[8]; l1 += SC[9]; pw1[0] = cvtpk(SC[8], SC[9]); PIN(l0); PIN(l1); PIN(pw1); SBAR(); \
        A_RD(2, (VP) + 4096); SBAR(); if (Q) SN = MFMA32(fr[6], q[6], SN); l0 += SC[10]; l1 += SC[11]; pw1[1] = cvtpk(SC[10], SC[11]); PIN(l0); PIN(l1); PIN(pw1); SBAR(); \
        A_RD(3, (VP) + 6144); SBAR(); if (Q) SN = MFMA32(fr[7], q[7], SN); SBAR(); } while (0)
#define PWF(x) __builtin_bit_cast(bf16x8, x)
#define A_P2A(SC, SN, VP, Q, MASK, DQ) do { \
        A_RD(4, (VP) + 1024); A_RD(5, (VP) + 3072); SBAR(); o[0] = MFMA32(fr[0], PWF(pw0), o[0]); l0 += SC[12]; l1 += SC[13]; pw1[2] = cvtpk(SC[12], SC[13]); PIN(l0); PIN(l1); PIN(pw1); SBAR(); \
        A_RD(6, (VP) + 5120); A_RD(7, (VP) + 7168); SBAR(); o[1] = MFMA32(fr[1], PWF(pw0), o[1]); l0 += SC[14]; l1 += SC[15]; pw1[3] = cvtpk(SC[14], SC[15]); PIN(l0); PIN(l1); PIN(pw1); SBAR(); \
        float ma_ = 0.f, mb_ = 0.f; \
        o[2] = MFMA32(fr[2], PWF(pw0), o[2]); \
        if (Q) { if (MASK) att_mask(SN, DQ); ma_ = MX3(SN[0], SN[1], SN[2]); mb_ = MX3(SN[3], SN[4], SN[5]); ma_ = MX3(ma_, SN[6], SN[7]); mb_ = MX3(mb_, SN[8], SN[9]); PIN(ma_); PIN(mb_); } SBAR(); \
        o[3] = MFMA32(fr[3], PWF(pw0), o[3]); \
        if (Q) { ma_ = MX3(ma_, SN[10], SN[11]); mb_ = MX3(mb_, SN[12], SN[13]); ma_ = MX3(ma_, SN[14], SN[15]); ma_ = __builtin_fmaxf(ma_, mb_); \
            if (__builtin_expect(__any(ma_ > ATT_THR), 0)) {            \
                const float dl_ = __builtin_fmaxf(swap_max(ma_), 0.f); mref += dl_; resc = EX2(-dl_); l0 *= resc; l1 *= resc; pend = true; \
                _Pragma("unroll") for (int e = 0; e < 16; ++e) { SN[e] -= dl_; negm[e] = -mref; } PIN(negm); } } SBAR(); } while (0)
#define A_P2B(SN, KN, Q, D0, D1, D2, D3) do { \
        if (Q) A_RD(0, (KN));        SBAR(); o[0] = MFMA32(fr[4], PWF(pw1), o[0]); if (Q) { SN[0] = EX2(SN[0]); SN[1] = EX2(SN[1]); PIN(SN); } D0; SBAR(); \
        if (Q) A_RD(1, (KN) + 1024); SBAR(); o[1] = MFMA32(fr[5], PWF(pw1), o[1]); if (Q) { SN[2] = EX2(SN[2]); SN[3] = EX2(SN[3]); PIN(SN); } D1; SBAR(); \
        if (Q) A_RD(2, (KN) + 2048); SBAR(); o[2] = MFMA32(fr[6], PWF(pw1), o[2]); if (Q) { SN[4] = EX2(SN[4]); SN[5] = EX2(SN[5]); l0 += SN[0]; l1 += SN[1]; PIN(SN); PIN(l0); PIN(l1); } D2; SBAR(); \
        if (Q) A_RD(3, (KN) + 3072); SBAR(); o[3] = MFMA32(fr[7], PWF(pw1), o[3]); if (Q) { SN[6] = EX2(SN[6]); SN[7] = EX2(SN[7]); l0 += SN[2]; l1 += SN[3]; PIN(SN); PIN(l0); PIN(l1); } D3; SBAR(); \
        if (Q) { if (__builtin_expect(pend, 0)) { pend = false;         \
            _Pragma("unroll") for (int d = 0; d < 4; ++d) _Pragma("unroll") for (int e = 0; e < 16; ++e) o[d][e] *= resc; } } } while (0)
#define A_BARRIER() asm volatile("s_waitcnt vmcnt(0) lgkmcnt(0)\n\ts_barrier" ::: "memory")
        int g = g0;
#pragma unroll 1
        for (int n = 0; n < ib; ++n) {
            A_P1(sA, sB, pc + 8192, pc + 32768, true);
            A_P2A(sA, sB, pc + 32768, true, false, 0);
            A_P2B(sB, pn, true, (void)0, (void)0, (void)0, (void)0);
            A_P1(sB, sA, pn, pc + 32768 + 8192, true);
            A_P2A(sB, sA, pc + 32768 + 8192, true, (n == ib - 1), qrow - 128 * ib);
            A_BARRIER();
            A_P2B(sA, pn + 8192, true, A_DMAK(g + 5), A_DMAV(g + 4), A_DMAK(g + 6), A_DMAV(g + 5));
            g += 2; so ^= 16384;
            const lds_cp t_ = pc; pc = pn; pn = t_;
        }
        A_P1(sA, sB, pc + 8192, pc + 32768, true);
        if (blkno == 0) {
            const bf16x8* qs = (const bf16x8*)(ws + WS_QF + (size_t)(head * 64 + 4 * i2 + rg) * 8192) + lane;
#pragma unroll
            for (int d = 0; d < 8; ++d) q[d] = qs[d * 64];
        }
        A_P2A(sA, sB, pc + 32768, true, true, qrow - 128 * ib - 32);
        {
            o[0] = MFMA32(fr[4], PWF(pw1), o[0]); sB[0] = EX2(sB[0]); sB[1] = EX2(sB[1]); PIN(sB); SBAR();
            o[1] = MFMA32(fr[5], PWF(pw1), o[1]); sB[2] = EX2(sB[2]); sB[3] = EX2(sB[3]); PIN(sB); SBAR();
            o[2] = MFMA32(fr[6], PWF(pw1), o[2]); sB[4] = EX2(sB[4]); sB[5] = EX2(sB[5]); l0 += sB[0]; l1 += sB[1]; PIN(sB); SBAR();
            o[3] = MFMA32(fr[7], PWF(pw1), o[3]); sB[6] = EX2(sB[6]); sB[7] = EX2(sB[7]); l0 += sB[2]; l1 += sB[3]; PIN(sB); SBAR();
            if (__builtin_expect(pend, 0)) { pend = false;
#pragma unroll
                for (int d = 0; d < 4; ++d)
#pragma unroll
                    for (int e = 0; e < 16; ++e) o[d][e] *= resc; }
        }
        {
            const lds_cp vp = pc + 32768 + 8192;
#pragma unroll
            for (int j = 0; j < 4; ++j) { fr[j] = ld128(vp + j * 2048); fr[4 + j] = ld128(vp + j * 2048 + 1024); }
#pragma unroll
            for (int e = 8; e < 16; ++e) sB[e] = EX2(sB[e]);
#pragma unroll
            for (int e = 4; e < 16; e += 2) { l0 += sB[e]; l1 += sB[e + 1]; }
#pragma unroll
            for (int e = 0; e < 4; ++e) { pw0[e] = cvtpk(sB[2 * e], sB[2 * e + 1]); pw1[e] = cvtpk(sB[8 + 2 * e], sB[9 + 2 * e]); }
            A_BARRIER();
#pragma unroll
            for (int j = 0; j < 4; ++j) o[j] = MFMA32(fr[j], PWF(pw0), o[j]);
#pragma unroll
            for (int j = 0; j < 4; ++j) o[j] = MFMA32(fr[4 + j], PWF(pw1), o[j]);
        }
        {
            const int sb_own = kh == 0 ? 131072 + rg * 8192 : (rg >> 1) * 65536 + 32768 + so + (rg & 1) * 8192;
            const int sb_par = kh == 1 ? 131072 + rg * 8192 : (rg >> 1) * 65536 + 32768 + so + (rg & 1) * 8192;
            const float mown = dead ? -__builtin_inff() : mref, lown = l0 + l1;
            *(LAS f32x2*)(lds + kh * 65536 + so + 8192 + rg * 512 + lane * 8) = (f32x2){mown, lown};
            const f32x16 s0 = kh ? o[0] : o[2], s1 = kh ? o[1] : o[3], k0 = kh ? o[2] : o[0], k1 = kh ? o[3] : o[1];
#pragma unroll
            for (int qd = 0; qd < 4; ++qd) {
                *(LAS f32x4*)(lds + sb_own + qd * 1024 + lane * 16) = (f32x4){s0[4 * qd], s0[4 * qd + 1], s0[4 * qd + 2], s0[4 * qd + 3]};
                *(LAS f32x4*)(lds + sb_own + 4096 + qd * 1024 + lane * 16) = (f32x4){s1[4 * qd], s1[4 * qd + 1], s1[4 * qd + 2], s1[4 * qd + 3]};
            }
            asm volatile("s_waitcnt lgkmcnt(0)\n\ts_barrier" ::: "memory");
            const f32x2 ml = *(const LAS f32x2*)(lds + (kh ^ 1) * 65536 + so + 8192 + rg * 512 + lane * 8);
            f32x16 p0, p1;
#pragma unroll
            for (int qd = 0; qd < 4; ++qd) {
                const f32x4 a = *(const LAS f32x4*)(lds + sb_par + qd * 1024 + lane * 16), b = *(const LAS f32x4*)(lds + sb_par + 4096 + qd * 1024 + lane * 16);
#pragma unroll
                for (int e = 0; e < 4; ++e) { p0[4 * qd + e] = a[e]; p1[4 * qd + e] = b[e]; }
            }
            const float mf = __builtin_fmaxf(mown, ml[0]);
            const float fo = EX2(mown - mf), fp = EX2(ml[0] - mf);
            const float inv = 1.0f / swap_add(fo * lown + fp * ml[1]);
            const float ca = fo * inv, cb = fp * inv;
#pragma unroll
            for (int e = 0; e < 16; ++e) { p0[e] = ca * k0[e] + cb * p0[e]; p1[e] = ca * k1[e] + cb * p1[e]; }
            unsigned char* dst = ws + WS_AF + ((size_t)(head * 8 + 4 * kh) * 64 + 4 * ib + rg) * 1024 + lane * 16;
            *(u32x4*)(dst) = pack8(p0, 0, 1.0f); *(u32x4*)(dst + 65536) = pack8(p0, 1, 1.0f);
            *(u32x4*)(dst + 2 * 65536) = pack8(p1, 0, 1.0f); *(u32x4*)(dst + 3 * 65536) = pack8(p1, 1, 1.0f);
        }
        g0 = G1; so ^= 16384;
    }
#undef A_TB
#undef A_DMAK
#undef A_DMAV
#undef A_RD
#undef A_P1
#undef A_P2A
#undef A_P2B
#undef A_BARRIER
#undef PWF
}

__global__ __launch_bounds__(512, 2) void k3_out(const bf16_t* __restrict__ x, const unsigned char* __restrict__ ws, bf16_t* __restrict__ out) {
    __shared__ __attribute__((aligned(16))) unsigned char smem[131072];
    const lds_p lds = (lds_p)smem;
    const int tid = threadIdx.x, wid = __builtin_amdgcn_readfirstlane(tid >> 6), lane = tid & 63;
    const int bid = blockIdx.x, tm = 4 * (bid & 7) + (bid >> 6), tn = (bid >> 3) & 7;
    const __amdgpu_buffer_rsrc_t ra = __builtin_amdgcn_make_buffer_rsrc((void*)(ws + WS_AF), (short)0, NKS * 65536, 0x00020000);
    const __amdgpu_buffer_rsrc_t rb = __builtin_amdgcn_make_buffer_rsrc((void*)(ws + WS_WOF), (short)0, NKS * 16384, 0x00020000);
    const int va = wid * 65536 + tm * 2048 + lane * 16, vb = wid * 16384 + tn * 2048 + lane * 16;
    constexpr int DEP = 4;
    u32x4 fa[DEP][2], fb[DEP][2];
    f32x16 acc[2][2] = {};
#define K3_LD(slot, it) do { fa[slot][0] = __builtin_amdgcn_raw_buffer_load_b128(ra, va, (it) * 524288, 0); fa[slot][1] = __builtin_amdgcn_raw_buffer_load_b128(ra, va + 1024, (it) * 524288, 0); \
        fb[slot][0] = __builtin_amdgcn_raw_buffer_load_b128(rb, vb, (it) * 131072, 0); fb[slot][1] = __builtin_amdgcn_raw_buffer_load_b128(rb, vb + 1024, (it) * 131072, 0); } while (0)
#pragma unroll
    for (int s = 0; s < DEP; ++s) K3_LD(s, s);
#pragma unroll 1
    for (int it = 0; it < 40; it += DEP) {
#pragma unroll
        for (int s = 0; s < DEP; ++s) {
            const bf16x8 a0 = __builtin_bit_cast(bf16x8, fa[s][0]), a1 = __builtin_bit_cast(bf16x8, fa[s][1]), b0 = __builtin_bit_cast(bf16x8, fb[s][0]), b1 = __builtin_bit_cast(bf16x8, fb[s][1]);
            acc[0][0] = MFMA32(b0, a0, acc[0][0]); acc[0][1] = MFMA32(b1, a0, acc[0][1]);
            acc[1][0] = MFMA32(b0, a1, acc[1][0]); acc[1][1] = MFMA32(b1, a1, acc[1][1]);
            SBAR();
            K3_LD(s, it + s + DEP);
            SBAR();
        }
    }
#undef K3_LD
#pragma unroll
    for (int b = 0; b < 4; ++b)
#pragma unroll
        for (int g = 0; g < 4; ++g) { const f32x16& a = acc[b >> 1][b & 1];
            *(LAS f32x4*)(lds + wid * 16384 + b * 4096 + g * 1024 + ((lane ^ (4 * g)) << 4)) = (f32x4){a[4 * g], a[4 * g + 1], a[4 * g + 2], a[4 * g + 3]}; }
    const int row = tid >> 3, c8 = tid & 7;
    const u32x4 xv = *(const u32x4*)(x + (size_t)(tm * 64 + row) * DM + tn * 64 + c8 * 8);
    __syncthreads();
    const lds_cp rd = lds + ((row >> 5) * 2 + (c8 >> 2)) * 4096 + (c8 & 3) * 1024 + (((row & 31) ^ (4 * (c8 & 3))) << 4);
    f32x4 lo = {0.f, 0.f, 0.f, 0.f}, hi = {0.f, 0.f, 0.f, 0.f};
#pragma unroll
    for (int w = 0; w < 8; ++w) { lo += *(const LAS f32x4*)(rd + w * 16384); hi += *(const LAS f32x4*)(rd + w * 16384 + 512); }
    u32x4 o;
    o[0] = cvtpk(bflo(xv[0]) + lo[0], bfhi(xv[0]) + lo[1]); o[1] = cvtpk(bflo(xv[1]) + lo[2], bfhi(xv[1]) + lo[3]);
    o[2] = cvtpk(bflo(xv[2]) + hi[0], bfhi(xv[2]) + hi[1]); o[3] = cvtpk(bflo(xv[3]) + hi[2], bfhi(xv[3]) + hi[3]);
    *(u32x4*)(out + (size_t)(tm * 64 + row) * DM + tn * 64 + c8 * 8) = o;
}

extern "C" void kernel_launch(void* const* d_in, const int* in_sizes, int n_in, void* d_out, int out_size, void* d_ws, size_t ws_size, hipStream_t stream) {
    (void)in_sizes; (void)n_in; (void)out_size; (void)ws_size;
    const bf16_t* x = (const bf16_t*)d_in[0];
    unsigned char* ws = (unsigned char*)d_ws;
    k0_z<<<256, 768, 0, stream>>>(x, (const bf16_t*)d_in[7], ws);
    k1_gemm<<<256, 512, 0, stream>>>((const bf16_t*)d_in[1], (const bf16_t*)d_in[2], (const bf16_t*)d_in[3], (const bf16_t*)d_in[4], (const bf16_t*)d_in[8],
                                     (const bf16_t*)d_in[5], (const bf16_t*)d_in[10], ws);
    k2_attn<<<256, 512, 0, stream>>>((const bf16_t*)d_in[9], ws);
    k3_out<<<256, 512, 0, stream>>>(x, ws, (bf16_t*)d_out);
}
```

```cpp
#include <hip/hip_runtime.h>
#include <hip/hip_bf16.h>
#include <stdint.h>

#define LAS __attribute__((address_space(3)))
#define DI __device__ __forceinline__
typedef unsigned short bf16_t;
typedef unsigned long long u64;
typedef __attribute__((address_space(1))) u64 gu64;
typedef short bf16x8 __attribute__((ext_vector_type(8)));
typedef short s16x4 __attribute__((ext_vector_type(4)));
typedef __bf16 bf16x2_t __attribute__((ext_vector_type(2)));
typedef float f32x16 __attribute__((ext_vector_type(16)));
typedef float f32x4 __attribute__((ext_vector_type(4)));
typedef float f32x2 __attribute__((ext_vector_type(2)));
typedef unsigned u32x4 __attribute__((ext_vector_type(4)));
typedef unsigned u32x2 __attribute__((ext_vector_type(2)));
typedef LAS unsigned char* lds_p;
typedef const LAS unsigned char* lds_cp;
typedef __amdgpu_buffer_rsrc_t rsrc_t;

constexpr int SEQ = 2048, DM = 512, NH = 32, FH = 688, FF = 1376;
constexpr int NKS = 299;
constexpr int F_E = 0;
constexpr int F_S = 512;
constexpr int F_X = 1024;
constexpr int F_Z = 1536;
constexpr int F_L = 2048;
constexpr int F_W = 2560;
constexpr int F_U = 3072;
constexpr int F_K = 3584;
constexpr int F_C = 4096;
constexpr unsigned WS_XN  = 65536;
constexpr unsigned WS_QF  = WS_XN + SEQ * DM * 2;
constexpr unsigned WS_KVF = WS_QF + NH * 64 * 8192;
constexpr unsigned WS_ZF  = WS_KVF + NH * 64 * 16384;
constexpr unsigned WS_Z2N = WS_ZF + 64 * 86 * 1024;
constexpr unsigned WS_AF  = WS_Z2N + SEQ * 768 * 2;
constexpr unsigned WS_WOF = WS_AF + 304 * 64 * 1024;
constexpr unsigned WS_END = WS_WOF + 304 * 16 * 1024;

#define PROF_DECL
#define PROF(i)
#define PROF_END(wsp)
#define MFMA32(a, b, c) __builtin_amdgcn_mfma_f32_32x32x16_bf16((a), (b), (c), 0, 0, 0)
#define SBAR() __builtin_amdgcn_sched_barrier(0)
#define PIN(x) asm volatile("" : "+v"(x))
#define WAIT_V(n) asm volatile("s_waitcnt vmcnt(" #n ")" ::: "memory")
#define WAIT_L(n) asm volatile("s_waitcnt lgkmcnt(" #n ")" ::: "memory")
#define BAR() asm volatile("s_barrier" ::: "memory")
#define SC1 16

DI unsigned cvtpk(float lo, float hi) { f32x2 v = {lo, hi}; bf16x2_t b = __builtin_convertvector(v, bf16x2_t); return __builtin_bit_cast(unsigned, b); }
DI float bflo(unsigned w) { return __uint_as_float(w << 16); }
DI float bfhi(unsigned w) { return __uint_as_float(w & 0xffff0000u); }
DI u32x4 pack8(const f32x16& a, int s, float sc) {
    u32x4 w;
    w[0] = cvtpk(a[8 * s + 0] * sc, a[8 * s + 1] * sc); w[1] = cvtpk(a[8 * s + 2] * sc, a[8 * s + 3] * sc);
    w[2] = cvtpk(a[8 * s + 4] * sc, a[8 * s + 5] * sc); w[3] = cvtpk(a[8 * s + 6] * sc, a[8 * s + 7] * sc);
    return w;
}
DI bf16x8 ld128(lds_cp p) { return *(const LAS bf16x8*)p; }
typedef short v4i16_t __attribute__((ext_vector_type(4)));
DI s16x4 ldtr(lds_cp p) { return __builtin_bit_cast(s16x4, __builtin_amdgcn_ds_read_tr16_b64_v4i16((LAS v4i16_t*)p)); }
DI bf16x8 ldtr2(lds_cp p, int step) { const s16x4 lo = ldtr(p), hi = ldtr(p + step); return __builtin_shufflevector(lo, hi, 0, 1, 2, 3, 4, 5, 6, 7); }
template <int I0, int I1> DI void dma_pair(unsigned v0, unsigned v1, const void* sbase, unsigned l) {
    asm volatile("s_add_u32 m0, %3, %4\n\ts_nop 0\n\tglobal_load_lds_dwordx4 %0, %2 sc1\n\ts_add_u32 m0, %3, %5\n\ts_nop 0\n\tglobal_load_lds_dwordx4 %1, %2 sc1"
                 :: "v"(v0), "v"(v1), "s"(sbase), "s"(l), "i"(I0), "i"(I1) : "memory", "scc");
}
template <int I, int O> DI void bdma1(unsigned voff, rsrc_t rs, unsigned soff, unsigned l) {
    asm volatile("s_add_u32 m0, %3, %4\n\ts_nop 0\n\tbuffer_load_dwordx4 %0, %1, %2 offen offset:%5 sc1 lds" :: "v"(voff), "s"(rs), "s"(soff), "s"(l), "i"(I), "i"(O) : "memory", "scc");
}
DI void dma_words(unsigned voff, const void* sbase, unsigned l) {
    asm volatile("s_mov_b32 m0, %2\n\ts_nop 0\n\tglobal_load_lds_dword %0, %1 sc1" :: "v"(voff), "s"(sbase), "s"(l) : "memory");
}
DI void st16(rsrc_t rs, unsigned off, u32x4 v) { __builtin_amdgcn_raw_buffer_store_b128(v, rs, off, 0, SC1); }
DI u32x4 ld16(rsrc_t rs, unsigned off) { return __builtin_amdgcn_raw_buffer_load_b128(rs, off, 0, SC1); }
DI float swap_add(float v) { auto r = __builtin_amdgcn_permlane32_swap(__float_as_uint(v), __float_as_uint(v), false, false); return __uint_as_float(r[0]) + __uint_as_float(r[1]); }
DI float swap_max(float v) { auto r = __builtin_amdgcn_permlane32_swap(__float_as_uint(v), __float_as_uint(v), false, false); return __builtin_fmaxf(__uint_as_float(r[0]), __uint_as_float(r[1])); }
DI float gelu_tanh(float v) {
    const float t = v * (-2.302208198f) * __builtin_fmaf(v * v, 0.044715f, 1.0f);
    return v * __builtin_amdgcn_rcpf(1.0f + __builtin_amdgcn_exp2f(t));
}
DI void set_flag(unsigned char* ws, int idx, u64 tag) { __hip_atomic_store((gu64*)ws + idx, tag, __ATOMIC_RELAXED, __HIP_MEMORY_SCOPE_AGENT); }
DI void poll_flags(unsigned char* ws, int idx, bool active, u64 tag) {
#pragma unroll 1
    for (unsigned spins = 0; spins < (1u << 17); ++spins) {
        const u64 v = __hip_atomic_load((gu64*)ws + idx, __ATOMIC_RELAXED, __HIP_MEMORY_SCOPE_AGENT);
        if (__all(!active || v == tag)) break;
        __builtin_amdgcn_s_sleep(4);
    }
}
#define ACQUIRE() asm volatile("buffer_inv sc1" ::: "memory")

DI void z_loop(const lds_p lds, const unsigned lds0, f32x16 (&acc)[2], const bf16_t* __restrict__ x, const bf16_t* __restrict__ w_in, const int rg, const int ct, const int tid, const int wid, const int lane) {
    const int rot = (rg & 1) * 4;
    const int row = tid >> 3, seg = tid & 7;
    const u32x4* src = (const u32x4*)(x + (size_t)(rg * 64 + row) * DM) + seg;
    u32x4 v[8];
#pragma unroll
    for (int j = 0; j < 8; ++j) asm volatile("global_load_dwordx4 %0, %1, off offset:%2" : "=v"(v[j]) : "v"(src), "i"(j * 128) : "memory");
    int gcol = ct * 256 + (wid >> 1) * 64 + (((lane & 7) ^ (((lane >> 4) & 1) << 2)) << 3);
    gcol = gcol > FF - 8 ? FF - 8 : gcol;
    const unsigned bv = (unsigned)(((32 * (wid & 1) + (lane >> 3)) * FF + gcol) * 2);
    const unsigned bdst = lds0 + wid * 4096;
    constexpr unsigned R8 = 8 * FF * 2;
#define Z_STAGE(j) do { const char* g_ = (const char*)w_in + (size_t)((((j) + rot) & 7) * 64) * (FF * 2); \
        dma_pair<65536 + ((j) % 3) * 32768, 65536 + ((j) % 3) * 32768 + 1024>(bv, bv + R8, g_, bdst); \
        dma_pair<65536 + ((j) % 3) * 32768 + 2048, 65536 + ((j) % 3) * 32768 + 3072>(bv + 2 * R8, bv + 3 * R8, g_, bdst); } while (0)
    SBAR();
    Z_STAGE(0); Z_STAGE(1); Z_STAGE(2);
    SBAR();
    {
        asm volatile("s_waitcnt vmcnt(12)" : "+v"(v[0]), "+v"(v[1]), "+v"(v[2]), "+v"(v[3]), "+v"(v[4]), "+v"(v[5]), "+v"(v[6]), "+v"(v[7]));
        float s = 0.f;
#pragma unroll
        for (int j = 0; j < 8; ++j)
#pragma unroll
            for (int e = 0; e < 4; ++e) s += bflo(v[j][e]) + bfhi(v[j][e]);
        s += __shfl_xor(s, 1); s += __shfl_xor(s, 2); s += __shfl_xor(s, 4);
        const float mean = s * (1.0f / DM);
        float q = 0.f;
#pragma unroll
        for (int j = 0; j < 8; ++j)
#pragma unroll
            for (int e = 0; e < 4; ++e) { const float a = bflo(v[j][e]) - mean, b = bfhi(v[j][e]) - mean; q += a * a + b * b; }
        q += __shfl_xor(q, 1); q += __shfl_xor(q, 2); q += __shfl_xor(q, 4);
        const float rstd = 1.0f / sqrtf(q * (1.0f / DM) + 1e-5f);
#pragma unroll
        for (int j = 0; j < 8; ++j) {
            u32x4 o;
#pragma unroll
            for (int e = 0; e < 4; ++e) o[e] = cvtpk((bflo(v[j][e]) - mean) * rstd, (bfhi(v[j][e]) - mean) * rstd);
            *(LAS u32x4*)(lds + j * 8192 + row * 128 + ((seg ^ ((row >> 1) & 7)) << 4)) = o;
        }
    }
    const int r = lane & 31, hh = lane >> 5, blk = (lane >> 4) & 1, qq = (lane & 15) >> 2, p = lane & 3;
    const lds_cp abase = lds + r * 128;
    const int asw = (r >> 1) & 7;
    const lds_cp bbase = lds + 65536 + (wid >> 1) * 8192 + (8 * hh + qq) * 128 + (((wid & 1) ^ (qq >> 1)) << 6) + blk * 32 + p * 8;
    const bool valid = ct * 8 + wid < 43;
    acc[0] = f32x16{}; acc[1] = f32x16{};
    asm volatile("s_waitcnt vmcnt(8) lgkmcnt(0)\n\ts_barrier" ::: "memory");
#pragma unroll
    for (int j = 0; j < 8; ++j) {
        const int kt = (j + rot) & 7;
        if (valid) {
            bf16x8 a0[4], a1[4], bf[4];
#pragma unroll
            for (int ks = 0; ks < 4; ++ks) {
                bf[ks] = ldtr2(bbase + (j % 3) * 32768 + ks * 2048, 512);
                a0[ks] = ld128(abase + kt * 8192 + (((2 * ks + hh) ^ asw) << 4));
                a1[ks] = ld128(abase + kt * 8192 + 4096 + (((2 * ks + hh) ^ asw) << 4));
            }
#pragma unroll
            for (int ks = 0; ks < 4; ++ks) { acc[0] = MFMA32(bf[ks], a0[ks], acc[0]); acc[1] = MFMA32(bf[ks], a1[ks], acc[1]); }
        }
        if (j < 6) asm volatile("s_waitcnt vmcnt(4) lgkmcnt(0)\n\ts_barrier" ::: "memory");
        else if (j == 6) asm volatile("s_waitcnt vmcnt(0) lgkmcnt(0)\n\ts_barrier" ::: "memory");
        else asm volatile("s_waitcnt lgkmcnt(0)\n\ts_barrier" ::: "memory");
        if (j == 0) Z_STAGE(3); else if (j == 1) Z_STAGE(4); else if (j == 2) Z_STAGE(5); else if (j == 3) Z_STAGE(6); else if (j == 4) Z_STAGE(7);
    }
#undef Z_STAGE
}
DI void z_epilogue(f32x16 (&acc)[2], const rsrc_t rws, const int rg, const int ct, const int wid, const int lane) {
    const int cb = ct * 8 + wid;
#pragma unroll
    for (int rb = 0; rb < 2; ++rb) {
#pragma unroll
        for (int e = 0; e < 16; ++e) acc[rb][e] = gelu_tanh(acc[rb][e]);
        const unsigned off = cb < 43 ? WS_ZF + ((rg * 2 + rb) * 86 + 2 * cb) * 1024 + lane * 16 : 0x80000000u;
        st16(rws, off, pack8(acc[rb], 0, 1.0f)); st16(rws, off + 1024, pack8(acc[rb], 1, 1.0f));
    }
}

struct Run { const char* A; const char* B; unsigned dA1, dA2, dB1, dB2, vA0, vA1, vB0, vB1; };
constexpr int HTB = 16384;
constexpr unsigned LDA = DM * 2, LDB = 4096 * 2;
#define G_SA(b, h) (((b) * 2 + (h)) * HTB)
#define G_SB(b, h) ((4 + (b) * 2 + (h)) * HTB)
#define G_STAGE(bufoff, gb, v0, v1) dma_pair<(bufoff), (bufoff) + 8192>((v0), (v1), (gb), ldsw)
#define G_LDA(dst, b, h) do { _Pragma("unroll") for (int m_ = 0; m_ < 2; ++m_) _Pragma("unroll") for (int k_ = 0; k_ < 4; ++k_) dst[m_][k_] = ld128(lds + G_SA(b, h) + aoff[k_] + m_ * 4096); } while (0)
#define G_LDB(dst, b, h) do { _Pragma("unroll") for (int k_ = 0; k_ < 4; ++k_) dst[k_] = ldtr2(lds + G_SB(b, h) + boff + k_ * 4096, 1024); } while (0)
#define G_MMA(ai, bj, At, Bt) do { __builtin_amdgcn_s_setprio(1); _Pragma("unroll") for (int k_ = 0; k_ < 4; ++k_) _Pragma("unroll") for (int m_ = 0; m_ < 2; ++m_) \
        acc[ai][bj][m_] = SWAP ? MFMA32(At[m_][k_], Bt[k_], acc[ai][bj][m_]) : MFMA32(Bt[k_], At[m_][k_], acc[ai][bj][m_]); __builtin_amdgcn_s_setprio(0); } while (0)

DI void g_prologue_b(const Run& u, const unsigned ldsw) {
    G_STAGE(G_SB(0, 0), u.B, u.vB0, u.vB1); G_STAGE(G_SB(0, 1), u.B + u.dB1, u.vB0, u.vB1);
}
DI void g_prologue_rest(const Run& u, const unsigned ldsw) {
    G_STAGE(G_SA(0, 0), u.A, u.vA0, u.vA1); G_STAGE(G_SA(0, 1), u.A + u.dA1, u.vA0, u.vA1);
    G_STAGE(G_SB(1, 0), u.B + u.dB2, u.vB0, u.vB1); G_STAGE(G_SA(1, 0), u.A + u.dA2, u.vA0, u.vA1); G_STAGE(G_SB(1, 1), u.B + u.dB2 + u.dB1, u.vB0, u.vB1);
}
DI void check_parked(lds_cp parked, unsigned char* ws, const int fidx, const u64 tag, const int lane) {
    const unsigned w = *(const LAS unsigned*)(parked + lane * 4);
    if (!__all(lane >= 24 || w == ((lane & 1) ? (unsigned)(tag >> 32) : (unsigned)tag))) poll_flags(ws, fidx + (lane & 15), lane < 12, tag);
    ACQUIRE();
}

template <bool SWAP, int ROUND>
DI void g_kloop(const lds_p lds, const unsigned lds0, f32x16 (&acc)[2][2][2], const Run& cur, const Run& nxt, const int (&aoff)[4], const int boff, const unsigned ldsw,
                unsigned char* ws, const u64 tag, const int fl0, const int fl1, const int fatt, const char* q1src, const int lane, const int wid) {
    bf16x8 At[2][4], B0[4], B1[4];
#pragma unroll
    for (int i = 0; i < 8; ++i) acc[i >> 2][(i >> 1) & 1][i & 1] = f32x16{};
#pragma unroll 1
    for (int t = 0; t < 8; t += 2) {
        const bool last = (t == 6);
        const char* a1 = cur.A + (t + 1) * 128 + 128 * LDA;
        const char* a2 = last ? nxt.A : cur.A + (t + 2) * 128;
        const char* b2 = last ? nxt.B : cur.B + (size_t)(t + 2) * 64 * LDB;
        const unsigned dA1 = last ? nxt.dA1 : cur.dA1, dA2 = last ? nxt.dA2 : cur.dA2, dB1 = last ? nxt.dB1 : cur.dB1, dB2 = last ? nxt.dB2 : cur.dB2;
        const unsigned vA0 = last ? nxt.vA0 : cur.vA0, vA1 = last ? nxt.vA1 : cur.vA1, vB0 = last ? nxt.vB0 : cur.vB0, vB1 = last ? nxt.vB1 : cur.vB1;
        if (ROUND == 1) {
            if (t == 4 && wid == 0) check_parked(lds + 131072, ws, fatt, tag, lane);
            if (last) { dma_pair<131072, 131072 + 1024>(lane * 16, lane * 16 + 1024, q1src, lds0 + wid * 4096); dma_pair<131072 + 2048, 131072 + 3072>(lane * 16 + 2048, lane * 16 + 3072, q1src, lds0 + wid * 4096); }
        }
        G_LDB(B0, 0, 0); SBAR(); G_LDA(At, 0, 0); G_STAGE(G_SA(1, 1), a1, cur.vA0, cur.vA1);
        WAIT_L(8); BAR(); WAIT_L(0); SBAR(); G_MMA(0, 0, At, B0); SBAR(); BAR();
        G_LDB(B1, 0, 1); G_STAGE(G_SB(0, 0), b2, vB0, vB1);
        BAR(); WAIT_L(0); SBAR(); G_MMA(0, 1, At, B1); SBAR(); BAR();
        G_LDA(At, 0, 1); G_STAGE(G_SA(0, 0), a2, vA0, vA1);
        BAR(); WAIT_L(0); SBAR(); G_MMA(1, 0, At, B0); SBAR(); BAR();
        G_STAGE(G_SB(0, 1), b2 + dB1, vB0, vB1);
        WAIT_V(6); BAR(); SBAR(); G_MMA(1, 1, At, B1); SBAR(); BAR();
        if (wid == 0) {
            if (t == 0) {
                if (lane == 0) { if (fl0 >= 0) set_flag(ws, fl0, tag); if (fl1 >= 0) set_flag(ws, fl1, tag); }
            }
            if (ROUND == 1 && t == 2) dma_words(lane * 4, ws + (size_t)fatt * 8, lds0 + 131072);
        }
        G_LDB(B0, 1, 0); SBAR(); G_LDA(At, 1, 0); G_STAGE(G_SA(0, 1), a2 + dA1, vA0, vA1);
        WAIT_L(8); BAR(); WAIT_L(0); SBAR(); G_MMA(0, 0, At, B0); SBAR(); BAR();
        G_LDB(B1, 1, 1); G_STAGE(G_SB(1, 0), b2 + dB2, vB0, vB1);
        BAR(); WAIT_L(0); SBAR(); G_MMA(0, 1, At, B1); SBAR(); BAR();
        G_LDA(At, 1, 1); G_STAGE(G_SA(1, 0), a2 + dA2, vA0, vA1);
        BAR(); WAIT_L(0); SBAR(); G_MMA(1, 0, At, B0); SBAR(); BAR();
        G_STAGE(G_SB(1, 1), b2 + dB2 + dB1, vB0, vB1);
        WAIT_V(6); BAR(); SBAR(); G_MMA(1, 1, At, B1); SBAR();
        if (!(last && wid >= 4)) BAR();
    }
}

DI void g_epilogue(const f32x16 (&acc)[2][2][2], const rsrc_t rws, const int type, const int pn, const int pm, const int wr, const int wc, const int lane) {
    constexpr float QS = 0.08838834764831845f * 1.4426950408889634f;
    const float sc = type == 2 ? QS : 1.0f;
    const unsigned base = (type == 2 ? WS_QF : WS_KVF + (type == 1 ? 8192 : 0)) + wc * 2048 + lane * 16;
    const unsigned hstride = type == 2 ? 8192 : 16384;
#pragma unroll
    for (int ai = 0; ai < 2; ++ai)
#pragma unroll
        for (int bj = 0; bj < 2; ++bj)
#pragma unroll
            for (int m = 0; m < 2; ++m) {
                const unsigned dst = base + (unsigned)((2 * pn + bj) * 64 + pm * 8 + ai * 4 + wr * 2 + m) * hstride;
                st16(rws, dst, pack8(acc[ai][bj][m], 0, sc)); st16(rws, dst + 1024, pack8(acc[ai][bj][m], 1, sc));
            }
}

#define EX2(v) __builtin_amdgcn_exp2f(v)
#define MX3(a, b, c) __builtin_fmaxf(__builtin_fmaxf((a), (b)), (c))
constexpr float ATT_THR = 32.0f;
DI void att_mask(f32x16& s, int dq) {
#pragma unroll
    for (int e = 0; e < 16; ++e) if ((e & 3) + 8 * (e >> 2) > dq) s[e] = -__builtin_inff();
}

__global__ __launch_bounds__(512, 2) void fused_fwd(const bf16_t* __restrict__ x, const bf16_t* __restrict__ ckv, const bf16_t* __restrict__ wq, const bf16_t* __restrict__ wk,
                                                    const bf16_t* __restrict__ wv, const bf16_t* __restrict__ wo, const bf16_t* __restrict__ w_in, const bf16_t* __restrict__ wsp,
                                                    const bf16_t* __restrict__ bsp, const bf16_t* __restrict__ wout, bf16_t* __restrict__ out, unsigned char* ws) {
    __shared__ __attribute__((aligned(16))) unsigned char smem[163840];
    const lds_p lds = (lds_p)smem;
    const unsigned lds0 = (unsigned)(uintptr_t)smem;
    const int tid = threadIdx.x, wid = __builtin_amdgcn_readfirstlane(tid >> 6), lane = tid & 63;
    const int bid = blockIdx.x, x8 = bid & 7, kk = bid >> 3;
    const int r = lane & 31, hh = lane >> 5, blk = (lane >> 4) & 1, qq = (lane & 15) >> 2, p = lane & 3;
    const rsrc_t rws = __builtin_amdgcn_make_buffer_rsrc((void*)ws, (short)0, WS_END, 0x00020000);
    const unsigned ldsw = lds0 + (unsigned)wid * 1024u;
    PROF_DECL;
    u64 eprev = __hip_atomic_load((gu64*)ws + F_E, __ATOMIC_RELAXED, __HIP_MEMORY_SCOPE_AGENT);
    u64 tag;
#define MAKE_TAG() do { PIN(eprev); u64 z_ = eprev + 0x9E3779B97F4A7C15ull; z_ = (z_ ^ (z_ >> 30)) * 0xBF58476D1CE4E5B9ull; z_ = (z_ ^ (z_ >> 27)) * 0x94D049BB133111EBull; z_ ^= z_ >> 31; if (z_ == eprev) z_ = ~z_; \
        tag = ((u64)(unsigned)__builtin_amdgcn_readfirstlane((unsigned)(z_ >> 32)) << 32) | (unsigned)__builtin_amdgcn_readfirstlane((unsigned)z_); } while (0)
    const int head = 4 * x8 + (kk >> 3), pr = kk & 7, i1 = 15 - pr, i2 = pr;
    const unsigned char* kvh = ws + WS_KVF + (size_t)head * 64 * 16384;
    const char* q1src = (const char*)ws + WS_QF + (size_t)(head * 64 + 4 * i1) * 8192 + wid * 4096;
    const int fatt = F_K + (head >> 1) * 32;
    Run rat;
    rat.A = (const char*)kvh; rat.B = (const char*)kvh + 32768; rat.dA1 = rat.dB1 = 65536; rat.dA2 = rat.dB2 = 8192;
    rat.vA0 = rat.vB0 = wid * 1024 + lane * 16; rat.vA1 = rat.vB1 = rat.vA0 + 16384;
    int pf;
    int aoff[4];
    const unsigned arow = 8 * wid + (lane >> 3), achk = ((lane & 7) ^ (((wid & 1) << 2) + (lane >> 4))) << 4;
    const unsigned brow = 4 * wid + (lane >> 4), bchk = ((lane & 15) ^ (((lane >> 4) & 3) << 2)) << 4;

    if (kk < 24) {
        const int type = kk >> 3, pn = 2 * x8 + ((kk >> 2) & 1), pm0 = (kk & 3) + (type == 2 ? 4 : 0), pm1 = pm0 ^ 4, wr = wid >> 2, wc = wid & 3;
        Run u0, u1;
        const char* Ab = type == 2 ? (const char*)ws + WS_XN : (const char*)ckv;
        u0.B = (const char*)(type == 0 ? wk : type == 1 ? wv : wq) + pn * 512; u0.A = Ab + (size_t)pm0 * 256 * LDA;
        u0.dA1 = 128 * LDA; u0.dA2 = 128; u0.dB1 = 256; u0.dB2 = 64 * LDB;
        u0.vA0 = arow * LDA + achk; u0.vA1 = u0.vA0 + 64 * LDA; u0.vB0 = brow * LDB + bchk; u0.vB1 = u0.vB0 + 32 * LDB;
        u1 = u0; u1.A = Ab + (size_t)pm1 * 256 * LDA;
        int fz = -1;
        if (type < 2) {
            f32x16 za[2];
            const int rg = 4 * x8 + (kk >> 2), ct = kk & 3;
            z_loop(lds, lds0, za, x, w_in, rg, ct, tid, wid, lane);
            MAKE_TAG();
            SBAR();
            g_prologue_b(u0, ldsw); g_prologue_rest(u0, ldsw);
            SBAR();
            z_epilogue(za, rws, rg, ct, wid, lane);
            fz = F_Z + rg * 8 + ct;
            PROF(1);
            WAIT_V(10);
        } else {
            const int j = x8 * 8 + (kk - 16), row = 32 * j + 4 * wid + (lane >> 4);
            const u32x4* src = (const u32x4*)(x + (size_t)row * DM) + (lane & 15);
            u32x4 v[4];
#pragma unroll
            for (int i = 0; i < 4; ++i) v[i] = src[16 * i];
            float s = 0.f;
#pragma unroll
            for (int i = 0; i < 4; ++i)
#pragma unroll
                for (int e = 0; e < 4; ++e) s += bflo(v[i][e]) + bfhi(v[i][e]);
            s += __shfl_xor(s, 1); s += __shfl_xor(s, 2); s += __shfl_xor(s, 4); s += __shfl_xor(s, 8);
            const float mean = s * (1.0f / DM);
            float q = 0.f;
#pragma unroll
            for (int i = 0; i < 4; ++i)
#pragma unroll
                for (int e = 0; e < 4; ++e) { const float a = bflo(v[i][e]) - mean, b = bfhi(v[i][e]) - mean; q += a * a + b * b; }
            q += __shfl_xor(q, 1); q += __shfl_xor(q, 2); q += __shfl_xor(q, 4); q += __shfl_xor(q, 8);
            const float rstd = 1.0f / sqrtf(q * (1.0f / DM) + 1e-5f);
#pragma unroll
            for (int i = 0; i < 4; ++i) {
                u32x4 o;
#pragma unroll
                for (int e = 0; e < 4; ++e) o[e] = cvtpk((bflo(v[i][e]) - mean) * rstd, (bfhi(v[i][e]) - mean) * rstd);
                st16(rws, WS_XN + row * 1024 + ((lane & 15) + 16 * i) * 16, o);
            }
            MAKE_TAG();
            WAIT_V(0); BAR();
            if (tid == 0) set_flag(ws, F_X + j, tag);
            g_prologue_b(u0, ldsw);
            if (wid == 0) { poll_flags(ws, F_X + ((lane & 8) ? pm1 : pm0) * 8 + (lane & 7), lane < 16, tag); ACQUIRE(); WAIT_V(0); }
            BAR();
            g_prologue_rest(u0, ldsw);
            PROF(1);
            WAIT_V(6);
        }
#pragma unroll
        for (int k = 0; k < 4; ++k) aoff[k] = (wr * 64 + r) * 128 + (((2 * k + hh) ^ ((r >> 1) & 7)) << 4);
        const int boff = (8 * hh + qq) * 256 + ((wc ^ qq) << 6) + blk * 32 + p * 8;
        BAR();
        if (wr == 1) BAR();
        PROF(2);
        f32x16 acc[2][2][2];
        const int fk = F_K + pn * 32 + type * 4 + (kk & 3);
        if (type == 1) g_kloop<true, 0>(lds, lds0, acc, u0, u1, aoff, boff, ldsw, ws, tag, fz, F_S + bid, fatt, q1src, lane, wid);
        else           g_kloop<false, 0>(lds, lds0, acc, u0, u1, aoff, boff, ldsw, ws, tag, fz, F_S + bid, fatt, q1src, lane, wid);
        PROF(3);
        g_epilogue(acc, rws, type, pn, pm0, wr, wc, lane);
        if (wr == 1) BAR();
        if (type == 1) g_kloop<true, 1>(lds, lds0, acc, u1, rat, aoff, boff, ldsw, ws, tag, fk, -1, fatt, q1src, lane, wid);
        else           g_kloop<false, 1>(lds, lds0, acc, u1, rat, aoff, boff, ldsw, ws, tag, fk, -1, fatt, q1src, lane, wid);
        PROF(4);
        G_STAGE(G_SA(1, 1), rat.A + rat.dA1 + rat.dA2, rat.vA0, rat.vA1);
        SBAR();
        g_epilogue(acc, rws, type, pn, pm1, wr, wc, lane);
        pf = fk + 16;
        WAIT_V(16);
    } else {
        {
            f32x16 za[2];
            const int rg = 4 * x8 + ((kk - 24) >> 1), ct = 4 + (kk & 1);
            z_loop(lds, lds0, za, x, w_in, rg, ct, tid, wid, lane);
            MAKE_TAG();
            z_epilogue(za, rws, rg, ct, wid, lane);
            WAIT_V(0); BAR();
            if (tid == 0) { set_flag(ws, F_Z + rg * 8 + ct, tag); set_flag(ws, F_S + bid, tag); }
        }
        PROF(1);
        {
            const int tb = 8 * x8 + (kk - 24);
            if (tid < 320) { const int zr = tid / 10; *(LAS u32x4*)(lds + zr * 1552 + 1376 + (tid - zr * 10) * 16) = (u32x4){0u, 0u, 0u, 0u}; }
            if (wid == 0) poll_flags(ws, F_Z + (tb >> 1) * 8 + 2 + (lane & 3), lane < 4, tag);
            BAR();
            u32x4 v[6];
#pragma unroll
            for (int i = 0; i < 6; ++i) v[i] = ld16(rws, wid + 8 * i < 43 ? WS_ZF + (tb * 86 + 43 + wid + 8 * i) * 1024 + lane * 16 : 0x80000000u);
            float s = 0.f;
#pragma unroll
            for (int i = 0; i < 6; ++i)
#pragma unroll
                for (int e = 0; e < 4; ++e) s += bflo(v[i][e]) + bfhi(v[i][e]);
            LAS float* red = (LAS float*)(lds + 53248);
            red[wid * 64 + lane] = s;
            WAIT_L(0); BAR();
            s = 0.f;
#pragma unroll
            for (int w = 0; w < 8; ++w) s += red[w * 64 + lane];
            const float mean = swap_add(s) * (1.0f / FH);
            float q = 0.f;
#pragma unroll
            for (int i = 0; i < 6; ++i)
                if (wid + 8 * i < 43) {
#pragma unroll
                    for (int e = 0; e < 4; ++e) { const float a = bflo(v[i][e]) - mean, b = bfhi(v[i][e]) - mean; q += a * a + b * b; }
                }
            red[512 + wid * 64 + lane] = q;
            WAIT_L(0); BAR();
            q = 0.f;
#pragma unroll
            for (int w = 0; w < 8; ++w) q += red[512 + w * 64 + lane];
            const float rstd = 1.0f / sqrtf(swap_add(q) * (1.0f / FH) + 1e-5f);
#pragma unroll
            for (int i = 0; i < 6; ++i)
                if (wid + 8 * i < 43) {
                    unsigned o[4];
#pragma unroll
                    for (int e = 0; e < 4; ++e) o[e] = cvtpk((bflo(v[i][e]) - mean) * rstd, (bfhi(v[i][e]) - mean) * rstd);
                    const lds_p d = lds + r * 1552 + (wid + 8 * i) * 32 + hh * 8;
                    *(LAS u32x2*)d = (u32x2){o[0], o[1]}; *(LAS u32x2*)(d + 16) = (u32x2){o[2], o[3]};
                }
            WAIT_L(0); BAR();
#pragma unroll
            for (int i = 0; i < 6; ++i) { const int c = tid + 512 * i, zr = c / 96; st16(rws, WS_Z2N + tb * 49152 + c * 16, *(const LAS u32x4*)(lds + zr * 1552 + (c - zr * 96) * 16)); }
            WAIT_V(0); BAR();
            if (tid == 0) set_flag(ws, F_L + tb, tag);
        }
        if (kk < 30) {
            const int pm = x8, pn = kk - 24, grp = wid >> 2;
            constexpr unsigned SLA = SEQ * 2, SLB = 768 * 2;
            const char* Ag = (const char*)wsp + (size_t)pm * 256 * SLA;
            const char* Bg = (const char*)ws + WS_Z2N + pn * 256;
            const unsigned vA0 = arow * SLA + achk, vA1 = vA0 + 64 * SLA, vB0 = brow * SLB + bchk, vB1 = vB0 + 32 * SLB;
            dma_pair<0, 8192>(vA0, vA1, Ag, ldsw); dma_pair<16384, 24576>(vA0, vA1, Ag + 128 * SLA, ldsw);
            dma_pair<49152, 49152 + 8192>(vA0, vA1, Ag + 128, ldsw); dma_pair<49152 + 16384, 49152 + 24576>(vA0, vA1, Ag + 128 + 128 * SLA, ldsw);
            if (wid == 0) {
                poll_flags(ws, F_L + lane, true, tag);
                poll_flags(ws, F_Z + (4 * pm + ((lane >> 2) & 3)) * 8 + (lane & 3), lane < 16 && (lane & 3) < 3, tag);
                ACQUIRE(); WAIT_V(0);
            }
            BAR();
            dma_pair<32768, 32768 + 8192>(vB0, vB1, Bg, ldsw); dma_pair<49152 + 32768, 49152 + 32768 + 8192>(vB0, vB1, Bg + 64 * SLB, ldsw);
            u32x4 z1[2][2][2];
            float bias[2];
            const int tb0 = 8 * pm + 2 * (wid & 3), fs0 = 8 * pn + 4 * grp;
#pragma unroll
            for (int m = 0; m < 2; ++m) {
                bias[m] = __uint_as_float((unsigned)bsp[(tb0 + m) * 32 + r] << 16);
#pragma unroll
                for (int c = 0; c < 4; ++c) z1[m][c >> 1][c & 1] = ld16(rws, fs0 + c < 43 ? WS_ZF + ((tb0 + m) * 86 + fs0 + c) * 1024 + lane * 16 : 0x80000000u);
            }
#pragma unroll
            for (int c = 0; c < 8; ++c) PIN(z1[c >> 2][(c >> 1) & 1][c & 1]);
            PIN(bias[0]); PIN(bias[1]);
#pragma unroll
            for (int k = 0; k < 4; ++k) aoff[k] = ((wid >> 1) & 1) * 16384 + ((wid & 1) * 64 + r) * 128 + (((2 * k + hh) ^ ((r >> 1) & 7)) << 4);
            const int bo0 = 32768 + (8 * hh + qq) * 256 + (((2 * grp) ^ qq) << 6) + blk * 32 + p * 8, bo1 = 32768 + (8 * hh + qq) * 256 + (((2 * grp + 1) ^ qq) << 6) + blk * 32 + p * 8;
            f32x16 acc[2][2];
#pragma unroll
            for (int c = 0; c < 4; ++c) acc[c >> 1][c & 1] = f32x16{};
            WAIT_V(0); BAR();
            if (grp == 1) BAR();
            PROF(2);
            unsigned so = 0, sn = 2 * 49152;
#pragma unroll 1
            for (int t = 0; t < 32; ++t) {
                bf16x8 At[2][4], Bt[2][4];
                const lds_cp sb = lds + so;
#pragma unroll
                for (int k = 0; k < 4; ++k) { Bt[0][k] = ldtr2(sb + bo0 + k * 4096, 1024); At[0][k] = ld128(sb + aoff[k]); At[1][k] = ld128(sb + aoff[k] + 4096); Bt[1][k] = ldtr2(sb + bo1 + k * 4096, 1024); }
                if (t < 30) {
                    const char* a_ = Ag + (t + 2) * 128;
                    dma_pair<0, 8192>(vA0, vA1, a_, ldsw + sn); dma_pair<16384, 24576>(vA0, vA1, a_ + 128 * SLA, ldsw + sn);
                    dma_pair<32768, 32768 + 8192>(vB0, vB1, Bg + (size_t)(t + 2) * 64 * SLB, ldsw + sn);
                }
                if (wid == 0) {
                    if (t == 20) dma_words(lane * 4, ws + (size_t)fatt * 8, lds0 + 147456);
                    if (t == 24) check_parked(lds + 147456, ws, fatt, tag, lane);
                }
                if (grp == 1) { if (t < 30) WAIT_V(6); else WAIT_V(0); }
                WAIT_L(0); BAR(); SBAR();
                __builtin_amdgcn_s_setprio(1);
#pragma unroll
                for (int k = 0; k < 4; ++k)
#pragma unroll
                    for (int c = 0; c < 4; ++c) acc[c >> 1][c & 1] = MFMA32(Bt[c & 1][k], At[c >> 1][k], acc[c >> 1][c & 1]);
                __builtin_amdgcn_s_setprio(0);
                SBAR();
                if (grp == 0) { if (t < 30) WAIT_V(6); else WAIT_V(0); }
                if (!(t == 31 && grp == 1)) BAR();
                sn = so; so = so == 2 * 49152 ? 0u : so + 49152u;
            }
            PROF(4);
            g_prologue_b(rat, ldsw); g_prologue_rest(rat, ldsw); G_STAGE(G_SA(1, 1), rat.A + rat.dA1 + rat.dA2, rat.vA0, rat.vA1);
            dma_pair<131072, 131072 + 1024>(lane * 16, lane * 16 + 1024, q1src, lds0 + wid * 4096); dma_pair<131072 + 2048, 131072 + 3072>(lane * 16 + 2048, lane * 16 + 3072, q1src, lds0 + wid * 4096);
            SBAR();
#pragma unroll
            for (int m = 0; m < 2; ++m)
#pragma unroll
                for (int c = 0; c < 4; ++c) {
                    const f32x16& a = acc[m][c >> 1]; const u32x4 z = z1[m][c >> 1][c & 1]; const int s8 = 8 * (c & 1);
                    u32x4 o;
#pragma unroll
                    for (int e = 0; e < 4; ++e) o[e] = cvtpk(bflo(z[e]) * (a[s8 + 2 * e] + bias[m]), bfhi(z[e]) * (a[s8 + 2 * e + 1] + bias[m]));
                    st16(rws, fs0 + c < 43 ? WS_AF + ((256 + fs0 + c) * 64 + tb0 + m) * 1024 + lane * 16 : 0x80000000u, o);
                }
            pf = F_U + pm * 8 + pn;
            WAIT_V(8);
        } else {
            const int aw = x8 * 2 + (kk - 30);
            const lds_cp rd = lds + wid * 16384 + (4 * hh + qq) * 1024 + blk * 32 + p * 8;
#pragma unroll 1
            for (int ks = aw * 8 + wid; ks < NKS; ks += 128) {
                const char* src = ks < 256 ? (const char*)wo + (size_t)ks * 16 * 1024 : (const char*)wout + (size_t)(ks - 256) * 16 * 1024;
#pragma unroll
                for (int i = 0; i < 8; ++i) dma_pair<0, 1024>(lane * 16 + i * 2048, lane * 16 + i * 2048 + 1024, src, lds0 + wid * 16384 + i * 2048);
                WAIT_V(0);
#pragma unroll
                for (int nb = 0; nb < 16; ++nb) st16(rws, WS_WOF + ks * 16384 + nb * 1024 + lane * 16, __builtin_bit_cast(u32x4, ldtr2(rd + nb * 64, 8192)));
                WAIT_L(0);
            }
            if (wid == 0) { poll_flags(ws, fatt + (lane & 15), lane < 12, tag); ACQUIRE(); }
            WAIT_V(0); BAR();
            if (tid == 0) set_flag(ws, F_W + aw, tag);
            PROF(4);
            g_prologue_b(rat, ldsw); g_prologue_rest(rat, ldsw); G_STAGE(G_SA(1, 1), rat.A + rat.dA1 + rat.dA2, rat.vA0, rat.vA1);
            dma_pair<131072, 131072 + 1024>(lane * 16, lane * 16 + 1024, q1src, lds0 + wid * 4096); dma_pair<131072 + 2048, 131072 + 3072>(lane * 16 + 2048, lane * 16 + 3072, q1src, lds0 + wid * 4096);
            pf = -1;
            WAIT_V(0);
        }
    }
    BAR();
    PROF(5);
    {
        const int kh = wid >> 2, rg = wid & 3;
        const rsrc_t rkv = __builtin_amdgcn_make_buffer_rsrc((void*)kvh, (short)0, 64 * 16384, 0x00020000);
        const unsigned dv0 = rg * 2048 + lane * 16 + kh * 32768, dv1 = dv0 + 8192, dv2 = dv0 + 16384, dv3 = dv0 + 24576;
        const unsigned wrapoff = (unsigned)(i1 + 1) * 65536u;
        unsigned sA_ = 0, sB_ = 65536, lA = lds0 + kh * 65536 + rg * 2048, lB = lA + 16384;
#define A_ADV() do { sA_ = sB_; sB_ += 65536u; if (sB_ == wrapoff) sB_ = 0; const unsigned t_ = lA; lA = lB; lB = t_; } while (0)
#define D_KO(h) bdma1<8192, (h) * 1024>(dv2, rkv, sA_, lA)
#define D_VE(h) bdma1<32768, (h) * 1024>(dv1, rkv, sA_, lA)
#define D_KE(h) bdma1<0, (h) * 1024>(dv0, rkv, sB_, lB)
#define D_VO(h) bdma1<40960, (h) * 1024>(dv3, rkv, sA_, lA)
#define NOP_ (void)0
#define PUBLISH() do { if (pf >= 0) { if (tid == 0) set_flag(ws, pf, tag); pf = -1; } } while (0)
        bf16x8 q[8];
        const lds_cp qrd = lds + 131072 + rg * 8192 + lane * 16;
#pragma unroll
        for (int d = 0; d < 8; ++d) q[d] = ld128(qrd + d * 1024);
        const lds_cp rbase = lds + kh * 65536 + lane * 16;
        int so = 0;
#pragma unroll 1
        for (int blkno = 0; blkno < 2; ++blkno) {
            const int ib = blkno ? i2 : i1;
            const int qrow = 128 * ib + 32 * rg + r - 4 * hh - 64 * kh;
            f32x16 o[4], sA, sB, negm;
            bf16x8 fr[8];
            u32x4 pw0, pw1;
            float mref, l0 = 0.f, l1 = 0.f, resc = 1.0f;
            bool dead = false, pend = false;
            lds_cp pc = rbase + so, pn = rbase + (so ^ 16384);
#pragma unroll
            for (int d = 0; d < 8; ++d) fr[d] = ld128(pc + d * 1024);
            sA = f32x16{};
#pragma unroll
            for (int d = 0; d < 8; ++d) sA = MFMA32(fr[d], q[d], sA);
            asm volatile("s_waitcnt lgkmcnt(0)\n\ts_barrier" ::: "memory");
            A_ADV();
            D_KO(0); D_KO(1);
#pragma unroll
            for (int d = 0; d < 4; ++d) fr[d] = ld128(pc + 8192 + d * 1024);
#pragma unroll
            for (int d = 0; d < 4; ++d) o[d] = f32x16{};
            if (ib == 0) att_mask(sA, qrow);
            {
                float m = MX3(sA[0], sA[1], sA[2]);
#pragma unroll
                for (int e = 3; e < 15; e += 2) m = MX3(m, sA[e], sA[e + 1]);
                m = swap_max(__builtin_fmaxf(m, sA[15]));
                dead = m == -__builtin_inff();
                mref = dead ? 0.f : m;
#pragma unroll
                for (int e = 0; e < 16; ++e) { sA[e] -= mref; negm[e] = -mref; }
                PIN(negm);
#pragma unroll
                for (int e = 0; e < 8; ++e) sA[e] = EX2(sA[e]);
                l0 = sA[0] + sA[2]; l1 = sA[1] + sA[3];
            }
#define A_RD(j, ptr) do { fr[j] = ld128(ptr); } while (0)
#define A_P1(SC, SN, KP, VP, E0, E1, E2) do { \
            A_RD(4, (KP) + 4096); SBAR(); SN = MFMA32(fr[0], q[0], negm); SC[8] = EX2(SC[8]); SC[9] = EX2(SC[9]); pw0[0] = cvtpk(SC[0], SC[1]); PIN(SC); PIN(pw0); SBAR(); \
            A_RD(5, (KP) + 5120); SBAR(); SN = MFMA32(fr[1], q[1], SN); SC[10] = EX2(SC[10]); SC[11] = EX2(SC[11]); pw0[1] = cvtpk(SC[2], SC[3]); PIN(SC); PIN(pw0); E0; SBAR(); \
            A_RD(6, (KP) + 6144); SBAR(); SN = MFMA32(fr[2], q[2], SN); SC[12] = EX2(SC[12]); SC[13] = EX2(SC[13]); pw0[2] = cvtpk(SC[4], SC[5]); PIN(SC); PIN(pw0); SBAR(); \
            A_RD(7, (KP) + 7168); SBAR(); SN = MFMA32(fr[3], q[3], SN); SC[14] = EX2(SC[14]); SC[15] = EX2(SC[15]); pw0[3] = cvtpk(SC[6], SC[7]); PIN(SC); PIN(pw0); SBAR(); \
            A_RD(0, (VP));        SBAR(); SN = MFMA32(fr[4], q[4], SN); l0 += SC[4]; l1 += SC[5]; l0 += SC[6]; l1 += SC[7]; PIN(l0); PIN(l1); E1; SBAR(); \
            A_RD(1, (VP) + 2048); SBAR(); SN = MFMA32(fr[5], q[5], SN); l0 += SC[8]; l1 += SC[9]; pw1[0] = cvtpk(SC[8], SC[9]); PIN(l0); PIN(l1); PIN(pw1); SBAR(); \
            A_RD(2, (VP) + 4096); SBAR(); SN = MFMA32(fr[6], q[6], SN); l0 += SC[10]; l1 += SC[11]; pw1[1] = cvtpk(SC[10], SC[11]); PIN(l0); PIN(l1); PIN(pw1); SBAR(); \
            A_RD(3, (VP) + 6144); SBAR(); SN = MFMA32(fr[7], q[7], SN); E2; SBAR(); } while (0)
#define PWF(x) __builtin_bit_cast(bf16x8, x)
#define A_P2A(SC, SN, VP, MASK, DQ) do { \
            A_RD(4, (VP) + 1024); A_RD(5, (VP) + 3072); SBAR(); o[0] = MFMA32(fr[0], PWF(pw0), o[0]); l0 += SC[12]; l1 += SC[13]; pw1[2] = cvtpk(SC[12], SC[13]); PIN(l0); PIN(l1); PIN(pw1); SBAR(); \
            A_RD(6, (VP) + 5120); A_RD(7, (VP) + 7168); SBAR(); o[1] = MFMA32(fr[1], PWF(pw0), o[1]); l0 += SC[14]; l1 += SC[15]; pw1[3] = cvtpk(SC[14], SC[15]); PIN(l0); PIN(l1); PIN(pw1); SBAR(); \
            float ma_ = 0.f, mb_ = 0.f; \
            o[2] = MFMA32(fr[2], PWF(pw0), o[2]); \
            if (MASK) att_mask(SN, DQ); \
            ma_ = MX3(SN[0], SN[1], SN[2]); mb_ = MX3(SN[3], SN[4], SN[5]); ma_ = MX3(ma_, SN[6], SN[7]); mb_ = MX3(mb_, SN[8], SN[9]); PIN(ma_); PIN(mb_); SBAR(); \
            o[3] = MFMA32(fr[3], PWF(pw0), o[3]); \
            ma_ = MX3(ma_, SN[10], SN[11]); mb_ = MX3(mb_, SN[12], SN[13]); ma_ = MX3(ma_, SN[14], SN[15]); ma_ = __builtin_fmaxf(ma_, mb_); \
            if (__builtin_expect(__any(ma_ > ATT_THR), 0)) {            \
                const float dl_ = __builtin_fmaxf(swap_max(ma_), 0.f); mref += dl_; resc = EX2(-dl_); l0 *= resc; l1 *= resc; pend = true; \
                _Pragma("unroll") for (int e = 0; e < 16; ++e) { SN[e] -= dl_; negm[e] = -mref; } PIN(negm); } SBAR(); } while (0)
#define A_RESC() do { if (__builtin_expect(pend, 0)) { pend = false;         \
            _Pragma("unroll") for (int d = 0; d < 4; ++d) _Pragma("unroll") for (int e = 0; e < 16; ++e) o[d][e] *= resc; } } while (0)
#define A_P2B(SN, KN, D0, D1) do { \
            A_RD(0, (KN));        SBAR(); o[0] = MFMA32(fr[4], PWF(pw1), o[0]); SN[0] = EX2(SN[0]); SN[1] = EX2(SN[1]); PIN(SN); D0; SBAR(); \
            A_RD(1, (KN) + 1024); SBAR(); o[1] = MFMA32(fr[5], PWF(pw1), o[1]); SN[2] = EX2(SN[2]); SN[3] = EX2(SN[3]); PIN(SN); SBAR(); \
            A_RD(2, (KN) + 2048); SBAR(); o[2] = MFMA32(fr[6], PWF(pw1), o[2]); SN[4] = EX2(SN[4]); SN[5] = EX2(SN[5]); l0 += SN[0]; l1 += SN[1]; PIN(SN); PIN(l0); PIN(l1); D1; SBAR(); \
            A_RD(3, (KN) + 3072); SBAR(); o[3] = MFMA32(fr[7], PWF(pw1), o[3]); SN[6] = EX2(SN[6]); SN[7] = EX2(SN[7]); l0 += SN[2]; l1 += SN[3]; PIN(SN); PIN(l0); PIN(l1); SBAR(); \
            A_RESC(); } while (0)
#pragma unroll 1
            for (int n = 0; n < ib; ++n) {
                A_P1(sA, sB, pc + 8192, pc + 32768, D_VE(0), D_VE(1), D_KE(0));
                A_P2A(sA, sB, pc + 32768, false, 0);
                A_P2B(sB, pn, D_KE(1), D_VO(0));
                A_P1(sB, sA, pn, pc + 32768 + 8192, D_VO(1), NOP_, NOP_);
                A_P2A(sB, sA, pc + 32768 + 8192, (n == ib - 1), qrow - 128 * ib);
                const int ev = blkno == 0 ? n : 4;
                if (ev != 0) WAIT_V(0);
                asm volatile("s_waitcnt lgkmcnt(0)\n\ts_barrier" ::: "memory");
                A_ADV();
                if (ev != 0) PUBLISH();
                if (ev < 4) {
                    if (ev == 1 && wid == 0) dma_words(lane * 4, ws + (size_t)(fatt + 16) * 8, lds0 + 131072);
                    if (ev == 2 && wid == 0) check_parked(lds + 131072, ws, fatt + 16, tag, lane);
                    if (ev == 3) { const char* q2src = (const char*)ws + WS_QF + (size_t)(head * 64 + 4 * i2) * 8192 + wid * 4096;
                        dma_pair<131072, 131072 + 1024>(lane * 16, lane * 16 + 1024, q2src, lds0 + wid * 4096); dma_pair<131072 + 2048, 131072 + 3072>(lane * 16 + 2048, lane * 16 + 3072, q2src, lds0 + wid * 4096); }
                }
                A_P2B(sA, pn + 8192, D_KO(0), D_KO(1));
                so ^= 16384;
                const lds_cp t_ = pc; pc = pn; pn = t_;
            }
            if (blkno == 0) PROF(6);
            A_P1(sA, sB, pc + 8192, pc + 32768, D_VE(0); D_VE(1), D_KE(0); D_KE(1), D_VO(0); D_VO(1));
            if (blkno == 0) {
#pragma unroll
                for (int d = 0; d < 8; ++d) q[d] = ld128(qrd + d * 1024);
            }
            A_P2A(sA, sB, pc + 32768, true, qrow - 128 * ib - 32);
            {
                o[0] = MFMA32(fr[4], PWF(pw1), o[0]); sB[0] = EX2(sB[0]); sB[1] = EX2(sB[1]); PIN(sB); SBAR();
                o[1] = MFMA32(fr[5], PWF(pw1), o[1]); sB[2] = EX2(sB[2]); sB[3] = EX2(sB[3]); PIN(sB); SBAR();
                o[2] = MFMA32(fr[6], PWF(pw1), o[2]); sB[4] = EX2(sB[4]); sB[5] = EX2(sB[5]); l0 += sB[0]; l1 += sB[1]; PIN(sB); SBAR();
                o[3] = MFMA32(fr[7], PWF(pw1), o[3]); sB[6] = EX2(sB[6]); sB[7] = EX2(sB[7]); l0 += sB[2]; l1 += sB[3]; PIN(sB); SBAR();
                A_RESC();
            }
            {
                const lds_cp vp = pc + 32768 + 8192;
#pragma unroll
                for (int j = 0; j < 4; ++j) { fr[j] = ld128(vp + j * 2048); fr[4 + j] = ld128(vp + j * 2048 + 1024); }
#pragma unroll
                for (int e = 8; e < 16; ++e) sB[e] = EX2(sB[e]);
#pragma unroll
                for (int e = 4; e < 16; e += 2) { l0 += sB[e]; l1 += sB[e + 1]; }
#pragma unroll
                for (int e = 0; e < 4; ++e) { pw0[e] = cvtpk(sB[2 * e], sB[2 * e + 1]); pw1[e] = cvtpk(sB[8 + 2 * e], sB[9 + 2 * e]); }
                asm volatile("s_waitcnt vmcnt(0) lgkmcnt(0)\n\ts_barrier" ::: "memory");
                PUBLISH();
#pragma unroll
                for (int j = 0; j < 4; ++j) o[j] = MFMA32(fr[j], PWF(pw0), o[j]);
#pragma unroll
                for (int j = 0; j < 4; ++j) o[j] = MFMA32(fr[4 + j], PWF(pw1), o[j]);
            }
            {
                const int sb_own = kh == 0 ? 131072 + rg * 8192 : (rg >> 1) * 65536 + 32768 + so + (rg & 1) * 8192;
                const int sb_par = kh == 1 ? 131072 + rg * 8192 : (rg >> 1) * 65536 + 32768 + so + (rg & 1) * 8192;
                const float mown = dead ? -__builtin_inff() : mref, lown = l0 + l1;
                *(LAS f32x2*)(lds + kh * 65536 + so + 8192 + rg * 512 + lane * 8) = (f32x2){mown, lown};
                const f32x16 s0 = kh ? o[0] : o[2], s1 = kh ? o[1] : o[3], k0 = kh ? o[2] : o[0], k1 = kh ? o[3] : o[1];
#pragma unroll
                for (int qd = 0; qd < 4; ++qd) {
                    *(LAS f32x4*)(lds + sb_own + qd * 1024 + lane * 16) = (f32x4){s0[4 * qd], s0[4 * qd + 1], s0[4 * qd + 2], s0[4 * qd + 3]};
                    *(LAS f32x4*)(lds + sb_own + 4096 + qd * 1024 + lane * 16) = (f32x4){s1[4 * qd], s1[4 * qd + 1], s1[4 * qd + 2], s1[4 * qd + 3]};
                }
                asm volatile("s_waitcnt lgkmcnt(0)\n\ts_barrier" ::: "memory");
                const f32x2 ml = *(const LAS f32x2*)(lds + (kh ^ 1) * 65536 + so + 8192 + rg * 512 + lane * 8);
                f32x16 p0, p1;
#pragma unroll
                for (int qd = 0; qd < 4; ++qd) {
                    const f32x4 a = *(const LAS f32x4*)(lds + sb_par + qd * 1024 + lane * 16), b = *(const LAS f32x4*)(lds + sb_par + 4096 + qd * 1024 + lane * 16);
#pragma unroll
                    for (int e = 0; e < 4; ++e) { p0[4 * qd + e] = a[e]; p1[4 * qd + e] = b[e]; }
                }
                const float mf = __builtin_fmaxf(mown, ml[0]);
                const float fo = EX2(mown - mf), fp = EX2(ml[0] - mf);
                const float inv = 1.0f / swap_add(fo * lown + fp * ml[1]);
                const float ca = fo * inv, cb = fp * inv;
#pragma unroll
                for (int e = 0; e < 16; ++e) { p0[e] = ca * k0[e] + cb * p0[e]; p1[e] = ca * k1[e] + cb * p1[e]; }
                const unsigned dst = WS_AF + ((head * 8 + 4 * kh) * 64 + 4 * ib + rg) * 1024 + lane * 16;
                st16(rws, dst, pack8(p0, 0, 1.0f)); st16(rws, dst + 65536, pack8(p0, 1, 1.0f));
                st16(rws, dst + 2 * 65536, pack8(p1, 0, 1.0f)); st16(rws, dst + 3 * 65536, pack8(p1, 1, 1.0f));
                pf = F_C + ib * 32 + head;
            }
            so ^= 16384;
            if (blkno == 0) PROF(7);
        }
        PROF(8);
        asm volatile("s_waitcnt vmcnt(0) lgkmcnt(0)\n\ts_barrier" ::: "memory");
        PUBLISH();
#undef A_RD
#undef A_P1
#undef A_P2A
#undef A_P2B
#undef PWF
    }

    {
        const int tm = 4 * x8 + (kk >> 3), tn = kk & 7;
        const rsrc_t ra = __builtin_amdgcn_make_buffer_rsrc((void*)(ws + WS_AF), (short)0, NKS * 65536, 0x00020000);
        const rsrc_t rb = __builtin_amdgcn_make_buffer_rsrc((void*)(ws + WS_WOF), (short)0, NKS * 16384, 0x00020000);
        if (wid == 0) {
            if (bid == 0) {
#pragma unroll 1
                for (int i = 0; i < 4; ++i) poll_flags(ws, F_S + i * 64 + lane, true, tag);
                if (lane == 0) set_flag(ws, F_E, tag);
            }
            poll_flags(ws, lane < 32 ? F_C + (tm >> 1) * 32 + lane : lane < 40 ? F_U + (tm >> 2) * 8 + (lane & 7) : F_W + (lane & 15), lane < 38 || lane >= 48, tag);
        }
        BAR();
        PROF(9);
        const int va = wid * 65536 + tm * 2048 + lane * 16, vb = wid * 16384 + tn * 2048 + lane * 16;
        constexpr int DEP = 4;
        u32x4 fa[DEP][2], fb[DEP][2];
        f32x16 acc[2][2] = {};
#define K3_LD(slot, it) do { fa[slot][0] = __builtin_amdgcn_raw_buffer_load_b128(ra, va, (it) * 524288, SC1); fa[slot][1] = __builtin_amdgcn_raw_buffer_load_b128(ra, va + 1024, (it) * 524288, SC1); \
            fb[slot][0] = __builtin_amdgcn_raw_buffer_load_b128(rb, vb, (it) * 131072, SC1); fb[slot][1] = __builtin_amdgcn_raw_buffer_load_b128(rb, vb + 1024, (it) * 131072, SC1); } while (0)
#pragma unroll
        for (int s = 0; s < DEP; ++s) K3_LD(s, s);
#pragma unroll 1
        for (int it = 0; it < 40; it += DEP) {
#pragma unroll
            for (int s = 0; s < DEP; ++s) {
                const bf16x8 a0 = __builtin_bit_cast(bf16x8, fa[s][0]), a1 = __builtin_bit_cast(bf16x8, fa[s][1]), b0 = __builtin_bit_cast(bf16x8, fb[s][0]), b1 = __builtin_bit_cast(bf16x8, fb[s][1]);
                acc[0][0] = MFMA32(b0, a0, acc[0][0]); acc[0][1] = MFMA32(b1, a0, acc[0][1]);
                acc[1][0] = MFMA32(b0, a1, acc[1][0]); acc[1][1] = MFMA32(b1, a1, acc[1][1]);
                SBAR();
                K3_LD(s, it + s + DEP);
                SBAR();
            }
        }
#undef K3_LD
        PROF(10);
#pragma unroll
        for (int b = 0; b < 4; ++b)
#pragma unroll
            for (int g = 0; g < 4; ++g) { const f32x16& a = acc[b >> 1][b & 1];
                *(LAS f32x4*)(lds + wid * 16384 + b * 4096 + g * 1024 + ((lane ^ (4 * g)) << 4)) = (f32x4){a[4 * g], a[4 * g + 1], a[4 * g + 2], a[4 * g + 3]}; }
        const int row = tid >> 3, c8 = tid & 7;
        const u32x4 xv = *(const u32x4*)(x + (size_t)(tm * 64 + row) * DM + tn * 64 + c8 * 8);
        WAIT_L(0); BAR();
        const lds_cp rd = lds + ((row >> 5) * 2 + (c8 >> 2)) * 4096 + (c8 & 3) * 1024 + (((row & 31) ^ (4 * (c8 & 3))) << 4);
        f32x4 lo = {0.f, 0.f, 0.f, 0.f}, hi = {0.f, 0.f, 0.f, 0.f};
#pragma unroll
        for (int w = 0; w < 8; ++w) { lo += *(const LAS f32x4*)(rd + w * 16384); hi += *(const LAS f32x4*)(rd + w * 16384 + 512); }
        u32x4 o;
        o[0] = cvtpk(bflo(xv[0]) + lo[0], bfhi(xv[0]) + lo[1]); o[1] = cvtpk(bflo(xv[1]) + lo[2], bfhi(xv[1]) + lo[3]);
        o[2] = cvtpk(bflo(xv[2]) + hi[0], bfhi(xv[2]) + hi[1]); o[3] = cvtpk(bflo(xv[3]) + hi[2], bfhi(xv[3]) + hi[3]);
        *(u32x4*)(out + (size_t)(tm * 64 + row) * DM + tn * 64 + c8 * 8) = o;
    }
    PROF_END(ws);
}

extern "C" void kernel_launch(void* const* d_in, const int* in_sizes, int n_in, void* d_out, int out_size, void* d_ws, size_t ws_size, hipStream_t stream) {
    (void)in_sizes; (void)n_in; (void)out_size; (void)ws_size;
    fused_fwd<<<256, 512, 0, stream>>>((const bf16_t*)d_in[0], (const bf16_t*)d_in[1], (const bf16_t*)d_in[2], (const bf16_t*)d_in[3], (const bf16_t*)d_in[4], (const bf16_t*)d_in[5],
                                       (const bf16_t*)d_in[7], (const bf16_t*)d_in[8], (const bf16_t*)d_in[9], (const bf16_t*)d_in[10], (bf16_t*)d_out, (unsigned char*)d_ws);
}
```

```cpp
#include <hip/hip_runtime.h>
#include <hip/hip_bf16.h>
#include <stdint.h>

#define LAS __attribute__((address_space(3)))
#define DI __device__ __forceinline__
typedef unsigned short bf16_t;
typedef unsigned long long u64;
typedef __attribute__((address_space(1))) u64 gu64;
typedef short bf16x8 __attribute__((ext_vector_type(8)));
typedef short s16x4 __attribute__((ext_vector_type(4)));
typedef __bf16 bf16x2_t __attribute__((ext_vector_type(2)));
typedef float f32x16 __attribute__((ext_vector_type(16)));
typedef float f32x4 __attribute__((ext_vector_type(4)));
typedef float f32x2 __attribute__((ext_vector_type(2)));
typedef unsigned u32x4 __attribute__((ext_vector_type(4)));
typedef unsigned u32x2 __attribute__((ext_vector_type(2)));
typedef LAS unsigned char* lds_p;
typedef const LAS unsigned char* lds_cp;
typedef __amdgpu_buffer_rsrc_t rsrc_t;

constexpr int SEQ = 2048, DM = 512, NH = 32, FH = 688, FF = 1376;
constexpr int NKS = 299;
constexpr int F_E = 0;
constexpr int F_S = 512;
constexpr int F_X = 1024;
constexpr int F_L = 2048;
constexpr int F_W = 2560;
constexpr int F_U = 3072;
constexpr int F_K = 3584;
constexpr int F_C = 4096;
constexpr unsigned WS_G   = 65536;
constexpr unsigned WS_XN  = 131072;
constexpr unsigned WS_QF  = WS_XN + SEQ * DM * 2;
constexpr unsigned WS_KVF = WS_QF + NH * 64 * 8192;
constexpr unsigned WS_Z2N = WS_KVF + NH * 64 * 16384;
constexpr unsigned WS_AF  = WS_Z2N + SEQ * 768 * 2;
constexpr unsigned WS_WOF = WS_AF + 304 * 64 * 1024;
constexpr unsigned WS_END = WS_WOF + 304 * 16 * 1024;

#define PROF_DECL
#define PROF(i)
#define PROF_END(wsp)
#define MFMA32(a, b, c) __builtin_amdgcn_mfma_f32_32x32x16_bf16((a), (b), (c), 0, 0, 0)
#define SBAR() __builtin_amdgcn_sched_barrier(0)
#define PIN(x) asm volatile("" : "+v"(x))
#define WAIT_V(n) asm volatile("s_waitcnt vmcnt(" #n ")" ::: "memory")
#define WAIT_L(n) asm volatile("s_waitcnt lgkmcnt(" #n ")" ::: "memory")
#define BAR() asm volatile("s_barrier" ::: "memory")
#define SC1 16

DI unsigned cvtpk(float lo, float hi) { f32x2 v = {lo, hi}; bf16x2_t b = __builtin_convertvector(v, bf16x2_t); return __builtin_bit_cast(unsigned, b); }
DI float bflo(unsigned w) { return __uint_as_float(w << 16); }
DI float bfhi(unsigned w) { return __uint_as_float(w & 0xffff0000u); }
DI u32x4 pack8(const f32x16& a, int s, float sc) {
    u32x4 w;
    w[0] = cvtpk(a[8 * s + 0] * sc, a[8 * s + 1] * sc); w[1] = cvtpk(a[8 * s + 2] * sc, a[8 * s + 3] * sc);
    w[2] = cvtpk(a[8 * s + 4] * sc, a[8 * s + 5] * sc); w[3] = cvtpk(a[8 * s + 6] * sc, a[8 * s + 7] * sc);
    return w;
}
DI bf16x8 ld128(lds_cp p) { return *(const LAS bf16x8*)p; }
typedef short v4i16_t __attribute__((ext_vector_type(4)));
DI s16x4 ldtr(lds_cp p) { return __builtin_bit_cast(s16x4, __builtin_amdgcn_ds_read_tr16_b64_v4i16((LAS v4i16_t*)p)); }
DI bf16x8 ldtr2(lds_cp p, int step) { const s16x4 lo = ldtr(p), hi = ldtr(p + step); return __builtin_shufflevector(lo, hi, 0, 1, 2, 3, 4, 5, 6, 7); }
template <int I0, int I1> DI void dma_pair(unsigned v0, unsigned v1, const void* sbase, unsigned l) {
    asm volatile("s_add_u32 m0, %3, %4\n\ts_nop 0\n\tglobal_load_lds_dwordx4 %0, %2 sc1\n\ts_add_u32 m0, %3, %5\n\ts_nop 0\n\tglobal_load_lds_dwordx4 %1, %2 sc1"
                 :: "v"(v0), "v"(v1), "s"(sbase), "s"(l), "i"(I0), "i"(I1) : "memory", "scc");
}
template <int I, int O> DI void bdma1(unsigned voff, rsrc_t rs, unsigned soff, unsigned l) {
    asm volatile("s_add_u32 m0, %3, %4\n\ts_nop 0\n\tbuffer_load_dwordx4 %0, %1, %2 offen offset:%5 sc1 lds" :: "v"(voff), "s"(rs), "s"(soff), "s"(l), "i"(I), "i"(O) : "memory", "scc");
}
DI void dma_words(unsigned voff, const void* sbase, unsigned l) {
    asm volatile("s_mov_b32 m0, %2\n\ts_nop 0\n\tglobal_load_lds_dword %0, %1 sc1" :: "v"(voff), "s"(sbase), "s"(l) : "memory");
}
DI void st16(rsrc_t rs, unsigned off, u32x4 v) { __builtin_amdgcn_raw_buffer_store_b128(v, rs, off, 0, SC1); }
DI u32x4 ld16(rsrc_t rs, unsigned off) { return __builtin_amdgcn_raw_buffer_load_b128(rs, off, 0, SC1); }
DI float swap_add(float v) { auto r = __builtin_amdgcn_permlane32_swap(__float_as_uint(v), __float_as_uint(v), false, false); return __uint_as_float(r[0]) + __uint_as_float(r[1]); }
DI float swap_max(float v) { auto r = __builtin_amdgcn_permlane32_swap(__float_as_uint(v), __float_as_uint(v), false, false); return __builtin_fmaxf(__uint_as_float(r[0]), __uint_as_float(r[1])); }
DI f32x2 unpk(unsigned w) { return (f32x2){bflo(w), bfhi(w)}; }
DI unsigned cvt2(f32x2 v) { return __builtin_bit_cast(unsigned, __builtin_convertvector(v, bf16x2_t)); }
DI float sum_pair(unsigned w, float acc) { return __builtin_amdgcn_fdot2_f32_bf16(__builtin_bit_cast(bf16x2_t, w), __builtin_bit_cast(bf16x2_t, 0x3f803f80u), acc, false); }
DI unsigned gelu2(float a, float b) {
    const f32x2 v = {a, b};
    const f32x2 t = v * __builtin_elementwise_fma(v * v, (f32x2){-2.302208198f * 0.044715f, -2.302208198f * 0.044715f}, (f32x2){-2.302208198f, -2.302208198f});
    const f32x2 d = (f32x2){__builtin_amdgcn_exp2f(t[0]), __builtin_amdgcn_exp2f(t[1])} + (f32x2){1.0f, 1.0f};
    return cvt2(v * (f32x2){__builtin_amdgcn_rcpf(d[0]), __builtin_amdgcn_rcpf(d[1])});
}
DI u32x4 gelu8(const f32x16& a, int s) { return (u32x4){gelu2(a[8 * s], a[8 * s + 1]), gelu2(a[8 * s + 2], a[8 * s + 3]), gelu2(a[8 * s + 4], a[8 * s + 5]), gelu2(a[8 * s + 6], a[8 * s + 7])}; }
DI void set_flag(unsigned char* ws, int idx, u64 tag) { __hip_atomic_store((gu64*)ws + idx, tag, __ATOMIC_RELAXED, __HIP_MEMORY_SCOPE_AGENT); }
DI void poll_flags(unsigned char* ws, int idx, bool active, u64 tag) {
#pragma unroll 1
    for (unsigned spins = 0; spins < (1u << 17); ++spins) {
        const u64 v = __hip_atomic_load((gu64*)ws + idx, __ATOMIC_RELAXED, __HIP_MEMORY_SCOPE_AGENT);
        if (__all(!active || v == tag)) break;
        __builtin_amdgcn_s_sleep(4);
    }
}
#define ACQUIRE() asm volatile("buffer_inv sc1" ::: "memory")

constexpr int LDS_TAG = 163584, LDS_TOUCH = 163328, LDS_PARK = 163072;
DI void touch(unsigned voff, const void* sbase, unsigned lds0) { dma_words(voff, sbase, lds0 + LDS_TOUCH); }
#define TOUCH_A(v, s, l) touch(v, s, l)
#define TOUCH_S(v, s, l) touch(v, s, l)
#define TOUCH_Z(v, s, l) touch(v, s, l)
template <int I> DI void dma_one(unsigned v0, const void* sbase, unsigned l) {
    asm volatile("s_add_u32 m0, %2, %3\n\ts_nop 0\n\tglobal_load_lds_dwordx4 %0, %1 sc1" :: "v"(v0), "s"(sbase), "s"(l), "i"(I) : "memory", "scc");
}
DI void z2_half(const lds_p lds, const unsigned lds0, f32x16 (&acc)[3], const bf16_t* __restrict__ x, const bf16_t* __restrict__ w_in, const unsigned char* fx, const int rg, const int hf,
                const int tid, const int wid, const int lane
#define GP(i)
                ) {
    constexpr int ZS = 704, ZST = 32 * ZS;
    constexpr unsigned WROW = FF * 2;
    const int row = tid >> 3, seg = tid & 7;
    const u32x4* src = (const u32x4*)(x + (size_t)(rg * 64 + row) * DM) + seg;
    u32x4 v[8];
#pragma unroll
    for (int j = 0; j < 8; ++j) asm volatile("global_load_dwordx4 %0, %1, off offset:%2" : "=v"(v[j]) : "v"(src), "i"(j * 128) : "memory");
    unsigned bv[3];
    const int p2 = wid < 6 ? wid + 16 : wid + 8;
#pragma unroll
    for (int i = 0; i < 3; ++i) { const int c = (i == 2 ? p2 : wid + 8 * i) * 64 + lane, zr = c / 44, zc = c - zr * 44; bv[i] = zr * WROW + (zc > 42 ? 42 : zc) * 16; }
    const unsigned lw = lds0 + 65536 + wid * 1024, lw2 = lds0 + 65536 + p2 * 1024;
    const char* wg = (const char*)w_in + FH * 2 + hf * FH;
#define Z_STAGE(slot, g_) do { dma_pair<(slot) * ZST, (slot) * ZST + 8192>(bv[0], bv[1], (g_), lw); dma_one<(slot) * ZST>(bv[2], (g_), lw2); } while (0)
    BAR();
    Z_STAGE(0, wg); Z_STAGE(1, wg + 32 * WROW); Z_STAGE(2, wg + 64 * WROW); Z_STAGE(3, wg + 96 * WROW);
#pragma unroll
    for (int i = 0; i < 2; ++i) { int c = (2 * wid + i) * 64 + lane; c = c > 895 ? 895 : c; const int zr = c / 7, zc = c - zr * 7; TOUCH_Z(((rg & 3) * 128 + zr) * WROW + (zc == 6 ? 684 : zc * 128), wg, lds0); }
    SBAR();
    GP(5);
    {
        asm volatile("s_waitcnt vmcnt(14)" : "+v"(v[0]), "+v"(v[1]), "+v"(v[2]), "+v"(v[3]), "+v"(v[4]), "+v"(v[5]), "+v"(v[6]), "+v"(v[7]));
        GP(6);
        float s = 0.f, s1 = 0.f;
#pragma unroll
        for (int j = 0; j < 8; ++j) { s = sum_pair(v[j][0], s); s1 = sum_pair(v[j][1], s1); s = sum_pair(v[j][2], s); s1 = sum_pair(v[j][3], s1); }
        s += s1;
        s += __shfl_xor(s, 1); s += __shfl_xor(s, 2); s += __shfl_xor(s, 4);
        const float mean = s * (1.0f / DM);
        f32x2 d[32], q2 = {0.f, 0.f}, q3 = {0.f, 0.f};
#pragma unroll
        for (int i = 0; i < 32; i += 2) { d[i] = unpk(v[i >> 2][i & 3]) - (f32x2){mean, mean}; d[i + 1] = unpk(v[i >> 2][(i & 3) + 1]) - (f32x2){mean, mean};
            q2 = __builtin_elementwise_fma(d[i], d[i], q2); q3 = __builtin_elementwise_fma(d[i + 1], d[i + 1], q3); }
        q2 += q3;
        float q = q2[0] + q2[1];
        q += __shfl_xor(q, 1); q += __shfl_xor(q, 2); q += __shfl_xor(q, 4);
        const float rstd = 1.0f / sqrtf(q * (1.0f / DM) + 1e-5f);
#pragma unroll
        for (int j = 0; j < 8; ++j) {
            u32x4 o;
#pragma unroll
            for (int e = 0; e < 4; ++e) o[e] = cvt2(d[4 * j + e] * (f32x2){rstd, rstd});
            *(LAS u32x4*)(lds + j * 8192 + row * 128 + ((seg ^ ((row >> 1) & 7)) << 4)) = o;
        }
    }
    const int r = lane & 31, hh = lane >> 5, blk = (lane >> 4) & 1, qq = (lane & 15) >> 2, p = lane & 3;
    const int asw = (r >> 1) & 7, oth = (wid & 1) ? -4096 : 4096;
    lds_cp ab[4];
#pragma unroll
    for (int k = 0; k < 4; ++k) ab[k] = lds + (wid & 1) * 4096 + r * 128 + (((2 * k + hh) ^ asw) << 4);
    const lds_cp bb = lds + 65536 + (8 * hh + qq) * ZS + wid * 64 + blk * 32 + p * 8;
    const lds_cp bx = bb + (8 + (wid >> 1) - wid) * 64;
    const bool w3 = wid < 6;
#pragma unroll
    for (int i = 0; i < 3; ++i) acc[i] = f32x16{};
    bf16x8 fa[2][2][2], fb[2][2][2];
#define Z_READ(set, slot, k0, aoff_) do { _Pragma("unroll") for (int k_ = 0; k_ < 2; ++k_) { \
        fa[set][k_][0] = ld128(ab[(k0) + k_] + (aoff_)); fa[set][k_][1] = ld128(ab[(k0) + k_] + oth + (aoff_)); \
        fb[set][k_][0] = ldtr2(bb + (slot) * ZST + k_ * 16 * ZS, 4 * ZS); if (w3) fb[set][k_][1] = ldtr2(bx + (slot) * ZST + k_ * 16 * ZS, 4 * ZS); } } while (0)
#define Z_MMA(set, k_) do { acc[0] = MFMA32(fb[set][k_][0], fa[set][k_][0], acc[0]); acc[1] = MFMA32(fb[set][k_][0], fa[set][k_][1], acc[1]); \
        if (w3) acc[2] = MFMA32(fb[set][k_][1], fa[set][k_][0], acc[2]); } while (0)
#define Z_SYNC(n) asm volatile("s_waitcnt vmcnt(" #n ") lgkmcnt(0)\n\ts_barrier" ::: "memory")
    GP(7);
    Z_SYNC(11);
    GP(8);
    Z_READ(0, 0, 0, 0);
    const char* g = wg + 128 * WROW;
#pragma unroll 1
    for (int kt = 0; kt < 3; ++kt) {
        Z_SYNC(6); Z_READ(1, 1, 2, 0);     SBAR(); Z_MMA(0, 0); SBAR(); Z_STAGE(0, g);             SBAR(); Z_MMA(0, 1); SBAR();
        Z_SYNC(6); Z_READ(0, 2, 0, 8192);  SBAR(); Z_MMA(1, 0); SBAR(); Z_STAGE(1, g + 32 * WROW); SBAR(); Z_MMA(1, 1); SBAR();
        Z_SYNC(6); Z_READ(1, 3, 2, 8192);  SBAR(); Z_MMA(0, 0); SBAR(); Z_STAGE(2, g + 64 * WROW); SBAR(); Z_MMA(0, 1); SBAR();
        Z_SYNC(6); Z_READ(0, 0, 0, 16384); SBAR(); Z_MMA(1, 0); SBAR(); Z_STAGE(3, g + 96 * WROW); SBAR(); Z_MMA(1, 1); SBAR();
        g += 128 * WROW;
#pragma unroll
        for (int k = 0; k < 4; ++k) ab[k] += 16384;
    }
    if (wid == 0) dma_words(lane * 4, fx, lds0 + LDS_PARK);
    Z_SYNC(6); Z_READ(1, 1, 2, 0);    SBAR(); Z_MMA(0, 0); Z_MMA(0, 1); SBAR();
    Z_SYNC(3); Z_READ(0, 2, 0, 8192); SBAR(); Z_MMA(1, 0); Z_MMA(1, 1); SBAR();
    Z_SYNC(0); Z_READ(1, 3, 2, 8192); SBAR(); Z_MMA(0, 0); Z_MMA(0, 1); SBAR();
    Z_SYNC(0);                                Z_MMA(1, 0); Z_MMA(1, 1);
#undef Z_STAGE
#undef Z_READ
#undef Z_MMA
#undef Z_SYNC
}

struct Run { const char* A; const char* B; unsigned dA1, dA2, dB1, dB2, vA0, vA1, vB0, vB1; };
constexpr int HTB = 16384;
constexpr unsigned LDA = DM * 2, LDB = 4096 * 2;
#define G_SA(b, h) (((b) * 2 + (h)) * HTB)
#define G_SB(b, h) ((4 + (b) * 2 + (h)) * HTB)
#define G_STAGE(bufoff, gb, v0, v1) dma_pair<(bufoff), (bufoff) + 8192>((v0), (v1), (gb), ldsw)
#define G_LDA(dst, b, h) do { _Pragma("unroll") for (int m_ = 0; m_ < 2; ++m_) _Pragma("unroll") for (int k_ = 0; k_ < 4; ++k_) dst[m_][k_] = ld128(lds + G_SA(b, h) + aoff[k_] + m_ * 4096); } while (0)
#define G_LDB(dst, b, h) do { _Pragma("unroll") for (int k_ = 0; k_ < 4; ++k_) dst[k_] = ldtr2(lds + G_SB(b, h) + boff + k_ * 4096, 1024); } while (0)
#define G_MMA(ai, bj, At, Bt) do { __builtin_amdgcn_s_setprio(1); _Pragma("unroll") for (int k_ = 0; k_ < 4; ++k_) _Pragma("unroll") for (int m_ = 0; m_ < 2; ++m_) \
        acc[ai][bj][m_] = SWAP ? MFMA32(At[m_][k_], Bt[k_], acc[ai][bj][m_]) : MFMA32(Bt[k_], At[m_][k_], acc[ai][bj][m_]); __builtin_amdgcn_s_setprio(0); } while (0)

DI void g_prologue_b(const Run& u, const unsigned ldsw) {
    G_STAGE(G_SB(0, 0), u.B, u.vB0, u.vB1); G_STAGE(G_SB(0, 1), u.B + u.dB1, u.vB0, u.vB1);
}
DI void g_prologue_rest(const Run& u, const unsigned ldsw) {
    G_STAGE(G_SA(0, 0), u.A, u.vA0, u.vA1); G_STAGE(G_SA(0, 1), u.A + u.dA1, u.vA0, u.vA1);
    G_STAGE(G_SB(1, 0), u.B + u.dB2, u.vB0, u.vB1); G_STAGE(G_SA(1, 0), u.A + u.dA2, u.vA0, u.vA1); G_STAGE(G_SB(1, 1), u.B + u.dB2 + u.dB1, u.vB0, u.vB1);
}
template <int LO, int HI> DI void check_parked(lds_cp parked, unsigned char* ws, const int fidx, const u64 tag, const int lane) {
    const unsigned w = *(const LAS unsigned*)(parked + lane * 4);
    const int sl = (lane >> 1) & 15;
    if (!__all(sl < LO || sl >= HI || w == ((lane & 1) ? (unsigned)(tag >> 32) : (unsigned)tag))) poll_flags(ws, fidx + (lane & 31), lane < 32 && (lane & 15) >= LO && (lane & 15) < HI, tag);
}

template <bool SWAP, int ROUND>
DI void g_kloop(const lds_p lds, const unsigned lds0, f32x16 (&acc)[2][2][2], const Run& cur, const Run& nxt, const int (&aoff)[4], const int boff, const unsigned ldsw,
                unsigned char* ws, const u64 tag, const int fl0, const int fatt, const int lane, const int wid, const unsigned tch) {
    bf16x8 At[2][4], B0[4], B1[4];
#pragma unroll
    for (int i = 0; i < 8; ++i) acc[i >> 2][(i >> 1) & 1][i & 1] = f32x16{};
#pragma unroll 1
    for (int t = 0; t < 8; t += 2) {
        const bool last = (t == 6);
        const char* a1 = cur.A + (t + 1) * 128 + 128 * LDA;
        const char* a2 = last ? nxt.A : cur.A + (t + 2) * 128;
        const char* b2 = last ? nxt.B : cur.B + (size_t)(t + 2) * 64 * LDB;
        const unsigned dA1 = last ? nxt.dA1 : cur.dA1, dA2 = last ? nxt.dA2 : cur.dA2, dB1 = last ? nxt.dB1 : cur.dB1, dB2 = last ? nxt.dB2 : cur.dB2;
        const unsigned vA0 = last ? nxt.vA0 : cur.vA0, vA1 = last ? nxt.vA1 : cur.vA1, vB0 = last ? nxt.vB0 : cur.vB0, vB1 = last ? nxt.vB1 : cur.vB1;
        if (ROUND == 1 && last && wid == 0) dma_words(lane * 4, ws + (size_t)fatt * 8, lds0 + 131072);
        if (t == 2 && wid == 0 && lane == 0) set_flag(ws, fl0, tag);
        G_LDB(B0, 0, 0); SBAR(); G_LDA(At, 0, 0); if (!(ROUND == 1 && t == 0)) G_STAGE(G_SA(1, 1), a1, cur.vA0, cur.vA1);
        if (ROUND == 0 && t == 2) touch(tch, nxt.A, lds0);
        if (ROUND == 1 && t == 4 && wid == 0) { check_parked<0, 8>(lds + 131072, ws, fatt, tag, lane); ACQUIRE(); }
        WAIT_L(8); BAR(); WAIT_L(0); SBAR(); G_MMA(0, 0, At, B0); SBAR(); BAR();
        G_LDB(B1, 0, 1);
        G_STAGE(G_SB(0, 0), b2, vB0, vB1);
        BAR(); WAIT_L(0); SBAR(); G_MMA(0, 1, At, B1); SBAR(); BAR();
        G_LDA(At, 0, 1); G_STAGE(G_SA(0, 0), a2, vA0, vA1);
        BAR(); WAIT_L(0); SBAR(); G_MMA(1, 0, At, B0); SBAR(); BAR();
        G_STAGE(G_SB(0, 1), b2 + dB1, vB0, vB1);
        if (ROUND == 1 && t == 0) WAIT_V(22); else if ((ROUND == 1 && t == 4 && wid == 0) || (ROUND == 0 && t == 2)) WAIT_V(7); else WAIT_V(6);
        BAR(); SBAR(); G_MMA(1, 1, At, B1); SBAR(); BAR();
        if (ROUND == 1 && wid == 0) {
            if (t == 2) dma_words(lane * 4, ws + (size_t)fatt * 8, lds0 + 131072);
            if (last) check_parked<8, 12>(lds + 131072, ws, fatt, tag, lane);
        }
        G_LDB(B0, 1, 0); SBAR(); G_LDA(At, 1, 0); G_STAGE(G_SA(0, 1), a2 + dA1, vA0, vA1);
        WAIT_L(8); BAR(); WAIT_L(0); SBAR(); G_MMA(0, 0, At, B0); SBAR(); BAR();
        G_LDB(B1, 1, 1); G_STAGE(G_SB(1, 0), b2 + dB2, vB0, vB1);
        BAR(); WAIT_L(0); SBAR(); G_MMA(0, 1, At, B1); SBAR(); BAR();
        G_LDA(At, 1, 1); G_STAGE(G_SA(1, 0), a2 + dA2, vA0, vA1);
        BAR(); WAIT_L(0); SBAR(); G_MMA(1, 0, At, B0); SBAR(); BAR();
        G_STAGE(G_SB(1, 1), b2 + dB2 + dB1, vB0, vB1);
        if (!(ROUND == 1 && last)) WAIT_V(6);
        BAR(); SBAR(); G_MMA(1, 1, At, B1); SBAR();
        if (!(last && wid >= 4)) BAR();
    }
}

DI void g_epilogue(const f32x16 (&acc)[2][2][2], const rsrc_t rws, const int type, const int pn, const int pm, const int wr, const int wc, const int lane) {
    constexpr float QS = 0.08838834764831845f * 1.4426950408889634f;
    const float sc = type == 2 ? QS : 1.0f;
    const unsigned base = (type == 2 ? WS_QF : WS_KVF + (type == 1 ? 8192 : 0)) + wc * 2048 + lane * 16;
    const unsigned hstride = type == 2 ? 8192 : 16384;
#pragma unroll
    for (int ai = 0; ai < 2; ++ai)
#pragma unroll
        for (int bj = 0; bj < 2; ++bj)
#pragma unroll
            for (int m = 0; m < 2; ++m) {
                const unsigned dst = base + (unsigned)((2 * pn + bj) * 64 + pm * 8 + ai * 4 + wr * 2 + m) * hstride;
                st16(rws, dst, pack8(acc[ai][bj][m], 0, sc)); st16(rws, dst + 1024, pack8(acc[ai][bj][m], 1, sc));
            }
}

#define NOP_ (void)0
#define SP_CHECK() if (t == 36) { poll_flags(ws, fatt + (lane & 31), lane < 32 && (lane & 15) < 12, tag); ACQUIRE(); }
#define EX2(v) __builtin_amdgcn_exp2f(v)
#define MX3(a, b, c) __builtin_fmaxf(__builtin_fmaxf((a), (b)), (c))
constexpr float ATT_THR = 32.0f;
DI void att_mask(f32x16& s, int dq) {
#pragma unroll
    for (int e = 0; e < 16; ++e) if ((e & 3) + 8 * (e >> 2) > dq) s[e] = -__builtin_inff();
}

#define IT(h1, i1, h2, i2) ((h1) | (i1) << 2 | (h2) << 6 | (i2) << 8)
__device__ constexpr unsigned ITEMS[32] = {
    IT(0, 15, 0, 2), IT(1, 15, 1, 2), IT(0, 14, 0, 3), IT(1, 14, 1, 3), IT(0, 13, 0, 4), IT(1, 13, 1, 4), IT(0, 12, 0, 5), IT(1, 12, 1, 5),
    IT(0, 11, 0, 6), IT(1, 11, 1, 6), IT(0, 10, 0, 7), IT(1, 10, 1, 7), IT(2, 15, 2, 1), IT(3, 15, 3, 1), IT(2, 14, 2, 2), IT(3, 14, 3, 2),
    IT(2, 13, 2, 3), IT(3, 13, 3, 3), IT(2, 12, 2, 4), IT(3, 12, 3, 4), IT(2, 11, 2, 5), IT(3, 11, 3, 5), IT(2, 10, 2, 6), IT(3, 10, 3, 6),
    IT(0, 8, 0, 1), IT(1, 8, 1, 1), IT(2, 8, 0, 0), IT(3, 8, 1, 0), IT(0, 9, 2, 0), IT(1, 9, 3, 0), IT(2, 9, 2, 7), IT(3, 9, 3, 7)
};

__global__ __launch_bounds__(512, 2) void fused_fwd(const bf16_t* __restrict__ x, const bf16_t* __restrict__ ckv, const bf16_t* __restrict__ wq, const bf16_t* __restrict__ wk,
                                                    const bf16_t* __restrict__ wv, const bf16_t* __restrict__ wo, const bf16_t* __restrict__ w_in, const bf16_t* __restrict__ wsp,
                                                    const bf16_t* __restrict__ bsp, const bf16_t* __restrict__ wout, bf16_t* __restrict__ out, unsigned char* ws) {
    __shared__ __attribute__((aligned(16))) unsigned char smem[163840];
    const lds_p lds = (lds_p)smem;
    const unsigned lds0 = (unsigned)(uintptr_t)smem;
    const int tid = threadIdx.x, wid = __builtin_amdgcn_readfirstlane(tid >> 6), lane = tid & 63;
    const int bid = blockIdx.x, x8 = bid & 7, kk = bid >> 3;
    const int r = lane & 31, hh = lane >> 5, blk = (lane >> 4) & 1, qq = (lane & 15) >> 2, p = lane & 3;
    const rsrc_t rws = __builtin_amdgcn_make_buffer_rsrc((void*)ws, (short)0, WS_END, 0x00020000);
    const unsigned ldsw = lds0 + (unsigned)wid * 1024u;
    PROF_DECL;
    dma_words(lane * 4, ws, lds0 + LDS_TAG);
    u64 tag;
#define MAKE_TAG() do { const u32x2 e_ = *(const LAS u32x2*)(lds + LDS_TAG); \
        const u64 ep_ = ((u64)(unsigned)__builtin_amdgcn_readfirstlane(e_[1]) << 32) | (unsigned)__builtin_amdgcn_readfirstlane(e_[0]); \
        u64 z_ = ep_ + 0x9E3779B97F4A7C15ull; z_ = (z_ ^ (z_ >> 30)) * 0xBF58476D1CE4E5B9ull; z_ = (z_ ^ (z_ >> 27)) * 0x94D049BB133111EBull; z_ ^= z_ >> 31; tag = z_ == ep_ ? ~z_ : z_; } while (0)
    const unsigned item = ITEMS[kk];
    const int h1 = item & 3, i1 = (item >> 2) & 15, h2 = (item >> 6) & 3, i2 = (item >> 8) & 15;
    const unsigned char* kvx = ws + WS_KVF + (size_t)x8 * 4 * 64 * 16384;
    const unsigned char* kvh = kvx + (size_t)h1 * 64 * 16384;
    const char* q1src = (const char*)ws + WS_QF + (size_t)((4 * x8 + h1) * 64 + 4 * i1 + (wid & 3)) * 8192;
    bf16x8 q[8];
#define Q1_LOAD() do { _Pragma("unroll") for (int d = 0; d < 8; ++d) asm volatile("global_load_dwordx4 %0, %1, %2 offset:%3 sc1" : "=v"(q[d]) : "v"(lane * 16 + (d >> 2) * 4096), "s"(q1src), "i"((d & 3) * 1024) : "memory"); } while (0)
#define Q1_WAIT(n) asm volatile("s_waitcnt vmcnt(" #n ")" : "+v"(q[0]), "+v"(q[1]), "+v"(q[2]), "+v"(q[3]), "+v"(q[4]), "+v"(q[5]), "+v"(q[6]), "+v"(q[7]) :: "memory")
    const int fatt = F_K + x8 * 64;
    Run rat;
    rat.A = (const char*)kvh; rat.B = (const char*)kvh + 32768; rat.dA1 = rat.dB1 = 65536; rat.dA2 = rat.dB2 = 8192;
    rat.vA0 = rat.vB0 = wid * 1024 + lane * 16; rat.vA1 = rat.vB1 = rat.vA0 + 16384;
    int pf;
    int aoff[4];
    const unsigned arow = 8 * wid + (lane >> 3), achk = ((lane & 7) ^ (((wid & 1) << 2) + (lane >> 4))) << 4;
    const unsigned brow = 4 * wid + (lane >> 4), bchk = ((lane & 15) ^ (((lane >> 4) & 3) << 2)) << 4;

    if (kk < 24) {
        const int type = kk >> 3, pn = 2 * x8 + ((kk >> 2) & 1), pm0 = (kk & 3) + (type == 2 ? 4 : 0), pm1 = pm0 ^ 4, wr = wid >> 2, wc = wid & 3;
        Run u0, u1;
        const char* Ab = type == 2 ? (const char*)ws + WS_XN : (const char*)ckv;
        u0.B = (const char*)(type == 0 ? wk : type == 1 ? wv : wq) + pn * 512; u0.A = Ab + (size_t)pm0 * 256 * LDA;
        u0.dA1 = 128 * LDA; u0.dA2 = 128; u0.dB1 = 256; u0.dB2 = 64 * LDB;
        u0.vA0 = arow * LDA + achk; u0.vA1 = u0.vA0 + 64 * LDA; u0.vB0 = brow * LDB + bchk; u0.vB1 = u0.vB0 + 32 * LDB;
        u1 = u0; u1.A = Ab + (size_t)pm1 * 256 * LDA;
        const unsigned tch = (type == 2 ? ((kk >> 2) & 1) * 2 : type * 2 + ((kk >> 2) & 1)) * 65536 + tid * 128, bl = ((kk & 3) * 128 + wid * 16 + (lane >> 2)) * LDB + (lane & 3) * 128;
        if (type < 2) {
            g_prologue_b(u0, ldsw); g_prologue_rest(u0, ldsw);
            PROF(1);
            WAIT_V(6);
            MAKE_TAG();
        } else {
            const int j = x8 * 8 + (kk - 16), row = 32 * j + 4 * wid + (lane >> 4);
            const u32x4* src = (const u32x4*)(x + (size_t)row * DM) + (lane & 15);
            u32x4 v[4];
#pragma unroll
            for (int i = 0; i < 4; ++i) v[i] = src[16 * i];
            SBAR();
            g_prologue_b(u0, ldsw); touch(bl, u0.B, lds0);
            SBAR();
            float s = 0.f;
#pragma unroll
            for (int i = 0; i < 4; ++i)
#pragma unroll
                for (int e = 0; e < 4; ++e) s += bflo(v[i][e]) + bfhi(v[i][e]);
            s += __shfl_xor(s, 1); s += __shfl_xor(s, 2); s += __shfl_xor(s, 4); s += __shfl_xor(s, 8);
            const float mean = s * (1.0f / DM);
            float q = 0.f;
#pragma unroll
            for (int i = 0; i < 4; ++i)
#pragma unroll
                for (int e = 0; e < 4; ++e) { const float a = bflo(v[i][e]) - mean, b = bfhi(v[i][e]) - mean; q += a * a + b * b; }
            q += __shfl_xor(q, 1); q += __shfl_xor(q, 2); q += __shfl_xor(q, 4); q += __shfl_xor(q, 8);
            const float rstd = 1.0f / sqrtf(q * (1.0f / DM) + 1e-5f);
#pragma unroll
            for (int i = 0; i < 4; ++i) {
                u32x4 o;
#pragma unroll
                for (int e = 0; e < 4; ++e) o[e] = cvtpk((bflo(v[i][e]) - mean) * rstd, (bfhi(v[i][e]) - mean) * rstd);
                st16(rws, WS_XN + row * 1024 + ((lane & 15) + 16 * i) * 16, o);
            }
            WAIT_V(0);
            MAKE_TAG();
            BAR();
            if (tid == 0) set_flag(ws, F_X + j, tag);
            if (wid == 0) { poll_flags(ws, F_X + ((lane & 8) ? pm1 : pm0) * 8 + (lane & 7), lane < 16, tag); ACQUIRE(); WAIT_V(0); }
            BAR();
            g_prologue_rest(u0, ldsw);
            PROF(1);
            WAIT_V(6);
        }
#pragma unroll
        for (int k = 0; k < 4; ++k) aoff[k] = (wr * 64 + r) * 128 + (((2 * k + hh) ^ ((r >> 1) & 7)) << 4);
        const int boff = (8 * hh + qq) * 256 + ((wc ^ qq) << 6) + blk * 32 + p * 8;
        BAR();
        if (wr == 1) BAR();
        PROF(2);
        f32x16 acc[2][2][2];
        const int fk = fatt + (pn & 1) * 16 + type * 4 + (kk & 3);
        if (type == 1) g_kloop<true, 0>(lds, lds0, acc, u0, u1, aoff, boff, ldsw, ws, tag, F_S + bid, fatt, lane, wid, tch);
        else           g_kloop<false, 0>(lds, lds0, acc, u0, u1, aoff, boff, ldsw, ws, tag, F_S + bid, fatt, lane, wid, tch);
        PROF(3);
        G_STAGE(G_SA(1, 1), u1.A + 128 + 128 * LDA, u1.vA0, u1.vA1);
        SBAR();
        g_epilogue(acc, rws, type, pn, pm0, wr, wc, lane);
        PROF(11);
        if (wr == 1) BAR();
        if (type == 1) g_kloop<true, 1>(lds, lds0, acc, u1, rat, aoff, boff, ldsw, ws, tag, fk, fatt, lane, wid, tch);
        else           g_kloop<false, 1>(lds, lds0, acc, u1, rat, aoff, boff, ldsw, ws, tag, fk, fatt, lane, wid, tch);
        PROF(4);
        Q1_LOAD();
        G_STAGE(G_SA(1, 1), rat.A + rat.dA1 + rat.dA2, rat.vA0, rat.vA1);
        SBAR();
        g_epilogue(acc, rws, type, pn, pm1, wr, wc, lane);
        PROF(12);
        pf = fk + 32;
        Q1_WAIT(18);
    } else {
        const int pm = x8, pn = kk - 24, grp = wid >> 2;
        constexpr unsigned SLA = SEQ * 2, SLB = 768 * 2, WROW = FF * 2;
        constexpr int RB = 65536, RA2 = 131072;
        const char* Ap = (const char*)ws + WS_XN + (size_t)pm * 256 * LDA;
        const char* Bp = (const char*)w_in + pn * 256;
        unsigned vA0 = arow * LDA + achk, vA1 = vA0 + 64 * LDA, vB0 = brow * WROW + bchk, vB1 = vB0 + 32 * WROW, dA = 128 * LDA, dB = 64 * WROW;
        const int tb0 = 8 * pm + 2 * (wid & 3), fs0 = 8 * pn + 4 * grp;
        float bias[2];
        const int flz = F_L + (4 * x8 + (pn >> 1)) * 2 + (kk & 1);
        {
            f32x16 za[3];
            const int rg = 4 * x8 + (pn >> 1), hf = kk & 1, m0_ = wid & 1;
#pragma unroll
            for (int m = 0; m < 2; ++m) bias[m] = __uint_as_float((unsigned)bsp[(tb0 + m) * 32 + r] << 16);
            z2_half(lds, lds0, za, x, w_in, ws + (F_X + 8 * pm) * 8, rg, hf, tid, wid, lane
                );
            PIN(bias[0]); PIN(bias[1]);
            PROF(3);
            MAKE_TAG();
            if (wid == 0 && kk < 30) {
                const unsigned w = *(const LAS unsigned*)(lds + LDS_PARK + lane * 4);
                if (!__all(lane >= 16 || w == ((lane & 1) ? (unsigned)(tag >> 32) : (unsigned)tag))) poll_flags(ws, F_X + 8 * pm + (lane & 7), true, tag);
                ACQUIRE();
            }
            {
                const char* slab = (const char*)wsp + (size_t)x8 * 1048576 + pn * 131072;
                TOUCH_S(tid * 128, slab, lds0); TOUCH_S(tid * 128 + 65536, slab, lds0);
            }
            u32x4 zp[3][2];
#pragma unroll
            for (int i = 0; i < 3; ++i) { zp[i][0] = gelu8(za[i], 0); zp[i][1] = gelu8(za[i], 1); }
            const int nx = wid < 4 ? 8 : wid < 6 ? 6 : 0;
            LAS float* red = (LAS float*)lds;
            float s[2] = {0.f, 0.f}, mean[2];
#pragma unroll
            for (int i = 0; i < 24; ++i) if (i < 16 || i - 16 < nx) s[(i >> 3) & 1] = sum_pair(zp[i >> 3][(i >> 2) & 1][i & 3], s[(i >> 3) & 1]);
            red[wid * 128 + m0_ * 64 + lane] = s[0]; red[wid * 128 + (m0_ ^ 1) * 64 + lane] = s[1];
            WAIT_L(0); BAR();
#pragma unroll
            for (int m = 0; m < 2; ++m) { float t = 0.f;
#pragma unroll
                for (int w = 0; w < 8; ++w) t += red[w * 128 + (m0_ ^ m) * 64 + lane];
                mean[m] = swap_add(t) * (2.0f / FH); }
            f32x2 d[24], q2[2] = {{0.f, 0.f}, {0.f, 0.f}};
#pragma unroll
            for (int i = 0; i < 24; ++i) { const int m = (i >> 3) & 1; d[i] = unpk(zp[i >> 3][(i >> 2) & 1][i & 3]) - (f32x2){mean[m], mean[m]};
                if (i < 16 || i - 16 < nx) q2[m] = __builtin_elementwise_fma(d[i], d[i], q2[m]); }
            red[1024 + wid * 128 + m0_ * 64 + lane] = q2[0][0] + q2[0][1]; red[1024 + wid * 128 + (m0_ ^ 1) * 64 + lane] = q2[1][0] + q2[1][1];
            GP(9);
            WAIT_V(2); WAIT_L(0); BAR();
            LAS f32x2* tab = (LAS f32x2*)(lds + 160768);
            if (wid == 0) {
#pragma unroll
                for (int m = 0; m < 2; ++m) { float t = 0.f;
#pragma unroll
                    for (int w = 0; w < 8; ++w) t += red[1024 + w * 128 + m * 64 + lane];
                    s[m] = swap_add(t); }
                const float mo = hh ? mean[1] : mean[0], qo = hh ? s[1] : s[0];
                const unsigned t32 = (unsigned)tag;
                st16(rws, WS_G + (rg * 2 + hf) * 1024 + lane * 16, (u32x4){__float_as_uint(mo), t32, __float_as_uint(qo), t32});
                u32x4 o;
#pragma unroll 1
                for (unsigned spins = 0; spins < (1u << 17); ++spins) {
                    o = ld16(rws, WS_G + (rg * 2 + (hf ^ 1)) * 1024 + lane * 16);
                    if (__all(o[1] == t32 && o[3] == t32)) break;
                    __builtin_amdgcn_s_sleep(2);
                }
                const float mp = __uint_as_float(o[0]), dl = mo - mp;
                tab[lane] = (f32x2){0.5f * (mo + mp), 1.0f / sqrtf((qo + __uint_as_float(o[2]) + dl * dl * (FH * 0.25f)) * (1.0f / FH) + 1e-5f)};
            }
            WAIT_L(0); BAR();
            GP(10);
            if (kk < 30) {
                dma_pair<0, 8192>(vA0, vA1, Ap, ldsw); dma_pair<16384, 24576>(vA0, vA1, Ap + 128 * LDA, ldsw); dma_pair<RB, RB + 8192>(vB0, vB1, Bp, ldsw);
                dma_pair<32768, 32768 + 8192>(vA0, vA1, Ap + 128, ldsw); dma_pair<32768 + 16384, 32768 + 24576>(vA0, vA1, Ap + 128 + 128 * LDA, ldsw);
                dma_pair<RB + 16384, RB + 16384 + 8192>(vB0, vB1, Bp + 64 * WROW, ldsw); dma_pair<RB + 32768, RB + 32768 + 8192>(vB0, vB1, Bp + 128 * WROW, ldsw);
            }
            f32x2 rs[2], of[2];
#pragma unroll
            for (int m = 0; m < 2; ++m) { const f32x2 t = tab[(m0_ ^ m) * 32 + r]; const float o = (mean[m] - t[0]) * t[1]; rs[m] = (f32x2){t[1], t[1]}; of[m] = (f32x2){o, o}; }
            const lds_p tr = lds + 114688;
#pragma unroll
            for (int i = 0; i < 12; ++i)
                if (i < 8 || 2 * (i - 8) < nx) {
                    const int b = i >> 2, m = b & 1;
                    *(LAS u32x2*)(tr + ((m0_ ^ m) * 32 + r) * 720 + (b == 2 ? 8 + (wid >> 1) : wid) * 64 + (i & 3) * 16 + hh * 8) =
                        (u32x2){cvt2(__builtin_elementwise_fma(d[2 * i], rs[m], of[m])), cvt2(__builtin_elementwise_fma(d[2 * i + 1], rs[m], of[m]))};
                }
            WAIT_L(0); BAR();
#pragma unroll
            for (int i = 0; i < 6; ++i) { int c = tid + 512 * i; const bool ok = c < 2752; c = ok ? c : 0; const int zr = c / 43, zc = c - zr * 43;
                st16(rws, ok ? WS_Z2N + (rg * 64 + zr) * SLB + hf * 688 + zc * 16 : 0x80000000u, *(const LAS u32x4*)(tr + zr * 720 + zc * 16)); }
#pragma unroll
            for (int i = 0; i < 2; ++i) { const int c = tid + 512 * i, zr = c / 10;
                st16(rws, hf == 1 && c < 640 ? WS_Z2N + (rg * 64 + zr) * SLB + 1376 + (c - zr * 10) * 16 : 0x80000000u, (u32x4){0u, 0u, 0u, 0u}); }
        }
        PROF(1);
        if (kk < 30) {
            u32x4 z1[2][2][2];
#pragma unroll
            for (int k = 0; k < 4; ++k) aoff[k] = ((wid >> 1) & 1) * 16384 + ((wid & 1) * 64 + r) * 128 + (((2 * k + hh) ^ ((r >> 1) & 7)) << 4);
            const int bo0 = RB + (8 * hh + qq) * 256 + (((2 * grp) ^ qq) << 6) + blk * 32 + p * 8, bo1 = RB + (8 * hh + qq) * 256 + (((2 * grp + 1) ^ qq) << 6) + blk * 32 + p * 8;
            f32x16 acc[2][2];
#pragma unroll
            for (int c = 0; c < 4; ++c) acc[c >> 1][c & 1] = f32x16{};
#pragma unroll
            for (int c = 0; c < 8; ++c) z1[c >> 2][(c >> 1) & 1][c & 1] = (u32x4){0u, 0u, 0u, 0u};
            Ap += 256; Bp += 192 * WROW;
            unsigned sa = 0, sa1 = 32768, sa2 = RA2;
            unsigned la = ldsw + RA2, la1 = ldsw, la2 = ldsw + 32768, lb = 49152;
            const char* Aq = Ap; unsigned lq = la, lq1 = la1, lq2 = la2, vQ0 = vA0, vQ1 = vA1, dQ = dA;
#define SP_SRC(s_) do { if ((s_) == 6) { Ap = (const char*)wsp + (size_t)pm * 256 * SLA; vA1 = arow * SLA + achk + 64 * SLA; dA = 128 * SLA; } \
                if ((s_) == 5) { Bp = (const char*)ws + WS_Z2N + pn * 256; vB0 = brow * SLB + bchk; vB1 = vB0 + 32 * SLB; dB = 64 * SLB; } \
                if (t == 6) { Aq = (const char*)wsp + (size_t)pm * 256 * SLA; vQ0 = arow * SLA + achk; vQ1 = vQ0 + 64 * SLA; dQ = 128 * SLA; } } while (0)
#define SP_LATE() do { dma_pair<0, 8192>(vQ0, vQ1, Aq, lq); dma_one<16384>(vQ0, Aq + dQ, lq); Aq += 128; const unsigned l_ = lq; lq = lq1; lq1 = lq2; lq2 = l_; } while (0)
#define SP_A1() dma_one<24576>(vA1, Ap + dA, la)
#define SP_B0() dma_one<RB>(vB0, Bp, ldsw + lb)
#define SP_B1() dma_one<RB + 8192>(vB1, Bp, ldsw + lb)
#define SP_ADV() do { Ap += 128; Bp += dB; const unsigned l_ = la; la = la1; la1 = la2; la2 = l_; lb = (lb + 16384) & 49152; } while (0)
#define SP_GELU() do { _Pragma("unroll") for (int c = 0; c < 4; ++c) { f32x16& a = acc[c >> 1][c & 1]; z1[c >> 1][c & 1][0] = gelu8(a, 0); z1[c >> 1][c & 1][1] = gelu8(a, 1); a = f32x16{}; } } while (0)
#define SP_MMA(k) do { _Pragma("unroll") for (int c = 0; c < 4; ++c) acc[c >> 1][c & 1] = MFMA32(Bt[c & 1][k], At[c >> 1][k], acc[c >> 1][c & 1]); } while (0)
#define SP_STEP(PRE, LATE, WAIT0, WAIT1, D0, D1, D2, POST, LASTBAR) do { \
                bf16x8 At[2][4], Bt[2][4]; \
                PRE; \
                const lds_cp pa = lds + sa, pb = lds + (t & 3) * 16384; \
                _Pragma("unroll") for (int k = 0; k < 4; ++k) { Bt[0][k] = ldtr2(pb + bo0 + k * 4096, 1024); At[0][k] = ld128(pa + aoff[k]); At[1][k] = ld128(pa + aoff[k] + 4096); Bt[1][k] = ldtr2(pb + bo1 + k * 4096, 1024); } \
                LATE; \
                if (grp == 1) { WAIT1; } \
                WAIT_L(0); BAR(); SBAR(); \
                __builtin_amdgcn_s_setprio(1); \
                SP_MMA(0); SBAR(); D0; SBAR(); SP_MMA(1); SBAR(); D1; SBAR(); SP_MMA(2); SBAR(); D2; SBAR(); SP_MMA(3); \
                __builtin_amdgcn_s_setprio(0); \
                SBAR(); \
                POST; \
                if (grp == 0) { WAIT0; } \
                if (LASTBAR) BAR(); \
                const unsigned s_ = sa; sa = sa1; sa1 = sa2; sa2 = s_; } while (0)
            asm volatile("s_waitcnt vmcnt(16) lgkmcnt(0)\n\ts_barrier" ::: "memory");
            if (grp == 1) { SP_A1(); SP_B0(); SP_B1(); SP_ADV(); BAR(); }
            PROF(2);
            int t = 0;
#pragma unroll 1
            for (; t < 8; ++t)
                SP_STEP({ if (wid == 0) { if (t == 2 && lane == 0) { set_flag(ws, flz, tag); set_flag(ws, F_S + bid, tag); } if (t == 3) { poll_flags(ws, F_L + lane, true, tag); ACQUIRE(); } } SP_SRC(t + grp); },
                        SP_LATE(), { if (t == 0) WAIT_V(16); else WAIT_V(8); }, { if (t == 0) WAIT_V(16); else WAIT_V(6); }, SP_A1(), SP_B0(), SP_B1(), { SP_ADV(); if (t == 7 && grp == 1) SP_GELU(); }, true);
            PROF(11);
            if (grp == 0) SP_GELU();
            PROF(12);
#pragma unroll 1
            for (; t < 36; ++t) SP_STEP(NOP_, SP_LATE(), WAIT_V(8), WAIT_V(6), SP_A1(), SP_B0(), SP_B1(), SP_ADV(), true);
#pragma unroll 1
            for (; t < 40; ++t)
                SP_STEP({ if (wid == 0) { SP_CHECK(); } }, if (t < 38) SP_LATE(),
                        { if (t == 36) WAIT_V(8); else if (t == 37) WAIT_V(6); else WAIT_V(0); }, { if (t == 36) WAIT_V(6); else if (t == 37) WAIT_V(4); else WAIT_V(0); },
                        if (t + grp < 38) SP_A1(), if (t + grp < 37) SP_B0(), if (t + grp < 37) SP_B1(), SP_ADV(), !(t == 39 && grp == 1));
            PROF(4);
            Q1_LOAD();
            G_STAGE(G_SA(0, 0), rat.A, rat.vA0, rat.vA1); G_STAGE(G_SB(0, 0), rat.B, rat.vA0, rat.vA1); G_STAGE(G_SA(1, 0), rat.A + 8192, rat.vA0, rat.vA1); G_STAGE(G_SB(1, 0), rat.B + 8192, rat.vA0, rat.vA1);
            G_STAGE(G_SA(0, 1), rat.A + 65536, rat.vA0, rat.vA1); G_STAGE(G_SB(0, 1), rat.B + 65536, rat.vA0, rat.vA1); G_STAGE(G_SA(1, 1), rat.A + 73728, rat.vA0, rat.vA1); G_STAGE(G_SB(1, 1), rat.B + 73728, rat.vA0, rat.vA1);
            SBAR();
#pragma unroll
            for (int m = 0; m < 2; ++m)
#pragma unroll
                for (int c = 0; c < 4; ++c) {
                    const f32x16& a = acc[m][c >> 1]; const u32x4 z = z1[m][c >> 1][c & 1]; const int s8 = 8 * (c & 1);
                    u32x4 o;
#pragma unroll
                    for (int e = 0; e < 4; ++e) o[e] = cvtpk(bflo(z[e]) * (a[s8 + 2 * e] + bias[m]), bfhi(z[e]) * (a[s8 + 2 * e + 1] + bias[m]));
                    st16(rws, fs0 + c < 43 ? WS_AF + ((256 + fs0 + c) * 64 + tb0 + m) * 1024 + lane * 16 : 0x80000000u, o);
                }
            pf = F_U + pm * 8 + pn;
            Q1_WAIT(16);
        } else {
            WAIT_V(0); WAIT_L(0); BAR();
            if (tid == 0) { set_flag(ws, flz, tag); set_flag(ws, F_S + bid, tag); }
            const int aw = x8 * 2 + (kk - 30);
            const lds_cp rd = lds + wid * 16384 + (4 * hh + qq) * 1024 + blk * 32 + p * 8;
#define WOF_LOAD(ks_) do { const char* src = (ks_) < 256 ? (const char*)wo + (size_t)(ks_) * 16 * 1024 : (const char*)wout + (size_t)((ks_) - 256) * 16 * 1024; \
                _Pragma("unroll") for (int i = 0; i < 8; ++i) dma_pair<0, 1024>(lane * 16 + i * 2048, lane * 16 + i * 2048 + 1024, src, lds0 + wid * 16384 + i * 2048); } while (0)
            WOF_LOAD(aw * 8 + wid);
            WAIT_V(0);
#pragma unroll 1
            for (int ks = aw * 8 + wid; ks < NKS; ks += 128) {
                bf16x8 f[16];
#pragma unroll
                for (int nb = 0; nb < 16; ++nb) f[nb] = ldtr2(rd + nb * 64, 8192);
#pragma unroll
                for (int nb = 0; nb < 16; ++nb) PIN(f[nb]);
                WAIT_L(0);
                if (ks + 128 < NKS) WOF_LOAD(ks + 128);
#pragma unroll
                for (int nb = 0; nb < 16; ++nb) st16(rws, WS_WOF + ks * 16384 + nb * 1024 + lane * 16, __builtin_bit_cast(u32x4, f[nb]));
                WAIT_V(16);
            }
            if (wid == 0) { poll_flags(ws, fatt + (lane & 31), lane < 32 && (lane & 15) < 12, tag); ACQUIRE(); }
            WAIT_V(0); BAR();
            if (tid == 0) set_flag(ws, F_W + aw, tag);
            PROF(4);
            g_prologue_b(rat, ldsw); g_prologue_rest(rat, ldsw); G_STAGE(G_SA(1, 1), rat.A + rat.dA1 + rat.dA2, rat.vA0, rat.vA1);
            Q1_LOAD();
            pf = -1;
            Q1_WAIT(0);
        }
    }
    BAR();
    PROF(5);
    {
        const int kh = wid >> 2, rg = wid & 3;
        const rsrc_t rkv = __builtin_amdgcn_make_buffer_rsrc((void*)kvx, (short)0, 4 * 64 * 16384, 0x00020000);
        const unsigned dv0 = rg * 2048 + lane * 16 + kh * 32768, dv1 = dv0 + 8192, dv2 = dv0 + 16384, dv3 = dv0 + 24576;
        const unsigned wrapoff = (unsigned)(h1 * 16 + i1 + 1) * 65536u, wrapto = (unsigned)h2 << 20;
        unsigned sA_ = 0, sB_ = ((unsigned)h1 << 20) + 65536u, lA = lds0 + kh * 65536 + rg * 2048, lB = lA + 16384;
#define A_ADV() do { sA_ = sB_; sB_ += 65536u; if (sB_ == wrapoff) sB_ = wrapto; const unsigned t_ = lA; lA = lB; lB = t_; } while (0)
#define D_KO(h) bdma1<8192, (h) * 1024>(dv2, rkv, sA_, lA)
#define D_VE(h) bdma1<32768, (h) * 1024>(dv1, rkv, sA_, lA)
#define D_KE(h) bdma1<0, (h) * 1024>(dv0, rkv, sB_, lB)
#define D_VO(h) bdma1<40960, (h) * 1024>(dv3, rkv, sA_, lA)
#define PUBLISH() do { if (pf >= 0) { if (tid == 0) set_flag(ws, pf, tag); pf = -1; } } while (0)
        const lds_cp qrd = lds + 131072 + rg * 8192 + lane * 16;
        const lds_cp rbase = lds + kh * 65536 + lane * 16;
        int so = 0;
#pragma unroll 1
        for (int blkno = 0; blkno < 2; ++blkno) {
            const int ib = blkno ? i2 : i1, head = 4 * x8 + (blkno ? h2 : h1);
            const int qrow = 128 * ib + 32 * rg + r - 4 * hh - 64 * kh;
            f32x16 o[4], sA, sB, negm;
            bf16x8 fr[8];
            u32x4 pw0, pw1;
            float mref, l0 = 0.f, l1 = 0.f, resc = 1.0f;
            bool dead = false, pend = false;
            lds_cp pc = rbase + so, pn = rbase + (so ^ 16384);
#pragma unroll
            for (int d = 0; d < 8; ++d) fr[d] = ld128(pc + d * 1024);
            sA = f32x16{};
#pragma unroll
            for (int d = 0; d < 8; ++d) sA = MFMA32(fr[d], q[d], sA);
            asm volatile("s_waitcnt lgkmcnt(0)\n\ts_barrier" ::: "memory");
            A_ADV();
            D_KO(0); D_KO(1);
#pragma unroll
            for (int d = 0; d < 4; ++d) fr[d] = ld128(pc + 8192 + d * 1024);
#pragma unroll
            for (int d = 0; d < 4; ++d) o[d] = f32x16{};
            if (ib == 0) att_mask(sA, qrow);
            {
                float m = MX3(sA[0], sA[1], sA[2]);
#pragma unroll
                for (int e = 3; e < 15; e += 2) m = MX3(m, sA[e], sA[e + 1]);
                m = swap_max(__builtin_fmaxf(m, sA[15]));
                dead = m == -__builtin_inff();
                mref = dead ? 0.f : m;
#pragma unroll
                for (int e = 0; e < 16; ++e) { sA[e] -= mref; negm[e] = -mref; }
                PIN(negm);
#pragma unroll
                for (int e = 0; e < 8; ++e) sA[e] = EX2(sA[e]);
                l0 = sA[0] + sA[2]; l1 = sA[1] + sA[3];
            }
#define A_RD(j, ptr) do { fr[j] = ld128(ptr); } while (0)
#define A_P1(SC, SN, KP, VP, E0, E1, E2) do { \
            A_RD(4, (KP) + 4096); SBAR(); SN = MFMA32(fr[0], q[0], negm); SC[8] = EX2(SC[8]); SC[9] = EX2(SC[9]); pw0[0] = cvtpk(SC[0], SC[1]); PIN(SC); PIN(pw0); SBAR(); \
            A_RD(5, (KP) + 5120); SBAR(); SN = MFMA32(fr[1], q[1], SN); SC[10] = EX2(SC[10]); SC[11] = EX2(SC[11]); pw0[1] = cvtpk(SC[2], SC[3]); PIN(SC); PIN(pw0); E0; SBAR(); \
            A_RD(6, (KP) + 6144); SBAR(); SN = MFMA32(fr[2], q[2], SN); SC[12] = EX2(SC[12]); SC[13] = EX2(SC[13]); pw0[2] = cvtpk(SC[4], SC[5]); PIN(SC); PIN(pw0); SBAR(); \
            A_RD(7, (KP) + 7168); SBAR(); SN = MFMA32(fr[3], q[3], SN); SC[14] = EX2(SC[14]); SC[15] = EX2(SC[15]); pw0[3] = cvtpk(SC[6], SC[7]); PIN(SC); PIN(pw0); SBAR(); \
            A_RD(0, (VP));        SBAR(); SN = MFMA32(fr[4], q[4], SN); l0 += SC[4]; l1 += SC[5]; l0 += SC[6]; l1 += SC[7]; PIN(l0); PIN(l1); E1; SBAR(); \
            A_RD(1, (VP) + 2048); SBAR(); SN = MFMA32(fr[5], q[5], SN); l0 += SC[8]; l1 += SC[9]; pw1[0] = cvtpk(SC[8], SC[9]); PIN(l0); PIN(l1); PIN(pw1); SBAR(); \
            A_RD(2, (VP) + 4096); SBAR(); SN = MFMA32(fr[6], q[6], SN); l0 += SC[10]; l1 += SC[11]; pw1[1] = cvtpk(SC[10], SC[11]); PIN(l0); PIN(l1); PIN(pw1); SBAR(); \
            A_RD(3, (VP) + 6144); SBAR(); SN = MFMA32(fr[7], q[7], SN); E2; SBAR(); } while (0)
#define PWF(x) __builtin_bit_cast(bf16x8, x)
#define A_P2A(SC, SN, VP, MASK, DQ) do { \
            A_RD(4, (VP) + 1024); A_RD(5, (VP) + 3072); SBAR(); o[0] = MFMA32(fr[0], PWF(pw0), o[0]); l0 += SC[12]; l1 += SC[13]; pw1[2] = cvtpk(SC[12], SC[13]); PIN(l0); PIN(l1); PIN(pw1); SBAR(); \
            A_RD(6, (VP) + 5120); A_RD(7, (VP) + 7168); SBAR(); o[1] = MFMA32(fr[1], PWF(pw0), o[1]); l0 += SC[14]; l1 += SC[15]; pw1[3] = cvtpk(SC[14], SC[15]); PIN(l0); PIN(l1); PIN(pw1); SBAR(); \
            float ma_ = 0.f, mb_ = 0.f; \
            o[2] = MFMA32(fr[2], PWF(pw0), o[2]); \
            if (MASK) att_mask(SN, DQ); \
            ma_ = MX3(SN[0], SN[1], SN[2]); mb_ = MX3(SN[3], SN[4], SN[5]); ma_ = MX3(ma_, SN[6], SN[7]); mb_ = MX3(mb_, SN[8], SN[9]); PIN(ma_); PIN(mb_); SBAR(); \
            o[3] = MFMA32(fr[3], PWF(pw0), o[3]); \
            ma_ = MX3(ma_, SN[10], SN[11]); mb_ = MX3(mb_, SN[12], SN[13]); ma_ = MX3(ma_, SN[14], SN[15]); ma_ = __builtin_fmaxf(ma_, mb_); \
            if (__builtin_expect(__any(ma_ > ATT_THR), 0)) {            \
                const float dl_ = __builtin_fmaxf(swap_max(ma_), 0.f); mref += dl_; resc = EX2(-dl_); l0 *= resc; l1 *= resc; pend = true; \
                _Pragma("unroll") for (int e = 0; e < 16; ++e) { SN[e] -= dl_; negm[e] = -mref; } PIN(negm); } SBAR(); } while (0)
#define A_RESC() do { if (__builtin_expect(pend, 0)) { pend = false;         \
            _Pragma("unroll") for (int d = 0; d < 4; ++d) _Pragma("unroll") for (int e = 0; e < 16; ++e) o[d][e] *= resc; } } while (0)
#define A_P2B(SN, KN, D0, D1) do { \
            A_RD(0, (KN));        SBAR(); o[0] = MFMA32(fr[4], PWF(pw1), o[0]); SN[0] = EX2(SN[0]); SN[1] = EX2(SN[1]); PIN(SN); D0; SBAR(); \
            A_RD(1, (KN) + 1024); SBAR(); o[1] = MFMA32(fr[5], PWF(pw1), o[1]); SN[2] = EX2(SN[2]); SN[3] = EX2(SN[3]); PIN(SN); SBAR(); \
            A_RD(2, (KN) + 2048); SBAR(); o[2] = MFMA32(fr[6], PWF(pw1), o[2]); SN[4] = EX2(SN[4]); SN[5] = EX2(SN[5]); l0 += SN[0]; l1 += SN[1]; PIN(SN); PIN(l0); PIN(l1); D1; SBAR(); \
            A_RD(3, (KN) + 3072); SBAR(); o[3] = MFMA32(fr[7], PWF(pw1), o[3]); SN[6] = EX2(SN[6]); SN[7] = EX2(SN[7]); l0 += SN[2]; l1 += SN[3]; PIN(SN); PIN(l0); PIN(l1); SBAR(); \
            A_RESC(); } while (0)
#pragma unroll 1
            for (int n = 0; n < ib; ++n) {
                A_P1(sA, sB, pc + 8192, pc + 32768, D_VE(0), D_VE(1), D_KE(0));
                A_P2A(sA, sB, pc + 32768, false, 0);
                A_P2B(sB, pn, D_KE(1), D_VO(0));
                A_P1(sB, sA, pn, pc + 32768 + 8192, D_VO(1), NOP_, NOP_);
                A_P2A(sB, sA, pc + 32768 + 8192, (n == ib - 1), qrow - 128 * ib);
                const int ev = blkno == 0 ? n : 4;
                if (ev != 0) WAIT_V(0); else if (kk < 24) WAIT_V(24); else WAIT_V(16);
                asm volatile("s_waitcnt lgkmcnt(0)\n\ts_barrier" ::: "memory");
                A_ADV();
                if (ev != 0) PUBLISH();
                if (ev < 4) {
                    if (ev == 1 && wid == 0) dma_words(lane * 4, ws + (size_t)(fatt + 32) * 8, lds0 + 131072);
                    if (ev == 2 && wid == 0) { check_parked<0, 12>(lds + 131072, ws, fatt + 32, tag, lane); ACQUIRE(); }
                    if (ev == 3) { const char* q2src = (const char*)ws + WS_QF + (size_t)((4 * x8 + h2) * 64 + 4 * i2) * 8192 + wid * 4096;
                        const unsigned lq = __builtin_amdgcn_readfirstlane(lds0 + wid * 4096);
                        dma_pair<131072, 131072 + 1024>(lane * 16, lane * 16 + 1024, q2src, lq); dma_pair<131072 + 2048, 131072 + 3072>(lane * 16 + 2048, lane * 16 + 3072, q2src, lq); }
                }
                A_P2B(sA, pn + 8192, D_KO(0), D_KO(1));
                so ^= 16384;
                const lds_cp t_ = pc; pc = pn; pn = t_;
            }
            if (blkno == 0) PROF(6);
            A_P1(sA, sB, pc + 8192, pc + 32768, D_VE(0); D_VE(1), D_KE(0); D_KE(1), D_VO(0); D_VO(1));
            if (blkno == 0) {
#pragma unroll
                for (int d = 0; d < 8; ++d) q[d] = ld128(qrd + d * 1024);
            }
            A_P2A(sA, sB, pc + 32768, true, qrow - 128 * ib - 32);
            {
                o[0] = MFMA32(fr[4], PWF(pw1), o[0]); sB[0] = EX2(sB[0]); sB[1] = EX2(sB[1]); PIN(sB); SBAR();
                o[1] = MFMA32(fr[5], PWF(pw1), o[1]); sB[2] = EX2(sB[2]); sB[3] = EX2(sB[3]); PIN(sB); SBAR();
                o[2] = MFMA32(fr[6], PWF(pw1), o[2]); sB[4] = EX2(sB[4]); sB[5] = EX2(sB[5]); l0 += sB[0]; l1 += sB[1]; PIN(sB); SBAR();
                o[3] = MFMA32(fr[7], PWF(pw1), o[3]); sB[6] = EX2(sB[6]); sB[7] = EX2(sB[7]); l0 += sB[2]; l1 += sB[3]; PIN(sB); SBAR();
                A_RESC();
            }
            {
                const lds_cp vp = pc + 32768 + 8192;
#pragma unroll
                for (int j = 0; j < 4; ++j) { fr[j] = ld128(vp + j * 2048); fr[4 + j] = ld128(vp + j * 2048 + 1024); }
#pragma unroll
                for (int e = 8; e < 16; ++e) sB[e] = EX2(sB[e]);
#pragma unroll
                for (int e = 4; e < 16; e += 2) { l0 += sB[e]; l1 += sB[e + 1]; }
#pragma unroll
                for (int e = 0; e < 4; ++e) { pw0[e] = cvtpk(sB[2 * e], sB[2 * e + 1]); pw1[e] = cvtpk(sB[8 + 2 * e], sB[9 + 2 * e]); }
                asm volatile("s_waitcnt vmcnt(0) lgkmcnt(0)\n\ts_barrier" ::: "memory");
                PUBLISH();
#pragma unroll
                for (int j = 0; j < 4; ++j) o[j] = MFMA32(fr[j], PWF(pw0), o[j]);
#pragma unroll
                for (int j = 0; j < 4; ++j) o[j] = MFMA32(fr[4 + j], PWF(pw1), o[j]);
            }
            {
                const int sb_own = kh == 0 ? 131072 + rg * 8192 : (rg >> 1) * 65536 + 32768 + so + (rg & 1) * 8192;
                const int sb_par = kh == 1 ? 131072 + rg * 8192 : (rg >> 1) * 65536 + 32768 + so + (rg & 1) * 8192;
                const float mown = dead ? -__builtin_inff() : mref, lown = l0 + l1;
                *(LAS f32x2*)(lds + kh * 65536 + so + 8192 + rg * 512 + lane * 8) = (f32x2){mown, lown};
                const f32x16 s0 = kh ? o[0] : o[2], s1 = kh ? o[1] : o[3], k0 = kh ? o[2] : o[0], k1 = kh ? o[3] : o[1];
#pragma unroll
                for (int qd = 0; qd < 4; ++qd) {
                    *(LAS f32x4*)(lds + sb_own + qd * 1024 + lane * 16) = (f32x4){s0[4 * qd], s0[4 * qd + 1], s0[4 * qd + 2], s0[4 * qd + 3]};
                    *(LAS f32x4*)(lds + sb_own + 4096 + qd * 1024 + lane * 16) = (f32x4){s1[4 * qd], s1[4 * qd + 1], s1[4 * qd + 2], s1[4 * qd + 3]};
                }
                asm volatile("s_waitcnt lgkmcnt(0)\n\ts_barrier" ::: "memory");
                const f32x2 ml = *(const LAS f32x2*)(lds + (kh ^ 1) * 65536 + so + 8192 + rg * 512 + lane * 8);
                f32x16 p0, p1;
#pragma unroll
                for (int qd = 0; qd < 4; ++qd) {
                    const f32x4 a = *(const LAS f32x4*)(lds + sb_par + qd * 1024 + lane * 16), b = *(const LAS f32x4*)(lds + sb_par + 4096 + qd * 1024 + lane * 16);
#pragma unroll
                    for (int e = 0; e < 4; ++e) { p0[4 * qd + e] = a[e]; p1[4 * qd + e] = b[e]; }
                }
                const float mf = __builtin_fmaxf(mown, ml[0]);
                const float fo = EX2(mown - mf), fp = EX2(ml[0] - mf);
                const float inv = 1.0f / swap_add(fo * lown + fp * ml[1]);
                const float ca = fo * inv, cb = fp * inv;
#pragma unroll
                for (int e = 0; e < 16; ++e) { p0[e] = ca * k0[e] + cb * p0[e]; p1[e] = ca * k1[e] + cb * p1[e]; }
                const unsigned dst = WS_AF + ((head * 8 + 4 * kh) * 64 + 4 * ib + rg) * 1024 + lane * 16;
                st16(rws, dst, pack8(p0, 0, 1.0f)); st16(rws, dst + 65536, pack8(p0, 1, 1.0f));
                st16(rws, dst + 2 * 65536, pack8(p1, 0, 1.0f)); st16(rws, dst + 3 * 65536, pack8(p1, 1, 1.0f));
                pf = F_C + ib * 32 + head;
            }
            so ^= 16384;
            if (blkno == 0) PROF(7);
        }
        PROF(8);
        asm volatile("s_waitcnt vmcnt(0) lgkmcnt(0)\n\ts_barrier" ::: "memory");
        PUBLISH();
#undef A_RD
#undef A_P1
#undef A_P2A
#undef A_P2B
#undef PWF
    }

    {
        const int tm = 4 * x8 + (kk >> 3), tn = kk & 7;
        const rsrc_t ra = __builtin_amdgcn_make_buffer_rsrc((void*)(ws + WS_AF), (short)0, NKS * 65536, 0x00020000);
        const rsrc_t rb = __builtin_amdgcn_make_buffer_rsrc((void*)(ws + WS_WOF), (short)0, NKS * 16384, 0x00020000);
        if (wid == 0) {
            if (bid == 0) {
#pragma unroll 1
                for (int i = 0; i < 4; ++i) poll_flags(ws, F_S + i * 64 + lane, true, tag);
                if (lane == 0) set_flag(ws, F_E, tag);
            }
            poll_flags(ws, lane < 32 ? F_C + (tm >> 1) * 32 + lane : lane < 40 ? F_U + (tm >> 2) * 8 + (lane & 7) : F_W + (lane & 15), lane < 38 || lane >= 48, tag);
        }
        BAR();
        PROF(9);
        const int va = wid * 65536 + tm * 2048 + lane * 16, vb = wid * 16384 + tn * 2048 + lane * 16;
        constexpr int DEP = 4;
        u32x4 fa[DEP][2], fb[DEP][2];
        f32x16 acc[2][2] = {};
#define K3_LD(slot, it) do { fa[slot][0] = __builtin_amdgcn_raw_buffer_load_b128(ra, va, (it) * 524288, SC1); fa[slot][1] = __builtin_amdgcn_raw_buffer_load_b128(ra, va + 1024, (it) * 524288, SC1); \
            fb[slot][0] = __builtin_amdgcn_raw_buffer_load_b128(rb, vb, (it) * 131072, SC1); fb[slot][1] = __builtin_amdgcn_raw_buffer_load_b128(rb, vb + 1024, (it) * 131072, SC1); } while (0)
#pragma unroll
        for (int s = 0; s < DEP; ++s) K3_LD(s, s);
#pragma unroll 1
        for (int it = 0; it < 40; it += DEP) {
#pragma unroll
            for (int s = 0; s < DEP; ++s) {
                const bf16x8 a0 = __builtin_bit_cast(bf16x8, fa[s][0]), a1 = __builtin_bit_cast(bf16x8, fa[s][1]), b0 = __builtin_bit_cast(bf16x8, fb[s][0]), b1 = __builtin_bit_cast(bf16x8, fb[s][1]);
                acc[0][0] = MFMA32(b0, a0, acc[0][0]); acc[0][1] = MFMA32(b1, a0, acc[0][1]);
                acc[1][0] = MFMA32(b0, a1, acc[1][0]); acc[1][1] = MFMA32(b1, a1, acc[1][1]);
                SBAR();
                K3_LD(s, it + s + DEP);
                SBAR();
            }
        }
#undef K3_LD
        PROF(10);
#pragma unroll
        for (int b = 0; b < 4; ++b)
#pragma unroll
            for (int g = 0; g < 4; ++g) { const f32x16& a = acc[b >> 1][b & 1];
                *(LAS f32x4*)(lds + wid * 16384 + b * 4096 + g * 1024 + ((lane ^ (4 * g)) << 4)) = (f32x4){a[4 * g], a[4 * g + 1], a[4 * g + 2], a[4 * g + 3]}; }
        const int row = tid >> 3, c8 = tid & 7;
        const u32x4 xv = *(const u32x4*)(x + (size_t)(tm * 64 + row) * DM + tn * 64 + c8 * 8);
        WAIT_L(0); BAR();
        const lds_cp rd = lds + ((row >> 5) * 2 + (c8 >> 2)) * 4096 + (c8 & 3) * 1024 + (((row & 31) ^ (4 * (c8 & 3))) << 4);
        f32x4 lo = {0.f, 0.f, 0.f, 0.f}, hi = {0.f, 0.f, 0.f, 0.f};
#pragma unroll
        for (int w = 0; w < 8; ++w) { lo += *(const LAS f32x4*)(rd + w * 16384); hi += *(const LAS f32x4*)(rd + w * 16384 + 512); }
        u32x4 o;
        o[0] = cvtpk(bflo(xv[0]) + lo[0], bfhi(xv[0]) + lo[1]); o[1] = cvtpk(bflo(xv[1]) + lo[2], bfhi(xv[1]) + lo[3]);
        o[2] = cvtpk(bflo(xv[2]) + hi[0], bfhi(xv[2]) + hi[1]); o[3] = cvtpk(bflo(xv[3]) + hi[2], bfhi(xv[3]) + hi[3]);
        *(u32x4*)(out + (size_t)(tm * 64 + row) * DM + tn * 64 + c8 * 8) = o;
    }
    PROF_END(ws);
}

extern "C" void kernel_launch(void* const* d_in, const int* in_sizes, int n_in, void* d_out, int out_size, void* d_ws, size_t ws_size, hipStream_t stream) {
    (void)in_sizes; (void)n_in; (void)out_size; (void)ws_size;
    fused_fwd<<<256, 512, 0, stream>>>((const bf16_t*)d_in[0], (const bf16_t*)d_in[1], (const bf16_t*)d_in[2], (const bf16_t*)d_in[3], (const bf16_t*)d_in[4], (const bf16_t*)d_in[5],
                                       (const bf16_t*)d_in[7], (const bf16_t*)d_in[8], (const bf16_t*)d_in[9], (const bf16_t*)d_in[10], (bf16_t*)d_out, (unsigned char*)d_ws);
}
```

```cpp
#include <hip/hip_runtime.h>
#include <hip/hip_bf16.h>
#include <stdint.h>

#define LAS __attribute__((address_space(3)))
#define DI __device__ __forceinline__
typedef unsigned short bf16_t;
typedef unsigned long long u64;
typedef __attribute__((address_space(1))) u64 gu64;
typedef short bf16x8 __attribute__((ext_vector_type(8)));
typedef short s16x4 __attribute__((ext_vector_type(4)));
typedef __bf16 bf16x2_t __attribute__((ext_vector_type(2)));
typedef float f32x16 __attribute__((ext_vector_type(16)));
typedef float f32x4 __attribute__((ext_vector_type(4)));
typedef float f32x2 __attribute__((ext_vector_type(2)));
typedef unsigned u32x4 __attribute__((ext_vector_type(4)));
typedef unsigned u32x2 __attribute__((ext_vector_type(2)));
typedef LAS unsigned char* lds_p;
typedef const LAS unsigned char* lds_cp;
typedef __amdgpu_buffer_rsrc_t rsrc_t;

constexpr int SEQ = 2048, DM = 512, NH = 32, FH = 688, FF = 1376;
constexpr int NKS = 299;
constexpr int F_E = 0;
constexpr int F_S = 512;
constexpr int F_X = 1024;
constexpr int F_L = 2048;
constexpr int F_U = 3072;
constexpr int F_K = 3584;
constexpr int F_C = 4096;
constexpr int F_P = 5120;
constexpr unsigned WS_G   = 65536;
constexpr unsigned WS_XN  = 131072;
constexpr unsigned WS_QF  = WS_XN + SEQ * DM * 2;
constexpr unsigned WS_KVF = WS_QF + NH * 64 * 8192;
constexpr unsigned WS_Z2N = WS_KVF + NH * 64 * 16384;
constexpr unsigned WS_AF  = WS_Z2N + SEQ * 768 * 2;
constexpr unsigned WS_P   = WS_AF + 304 * 64 * 1024;
constexpr unsigned WS_END = WS_P + 256 * 65536;

#define PROF_DECL
#define PROF(i)
#define PROF_END(wsp)
#define MFMA32(a, b, c) __builtin_amdgcn_mfma_f32_32x32x16_bf16((a), (b), (c), 0, 0, 0)
#define SBAR() __builtin_amdgcn_sched_barrier(0)
#define PIN(x) asm volatile("" : "+v"(x))
#define WAIT_V(n) asm volatile("s_waitcnt vmcnt(" #n ")" ::: "memory")
#define WAIT_L(n) asm volatile("s_waitcnt lgkmcnt(" #n ")" ::: "memory")
#define BAR() asm volatile("s_barrier" ::: "memory")
#define SC1 16

DI unsigned cvtpk(float lo, float hi) { f32x2 v = {lo, hi}; bf16x2_t b = __builtin_convertvector(v, bf16x2_t); return __builtin_bit_cast(unsigned, b); }
DI float bflo(unsigned w) { return __uint_as_float(w << 16); }
DI float bfhi(unsigned w) { return __uint_as_float(w & 0xffff0000u); }
DI u32x4 pack8(const f32x16& a, int s, float sc) {
    u32x4 w;
    w[0] = cvtpk(a[8 * s + 0] * sc, a[8 * s + 1] * sc); w[1] = cvtpk(a[8 * s + 2] * sc, a[8 * s + 3] * sc);
    w[2] = cvtpk(a[8 * s + 4] * sc, a[8 * s + 5] * sc); w[3] = cvtpk(a[8 * s + 6] * sc, a[8 * s + 7] * sc);
    return w;
}
DI bf16x8 ld128(lds_cp p) { return *(const LAS bf16x8*)p; }
typedef short v4i16_t __attribute__((ext_vector_type(4)));
DI s16x4 ldtr(lds_cp p) { return __builtin_bit_cast(s16x4, __builtin_amdgcn_ds_read_tr16_b64_v4i16((LAS v4i16_t*)p)); }
DI bf16x8 ldtr2(lds_cp p, int step) { const s16x4 lo = ldtr(p), hi = ldtr(p + step); return __builtin_shufflevector(lo, hi, 0, 1, 2, 3, 4, 5, 6, 7); }
template <int I0, int I1> DI void dma_pair(unsigned v0, unsigned v1, const void* sbase, unsigned l) {
    asm volatile("s_add_u32 m0, %3, %4\n\ts_nop 0\n\tglobal_load_lds_dwordx4 %0, %2 sc1\n\ts_add_u32 m0, %3, %5\n\ts_nop 0\n\tglobal_load_lds_dwordx4 %1, %2 sc1"
                 :: "v"(v0), "v"(v1), "s"(sbase), "s"(l), "i"(I0), "i"(I1) : "memory", "scc");
}
template <int I, int O> DI void bdma1(unsigned voff, rsrc_t rs, unsigned soff, unsigned l) {
    asm volatile("s_add_u32 m0, %3, %4\n\ts_nop 0\n\tbuffer_load_dwordx4 %0, %1, %2 offen offset:%5 sc1 lds" :: "v"(voff), "s"(rs), "s"(soff), "s"(l), "i"(I), "i"(O) : "memory", "scc");
}
DI void dma_words(unsigned voff, const void* sbase, unsigned l) {
    asm volatile("s_mov_b32 m0, %2\n\ts_nop 0\n\tglobal_load_lds_dword %0, %1 sc1" :: "v"(voff), "s"(sbase), "s"(l) : "memory");
}
DI void st16(rsrc_t rs, unsigned off, u32x4 v) { __builtin_amdgcn_raw_buffer_store_b128(v, rs, off, 0, SC1); }
DI u32x4 ld16(rsrc_t rs, unsigned off) { return __builtin_amdgcn_raw_buffer_load_b128(rs, off, 0, SC1); }
DI float swap_add(float v) { auto r = __builtin_amdgcn_permlane32_swap(__float_as_uint(v), __float_as_uint(v), false, false); return __uint_as_float(r[0]) + __uint_as_float(r[1]); }
DI float swap_max(float v) { auto r = __builtin_amdgcn_permlane32_swap(__float_as_uint(v), __float_as_uint(v), false, false); return __builtin_fmaxf(__uint_as_float(r[0]), __uint_as_float(r[1])); }
DI f32x2 unpk(unsigned w) { return (f32x2){bflo(w), bfhi(w)}; }
DI unsigned cvt2(f32x2 v) { return __builtin_bit_cast(unsigned, __builtin_convertvector(v, bf16x2_t)); }
DI float sum_pair(unsigned w, float acc) { return __builtin_amdgcn_fdot2_f32_bf16(__builtin_bit_cast(bf16x2_t, w), __builtin_bit_cast(bf16x2_t, 0x3f803f80u), acc, false); }
DI unsigned gelu2(float a, float b) {
    const f32x2 v = {a, b};
    const f32x2 t = v * __builtin_elementwise_fma(v * v, (f32x2){-2.302208198f * 0.044715f, -2.302208198f * 0.044715f}, (f32x2){-2.302208198f, -2.302208198f});
    const f32x2 d = (f32x2){__builtin_amdgcn_exp2f(t[0]), __builtin_amdgcn_exp2f(t[1])} + (f32x2){1.0f, 1.0f};
    return cvt2(v * (f32x2){__builtin_amdgcn_rcpf(d[0]), __builtin_amdgcn_rcpf(d[1])});
}
DI u32x4 gelu8(const f32x16& a, int s) { return (u32x4){gelu2(a[8 * s], a[8 * s + 1]), gelu2(a[8 * s + 2], a[8 * s + 3]), gelu2(a[8 * s + 4], a[8 * s + 5]), gelu2(a[8 * s + 6], a[8 * s + 7])}; }
DI void set_flag(unsigned char* ws, int idx, u64 tag) { __hip_atomic_store((gu64*)ws + idx, tag, __ATOMIC_RELAXED, __HIP_MEMORY_SCOPE_AGENT); }
DI void poll_flags(unsigned char* ws, int idx, bool active, u64 tag) {
#pragma unroll 1
    for (unsigned spins = 0; spins < (1u << 17); ++spins) {
        const u64 v = __hip_atomic_load((gu64*)ws + idx, __ATOMIC_RELAXED, __HIP_MEMORY_SCOPE_AGENT);
        if (__all(!active || v == tag)) break;
        __builtin_amdgcn_s_sleep(4);
    }
}
#define ACQUIRE() asm volatile("buffer_inv sc1" ::: "memory")

constexpr int LDS_TAG = 163584, LDS_TOUCH = 163328, LDS_PARK = 163072;
DI void touch(unsigned voff, const void* sbase, unsigned lds0) { dma_words(voff, sbase, lds0 + LDS_TOUCH); }
#define TOUCH_A(v, s, l) touch(v, s, l)
#define TOUCH_S(v, s, l) touch(v, s, l)
#define TOUCH_Z(v, s, l) touch(v, s, l)
template <int I> DI void dma_one(unsigned v0, const void* sbase, unsigned l) {
    asm volatile("s_add_u32 m0, %2, %3\n\ts_nop 0\n\tglobal_load_lds_dwordx4 %0, %1 sc1" :: "v"(v0), "s"(sbase), "s"(l), "i"(I) : "memory", "scc");
}
DI void z2_half(const lds_p lds, const unsigned lds0, f32x16 (&acc)[3], const bf16_t* __restrict__ x, const bf16_t* __restrict__ w_in, const unsigned char* fx, const int rg, const int hf,
                const int tid, const int wid, const int lane
#define GP(i)
                ) {
    constexpr int ZS = 704, ZST = 32 * ZS;
    constexpr unsigned WROW = FF * 2;
    const int row = tid >> 3, seg = tid & 7;
    const u32x4* src = (const u32x4*)(x + (size_t)(rg * 64 + row) * DM) + seg;
    u32x4 v[8];
#pragma unroll
    for (int j = 0; j < 8; ++j) asm volatile("global_load_dwordx4 %0, %1, off offset:%2" : "=v"(v[j]) : "v"(src), "i"(j * 128) : "memory");
    unsigned bv[3];
    const int p2 = wid < 6 ? wid + 16 : wid + 8;
#pragma unroll
    for (int i = 0; i < 3; ++i) { const int c = (i == 2 ? p2 : wid + 8 * i) * 64 + lane, zr = c / 44, zc = c - zr * 44; bv[i] = zr * WROW + (zc > 42 ? 42 : zc) * 16; }
    const unsigned lw = lds0 + 65536 + wid * 1024, lw2 = lds0 + 65536 + p2 * 1024;
    const char* wg = (const char*)w_in + FH * 2 + hf * FH;
#define Z_STAGE(slot, g_) do { dma_pair<(slot) * ZST, (slot) * ZST + 8192>(bv[0], bv[1], (g_), lw); dma_one<(slot) * ZST>(bv[2], (g_), lw2); } while (0)
    BAR();
    Z_STAGE(0, wg); Z_STAGE(1, wg + 32 * WROW); Z_STAGE(2, wg + 64 * WROW); Z_STAGE(3, wg + 96 * WROW);
#pragma unroll
    for (int i = 0; i < 2; ++i) { int c = (2 * wid + i) * 64 + lane; c = c > 895 ? 895 : c; const int zr = c / 7, zc = c - zr * 7; TOUCH_Z(((rg & 3) * 128 + zr) * WROW + (zc == 6 ? 684 : zc * 128), wg, lds0); }
    SBAR();
    GP(5);
    {
        asm volatile("s_waitcnt vmcnt(14)" : "+v"(v[0]), "+v"(v[1]), "+v"(v[2]), "+v"(v[3]), "+v"(v[4]), "+v"(v[5]), "+v"(v[6]), "+v"(v[7]));
        GP(6);
        float s = 0.f, s1 = 0.f;
#pragma unroll
        for (int j = 0; j < 8; ++j) { s = sum_pair(v[j][0], s); s1 = sum_pair(v[j][1], s1); s = sum_pair(v[j][2], s); s1 = sum_pair(v[j][3], s1); }
        s += s1;
        s += __shfl_xor(s, 1); s += __shfl_xor(s, 2); s += __shfl_xor(s, 4);
        const float mean = s * (1.0f / DM);
        f32x2 d[32], q2 = {0.f, 0.f}, q3 = {0.f, 0.f};
#pragma unroll
        for (int i = 0; i < 32; i += 2) { d[i] = unpk(v[i >> 2][i & 3]) - (f32x2){mean, mean}; d[i + 1] = unpk(v[i >> 2][(i & 3) + 1]) - (f32x2){mean, mean};
            q2 = __builtin_elementwise_fma(d[i], d[i], q2); q3 = __builtin_elementwise_fma(d[i + 1], d[i + 1], q3); }
        q2 += q3;
        float q = q2[0] + q2[1];
        q += __shfl_xor(q, 1); q += __shfl_xor(q, 2); q += __shfl_xor(q, 4);
        const float rstd = 1.0f / sqrtf(q * (1.0f / DM) + 1e-5f);
#pragma unroll
        for (int j = 0; j < 8; ++j) {
            u32x4 o;
#pragma unroll
            for (int e = 0; e < 4; ++e) o[e] = cvt2(d[4 * j + e] * (f32x2){rstd, rstd});
            *(LAS u32x4*)(lds + j * 8192 + row * 128 + ((seg ^ ((row >> 1) & 7)) << 4)) = o;
        }
    }
    const int r = lane & 31, hh = lane >> 5, blk = (lane >> 4) & 1, qq = (lane & 15) >> 2, p = lane & 3;
    const int asw = (r >> 1) & 7, oth = (wid & 1) ? -4096 : 4096;
    lds_cp ab[4];
#pragma unroll
    for (int k = 0; k < 4; ++k) ab[k] = lds + (wid & 1) * 4096 + r * 128 + (((2 * k + hh) ^ asw) << 4);
    const lds_cp bb = lds + 65536 + (8 * hh + qq) * ZS + wid * 64 + blk * 32 + p * 8;
    const lds_cp bx = bb + (8 + (wid >> 1) - wid) * 64;
    const bool w3 = wid < 6;
#pragma unroll
    for (int i = 0; i < 3; ++i) acc[i] = f32x16{};
    bf16x8 fa[2][2][2], fb[2][2][2];
#define Z_READ(set, slot, k0, aoff_) do { _Pragma("unroll") for (int k_ = 0; k_ < 2; ++k_) { \
        fa[set][k_][0] = ld128(ab[(k0) + k_] + (aoff_)); fa[set][k_][1] = ld128(ab[(k0) + k_] + oth + (aoff_)); \
        fb[set][k_][0] = ldtr2(bb + (slot) * ZST + k_ * 16 * ZS, 4 * ZS); if (w3) fb[set][k_][1] = ldtr2(bx + (slot) * ZST + k_ * 16 * ZS, 4 * ZS); } } while (0)
#define Z_MMA(set, k_) do { acc[0] = MFMA32(fb[set][k_][0], fa[set][k_][0], acc[0]); acc[1] = MFMA32(fb[set][k_][0], fa[set][k_][1], acc[1]); \
        if (w3) acc[2] = MFMA32(fb[set][k_][1], fa[set][k_][0], acc[2]); } while (0)
#define Z_SYNC(n) asm volatile("s_waitcnt vmcnt(" #n ") lgkmcnt(0)\n\ts_barrier" ::: "memory")
    GP(7);
    Z_SYNC(11);
    GP(8);
    Z_READ(0, 0, 0, 0);
    const char* g = wg + 128 * WROW;
#pragma unroll 1
    for (int kt = 0; kt < 3; ++kt) {
        Z_SYNC(6); Z_READ(1, 1, 2, 0);     SBAR(); Z_MMA(0, 0); SBAR(); Z_STAGE(0, g);             SBAR(); Z_MMA(0, 1); SBAR();
        Z_SYNC(6); Z_READ(0, 2, 0, 8192);  SBAR(); Z_MMA(1, 0); SBAR(); Z_STAGE(1, g + 32 * WROW); SBAR(); Z_MMA(1, 1); SBAR();
        Z_SYNC(6); Z_READ(1, 3, 2, 8192);  SBAR(); Z_MMA(0, 0); SBAR(); Z_STAGE(2, g + 64 * WROW); SBAR(); Z_MMA(0, 1); SBAR();
        Z_SYNC(6); Z_READ(0, 0, 0, 16384); SBAR(); Z_MMA(1, 0); SBAR(); Z_STAGE(3, g + 96 * WROW); SBAR(); Z_MMA(1, 1); SBAR();
        g += 128 * WROW;
#pragma unroll
        for (int k = 0; k < 4; ++k) ab[k] += 16384;
    }
    if (wid == 0) dma_words(lane * 4, fx, lds0 + LDS_PARK);
    Z_SYNC(6); Z_READ(1, 1, 2, 0);    SBAR(); Z_MMA(0, 0); Z_MMA(0, 1); SBAR();
    Z_SYNC(3); Z_READ(0, 2, 0, 8192); SBAR(); Z_MMA(1, 0); Z_MMA(1, 1); SBAR();
    Z_SYNC(0); Z_READ(1, 3, 2, 8192); SBAR(); Z_MMA(0, 0); Z_MMA(0, 1); SBAR();
    Z_SYNC(0);                                Z_MMA(1, 0); Z_MMA(1, 1);
#undef Z_STAGE
#undef Z_READ
#undef Z_MMA
#undef Z_SYNC
}

struct Run { const char* A; const char* B; unsigned dA1, dA2, dB1, dB2, vA0, vA1, vB0, vB1; };
constexpr int HTB = 16384;
constexpr unsigned LDA = DM * 2, LDB = 4096 * 2;
#define G_SA(b, h) (((b) * 2 + (h)) * HTB)
#define G_SB(b, h) ((4 + (b) * 2 + (h)) * HTB)
#define G_STAGE(bufoff, gb, v0, v1) dma_pair<(bufoff), (bufoff) + 8192>((v0), (v1), (gb), ldsw)
#define G_LDA(dst, b, h) do { _Pragma("unroll") for (int m_ = 0; m_ < 2; ++m_) _Pragma("unroll") for (int k_ = 0; k_ < 4; ++k_) dst[m_][k_] = ld128(lds + G_SA(b, h) + aoff[k_] + m_ * 4096); } while (0)
#define G_LDB(dst, b, h) do { _Pragma("unroll") for (int k_ = 0; k_ < 4; ++k_) dst[k_] = ldtr2(lds + G_SB(b, h) + boff + k_ * 4096, 1024); } while (0)
#define G_MMA(ai, bj, At, Bt) do { __builtin_amdgcn_s_setprio(1); _Pragma("unroll") for (int k_ = 0; k_ < 4; ++k_) _Pragma("unroll") for (int m_ = 0; m_ < 2; ++m_) \
        acc[ai][bj][m_] = SWAP ? MFMA32(At[m_][k_], Bt[k_], acc[ai][bj][m_]) : MFMA32(Bt[k_], At[m_][k_], acc[ai][bj][m_]); __builtin_amdgcn_s_setprio(0); } while (0)

DI void g_prologue_b(const Run& u, const unsigned ldsw) {
    G_STAGE(G_SB(0, 0), u.B, u.vB0, u.vB1); G_STAGE(G_SB(0, 1), u.B + u.dB1, u.vB0, u.vB1);
}
DI void g_prologue_rest(const Run& u, const unsigned ldsw) {
    G_STAGE(G_SA(0, 0), u.A, u.vA0, u.vA1); G_STAGE(G_SA(0, 1), u.A + u.dA1, u.vA0, u.vA1);
    G_STAGE(G_SB(1, 0), u.B + u.dB2, u.vB0, u.vB1); G_STAGE(G_SA(1, 0), u.A + u.dA2, u.vA0, u.vA1); G_STAGE(G_SB(1, 1), u.B + u.dB2 + u.dB1, u.vB0, u.vB1);
}
template <int LO> DI void check_parked(const int HI, lds_cp parked, unsigned char* ws, const int fidx, const u64 tag, const int lane) {
    const unsigned w = *(const LAS unsigned*)(parked + lane * 4);
    const int sl = (lane >> 1) & 15;
    if (!__all(sl < LO || sl >= HI || w == ((lane & 1) ? (unsigned)(tag >> 32) : (unsigned)tag))) poll_flags(ws, fidx + (lane & 31), lane < 32 && (lane & 15) >= LO && (lane & 15) < HI, tag);
}

template <bool SWAP, int ROUND>
DI void g_kloop(const lds_p lds, const unsigned lds0, f32x16 (&acc)[2][2][2], const Run& cur, const Run& nxt, const int (&aoff)[4], const int boff, const unsigned ldsw,
                unsigned char* ws, const u64 tag, const int fl0, const int fatt, const int lane, const int wid, const unsigned tch) {
    bf16x8 At[2][4], B0[4], B1[4];
#pragma unroll
    for (int i = 0; i < 8; ++i) acc[i >> 2][(i >> 1) & 1][i & 1] = f32x16{};
#pragma unroll 1
    for (int t = 0; t < 8; t += 2) {
        const bool last = (t == 6);
        const char* a1 = cur.A + (t + 1) * 128 + 128 * LDA;
        const char* a2 = last ? nxt.A : cur.A + (t + 2) * 128;
        const char* b2 = last ? nxt.B : cur.B + (size_t)(t + 2) * 64 * LDB;
        const unsigned dA1 = last ? nxt.dA1 : cur.dA1, dA2 = last ? nxt.dA2 : cur.dA2, dB1 = last ? nxt.dB1 : cur.dB1, dB2 = last ? nxt.dB2 : cur.dB2;
        const unsigned vA0 = last ? nxt.vA0 : cur.vA0, vA1 = last ? nxt.vA1 : cur.vA1, vB0 = last ? nxt.vB0 : cur.vB0, vB1 = last ? nxt.vB1 : cur.vB1;
        if (ROUND == 1 && last && wid == 0) dma_words(lane * 4, ws + (size_t)fatt * 8, lds0 + 131072);
        if (t == 2 && wid == 0 && lane == 0) set_flag(ws, fl0, tag);
        G_LDB(B0, 0, 0); SBAR(); G_LDA(At, 0, 0); if (!(ROUND == 1 && t == 0)) G_STAGE(G_SA(1, 1), a1, cur.vA0, cur.vA1);
        if (ROUND == 0 && t == 2) touch(tch, nxt.A, lds0);
        if (ROUND == 1 && t == 4 && wid == 0) { check_parked<0>(8, lds + 131072, ws, fatt, tag, lane); ACQUIRE(); }
        WAIT_L(8); BAR(); WAIT_L(0); SBAR(); G_MMA(0, 0, At, B0); SBAR(); BAR();
        G_LDB(B1, 0, 1);
        G_STAGE(G_SB(0, 0), b2, vB0, vB1);
        BAR(); WAIT_L(0); SBAR(); G_MMA(0, 1, At, B1); SBAR(); BAR();
        G_LDA(At, 0, 1); G_STAGE(G_SA(0, 0), a2, vA0, vA1);
        BAR(); WAIT_L(0); SBAR(); G_MMA(1, 0, At, B0); SBAR(); BAR();
        G_STAGE(G_SB(0, 1), b2 + dB1, vB0, vB1);
        if (ROUND == 1 && t == 0) WAIT_V(22); else if ((ROUND == 1 && t == 4 && wid == 0) || (ROUND == 0 && t == 2)) WAIT_V(7); else WAIT_V(6);
        BAR(); SBAR(); G_MMA(1, 1, At, B1); SBAR(); BAR();
        if (ROUND == 1 && wid == 0) {
            if (t == 2) dma_words(lane * 4, ws + (size_t)fatt * 8, lds0 + 131072);
            if (last) check_parked<8>(12, lds + 131072, ws, fatt, tag, lane);
        }
        G_LDB(B0, 1, 0); SBAR(); G_LDA(At, 1, 0); G_STAGE(G_SA(0, 1), a2 + dA1, vA0, vA1);
        WAIT_L(8); BAR(); WAIT_L(0); SBAR(); G_MMA(0, 0, At, B0); SBAR(); BAR();
        G_LDB(B1, 1, 1); G_STAGE(G_SB(1, 0), b2 + dB2, vB0, vB1);
        BAR(); WAIT_L(0); SBAR(); G_MMA(0, 1, At, B1); SBAR(); BAR();
        G_LDA(At, 1, 1); G_STAGE(G_SA(1, 0), a2 + dA2, vA0, vA1);
        BAR(); WAIT_L(0); SBAR(); G_MMA(1, 0, At, B0); SBAR(); BAR();
        G_STAGE(G_SB(1, 1), b2 + dB2 + dB1, vB0, vB1);
        if (!(ROUND == 1 && last)) WAIT_V(6);
        BAR(); SBAR(); G_MMA(1, 1, At, B1); SBAR();
        if (!(last && wid >= 4)) BAR();
    }
}

DI void g_epilogue(const f32x16 (&acc)[2][2][2], const rsrc_t rws, const int type, const int pn, const int pm, const int wr, const int wc, const int lane) {
    constexpr float QS = 0.08838834764831845f * 1.4426950408889634f;
    const float sc = type == 2 ? QS : 1.0f;
    const unsigned base = (type == 2 ? WS_QF : WS_KVF + (type == 1 ? 8192 : 0)) + wc * 2048 + lane * 16;
    const unsigned hstride = type == 2 ? 8192 : 16384;
#pragma unroll
    for (int ai = 0; ai < 2; ++ai)
#pragma unroll
        for (int bj = 0; bj < 2; ++bj)
#pragma unroll
            for (int m = 0; m < 2; ++m) {
                const unsigned dst = base + (unsigned)((2 * pn + bj) * 64 + pm * 8 + ai * 4 + wr * 2 + m) * hstride;
                st16(rws, dst, pack8(acc[ai][bj][m], 0, sc)); st16(rws, dst + 1024, pack8(acc[ai][bj][m], 1, sc));
            }
}

#define NOP_ (void)0
#define SP_CHECK() if (t == 36) { poll_flags(ws, fatt + lane, (lane & 15) < 12, tag); ACQUIRE(); }
#define EX2(v) __builtin_amdgcn_exp2f(v)
#define MX3(a, b, c) __builtin_fmaxf(__builtin_fmaxf((a), (b)), (c))
constexpr float ATT_THR = 32.0f;
DI void att_mask(f32x16& s, int dq) {
#pragma unroll
    for (int e = 0; e < 16; ++e) if ((e & 3) + 8 * (e >> 2) > dq) s[e] = -__builtin_inff();
}

struct OutArgs { rsrc_t ra; const char* wo; const char* wu; lds_cp rd[4]; unsigned wv, uv[6], va, sc, su, ldsw; };
template <int T> DI void out_w(const OutArgs& o) {
    constexpr int S = (T % 9) * 16384;
    if constexpr (T < 16) dma_pair<S, S + 8192>(o.wv, o.wv + 32768, o.wo + T * 65536, o.ldsw);
    else if constexpr (T < 19) dma_pair<S, S + 8192>(o.uv[2 * (T - 16)], o.uv[2 * (T - 16) + 1], o.wu, o.ldsw);
}
template <int T> DI void out_a(const OutArgs& o, u32x4 (&af)[6][2]) {
    if constexpr (T < 19) {
        const unsigned so = T < 16 ? o.sc + T * 262144 : o.su + (T - 16) * 262144;
        asm volatile("buffer_load_dwordx4 %0, %1, %2, %3 offen sc1" : "=v"(af[T % 6][0]) : "v"(o.va), "s"(o.ra), "s"(so) : "memory");
        asm volatile("buffer_load_dwordx4 %0, %1, %2, %3 offen sc1" : "=v"(af[T % 6][1]) : "v"(o.va), "s"(o.ra), "s"(so + 65536) : "memory");
    }
}
constexpr int out_cnt(int i) { return (i + 8 < 19 ? 2 : 0) + (i + 5 < 19 ? 2 : 0); }
constexpr int out_allow(int t) { int n = t < 4 ? 2 * (4 - t) : 0; for (int i = t < 4 ? 0 : t - 4; i < t; ++i) n += out_cnt(i); return n; }
template <int T> DI void out_step(const OutArgs& o, u32x4 (&af)[6][2], bf16x8 (&wf)[2][4], f32x16 (&acc)[4]) {
    asm volatile("s_waitcnt vmcnt(%2)\n\ts_barrier" : "+v"(af[T % 6][0]), "+v"(af[T % 6][1]) : "n"(out_allow(T)) : "memory");
    const bf16x8 a0 = __builtin_bit_cast(bf16x8, af[T % 6][0]), a1 = __builtin_bit_cast(bf16x8, af[T % 6][1]);
#pragma unroll
    for (int c = 0; c < 4; ++c) wf[1][c] = ldtr2(o.rd[c] + (T % 9) * 16384 + 4096, 2048);
    SBAR();
    acc[0] = MFMA32(wf[0][0], a0, acc[0]); acc[1] = MFMA32(wf[0][1], a0, acc[1]); SBAR();
    out_w<T + 8>(o); SBAR();
    acc[2] = MFMA32(wf[0][2], a0, acc[2]); acc[3] = MFMA32(wf[0][3], a0, acc[3]); SBAR();
    if constexpr (T < 18) {
#pragma unroll
        for (int c = 0; c < 4; ++c) wf[0][c] = ldtr2(o.rd[c] + ((T + 1) % 9) * 16384, 2048);
    }
    SBAR();
    acc[0] = MFMA32(wf[1][0], a1, acc[0]); acc[1] = MFMA32(wf[1][1], a1, acc[1]); SBAR();
    out_a<T + 5>(o, af); SBAR();
    acc[2] = MFMA32(wf[1][2], a1, acc[2]); acc[3] = MFMA32(wf[1][3], a1, acc[3]); SBAR();
}

#define IT(h1, i1, h2, i2) ((h1) | (i1) << 2 | (h2) << 6 | (i2) << 8)
__device__ constexpr unsigned ITEMS[32] = {
    IT(0, 14, 0, 4), IT(1, 14, 1, 4), IT(2, 14, 2, 4), IT(3, 14, 3, 4), IT(0, 13, 0, 5), IT(1, 13, 1, 5), IT(2, 13, 2, 5), IT(3, 13, 3, 5),
    IT(0, 12, 0, 6), IT(1, 12, 1, 6), IT(2, 12, 2, 6), IT(3, 12, 3, 6), IT(0, 11, 0, 7), IT(1, 11, 1, 7), IT(2, 11, 2, 7), IT(3, 11, 3, 7),
    IT(0, 15, 0, 2), IT(1, 15, 1, 2), IT(2, 15, 2, 2), IT(3, 15, 3, 2), IT(0, 9, 0, 8), IT(1, 9, 1, 8), IT(2, 9, 2, 8), IT(3, 9, 3, 8),
    IT(0, 3, 0, 0), IT(1, 3, 1, 0), IT(2, 3, 2, 0), IT(3, 3, 3, 0), IT(0, 1, 1, 1), IT(2, 1, 3, 1), IT(0, 10, 1, 10), IT(2, 10, 3, 10)
};

__global__ __launch_bounds__(512, 2) void fused_fwd(const bf16_t* __restrict__ x, const bf16_t* __restrict__ ckv, const bf16_t* __restrict__ wq, const bf16_t* __restrict__ wk,
                                                    const bf16_t* __restrict__ wv, const bf16_t* __restrict__ wo, const bf16_t* __restrict__ w_in, const bf16_t* __restrict__ wsp,
                                                    const bf16_t* __restrict__ bsp, const bf16_t* __restrict__ wout, bf16_t* __restrict__ out, unsigned char* ws) {
    __shared__ __attribute__((aligned(16))) unsigned char smem[163840];
    const lds_p lds = (lds_p)smem;
    const unsigned lds0 = (unsigned)(uintptr_t)smem;
    const int tid = threadIdx.x, wid = __builtin_amdgcn_readfirstlane(tid >> 6), lane = tid & 63;
    const int bid = blockIdx.x, x8 = bid & 7, kk = bid >> 3;
    const int r = lane & 31, hh = lane >> 5, blk = (lane >> 4) & 1, qq = (lane & 15) >> 2, p = lane & 3;
    const rsrc_t rws = __builtin_amdgcn_make_buffer_rsrc((void*)ws, (short)0, WS_END, 0x00020000);
    const unsigned ldsw = lds0 + (unsigned)wid * 1024u;
    PROF_DECL;
    dma_words(lane * 4, ws, lds0 + LDS_TAG);
    u64 tag;
#define MAKE_TAG() do { const u32x2 e_ = *(const LAS u32x2*)(lds + LDS_TAG); \
        const u64 ep_ = ((u64)(unsigned)__builtin_amdgcn_readfirstlane(e_[1]) << 32) | (unsigned)__builtin_amdgcn_readfirstlane(e_[0]); \
        u64 z_ = ep_ + 0x9E3779B97F4A7C15ull; z_ = (z_ ^ (z_ >> 30)) * 0xBF58476D1CE4E5B9ull; z_ = (z_ ^ (z_ >> 27)) * 0x94D049BB133111EBull; z_ ^= z_ >> 31; tag = z_ == ep_ ? ~z_ : z_; } while (0)
    const unsigned item = ITEMS[kk];
    const int h1 = item & 3, i1 = (item >> 2) & 15, h2 = (item >> 6) & 3, i2 = (item >> 8) & 15;
    const unsigned char* kvx = ws + WS_KVF + (size_t)x8 * 4 * 64 * 16384;
    const unsigned char* kvh = kvx + (size_t)h1 * 64 * 16384;
    const char* q1src = (const char*)ws + WS_QF + (size_t)((4 * x8 + h1) * 64 + 4 * i1 + (wid & 3)) * 8192;
    bf16x8 q[8];
#define Q1_LOAD() do { _Pragma("unroll") for (int d = 0; d < 8; ++d) asm volatile("global_load_dwordx4 %0, %1, %2 offset:%3 sc1" : "=v"(q[d]) : "v"(lane * 16 + (d >> 2) * 4096), "s"(q1src), "i"((d & 3) * 1024) : "memory"); } while (0)
#define Q1_WAIT(n) asm volatile("s_waitcnt vmcnt(" #n ")" : "+v"(q[0]), "+v"(q[1]), "+v"(q[2]), "+v"(q[3]), "+v"(q[4]), "+v"(q[5]), "+v"(q[6]), "+v"(q[7]) :: "memory")
    const int fatt = F_K + x8 * 64;
#define Q2_LOAD() do { const char* q2src = (const char*)ws + WS_QF + (size_t)((4 * x8 + h2) * 64 + 4 * i2) * 8192 + wid * 4096; \
        const unsigned lq_ = __builtin_amdgcn_readfirstlane(lds0 + wid * 4096); \
        dma_pair<131072, 131072 + 1024>(lane * 16, lane * 16 + 1024, q2src, lq_); dma_pair<131072 + 2048, 131072 + 3072>(lane * 16 + 2048, lane * 16 + 3072, q2src, lq_); } while (0)
    Run rat;
    rat.A = (const char*)kvh; rat.B = (const char*)kvh + 32768; rat.dA1 = rat.dB1 = 65536; rat.dA2 = rat.dB2 = 8192;
    rat.vA0 = rat.vB0 = wid * 1024 + lane * 16; rat.vA1 = rat.vB1 = rat.vA0 + 16384;
    int pf;
    int aoff[4];
    const unsigned arow = 8 * wid + (lane >> 3), achk = ((lane & 7) ^ (((wid & 1) << 2) + (lane >> 4))) << 4;
    const unsigned brow = 4 * wid + (lane >> 4), bchk = ((lane & 15) ^ (((lane >> 4) & 3) << 2)) << 4;

    if (kk < 24) {
        const int type = kk >> 3, pn = 2 * x8 + ((kk >> 2) & 1), pm0 = (kk & 3) + (type == 2 ? 4 : 0), pm1 = pm0 ^ 4, wr = wid >> 2, wc = wid & 3;
        Run u0, u1;
        const char* Ab = type == 2 ? (const char*)ws + WS_XN : (const char*)ckv;
        u0.B = (const char*)(type == 0 ? wk : type == 1 ? wv : wq) + pn * 512; u0.A = Ab + (size_t)pm0 * 256 * LDA;
        u0.dA1 = 128 * LDA; u0.dA2 = 128; u0.dB1 = 256; u0.dB2 = 64 * LDB;
        u0.vA0 = arow * LDA + achk; u0.vA1 = u0.vA0 + 64 * LDA; u0.vB0 = brow * LDB + bchk; u0.vB1 = u0.vB0 + 32 * LDB;
        u1 = u0; u1.A = Ab + (size_t)pm1 * 256 * LDA;
        const unsigned tch = (type == 2 ? ((kk >> 2) & 1) * 2 : type * 2 + ((kk >> 2) & 1)) * 65536 + tid * 128, bl = ((kk & 3) * 128 + wid * 16 + (lane >> 2)) * LDB + (lane & 3) * 128;
        if (type < 2) {
            g_prologue_b(u0, ldsw); g_prologue_rest(u0, ldsw);
            PROF(1);
            WAIT_V(6);
            MAKE_TAG();
        } else {
            const int j = x8 * 8 + (kk - 16), row = 32 * j + 4 * wid + (lane >> 4);
            const u32x4* src = (const u32x4*)(x + (size_t)row * DM) + (lane & 15);
            u32x4 v[4];
#pragma unroll
            for (int i = 0; i < 4; ++i) v[i] = src[16 * i];
            SBAR();
            g_prologue_b(u0, ldsw); touch(bl, u0.B, lds0);
            SBAR();
            float s = 0.f;
#pragma unroll
            for (int i = 0; i < 4; ++i)
#pragma unroll
                for (int e = 0; e < 4; ++e) s += bflo(v[i][e]) + bfhi(v[i][e]);
            s += __shfl_xor(s, 1); s += __shfl_xor(s, 2); s += __shfl_xor(s, 4); s += __shfl_xor(s, 8);
            const float mean = s * (1.0f / DM);
            float q = 0.f;
#pragma unroll
            for (int i = 0; i < 4; ++i)
#pragma unroll
                for (int e = 0; e < 4; ++e) { const float a = bflo(v[i][e]) - mean, b = bfhi(v[i][e]) - mean; q += a * a + b * b; }
            q += __shfl_xor(q, 1); q += __shfl_xor(q, 2); q += __shfl_xor(q, 4); q += __shfl_xor(q, 8);
            const float rstd = 1.0f / sqrtf(q * (1.0f / DM) + 1e-5f);
#pragma unroll
            for (int i = 0; i < 4; ++i) {
                u32x4 o;
#pragma unroll
                for (int e = 0; e < 4; ++e) o[e] = cvtpk((bflo(v[i][e]) - mean) * rstd, (bfhi(v[i][e]) - mean) * rstd);
                st16(rws, WS_XN + row * 1024 + ((lane & 15) + 16 * i) * 16, o);
            }
            WAIT_V(0);
            MAKE_TAG();
            BAR();
            if (tid == 0) set_flag(ws, F_X + j, tag);
            if (wid == 0) { poll_flags(ws, F_X + ((lane & 8) ? pm1 : pm0) * 8 + (lane & 7), lane < 16, tag); ACQUIRE(); WAIT_V(0); }
            BAR();
            g_prologue_rest(u0, ldsw);
            PROF(1);
            WAIT_V(6);
        }
#pragma unroll
        for (int k = 0; k < 4; ++k) aoff[k] = (wr * 64 + r) * 128 + (((2 * k + hh) ^ ((r >> 1) & 7)) << 4);
        const int boff = (8 * hh + qq) * 256 + ((wc ^ qq) << 6) + blk * 32 + p * 8;
        BAR();
        if (wr == 1) BAR();
        PROF(2);
        f32x16 acc[2][2][2];
        const int fk = fatt + (pn & 1) * 16 + type * 4 + (kk & 3);
        if (type == 1) g_kloop<true, 0>(lds, lds0, acc, u0, u1, aoff, boff, ldsw, ws, tag, F_S + bid, fatt, lane, wid, tch);
        else           g_kloop<false, 0>(lds, lds0, acc, u0, u1, aoff, boff, ldsw, ws, tag, F_S + bid, fatt, lane, wid, tch);
        PROF(3);
        G_STAGE(G_SA(1, 1), u1.A + 128 + 128 * LDA, u1.vA0, u1.vA1);
        SBAR();
        g_epilogue(acc, rws, type, pn, pm0, wr, wc, lane);
        PROF(11);
        if (wr == 1) BAR();
        if (type == 1) g_kloop<true, 1>(lds, lds0, acc, u1, rat, aoff, boff, ldsw, ws, tag, fk, fatt, lane, wid, tch);
        else           g_kloop<false, 1>(lds, lds0, acc, u1, rat, aoff, boff, ldsw, ws, tag, fk, fatt, lane, wid, tch);
        PROF(4);
        Q1_LOAD();
        G_STAGE(G_SA(1, 1), rat.A + rat.dA1 + rat.dA2, rat.vA0, rat.vA1);
        SBAR();
        g_epilogue(acc, rws, type, pn, pm1, wr, wc, lane);
        PROF(12);
        pf = fk + 32;
        Q1_WAIT(18);
    } else {
        const int pm = x8, pn = kk - 24, grp = wid >> 2;
        constexpr unsigned SLA = SEQ * 2, SLB = 768 * 2, WROW = FF * 2;
        constexpr int RB = 65536, RA2 = 131072;
        const char* Ap = (const char*)ws + WS_XN + (size_t)pm * 256 * LDA;
        const char* Bp = (const char*)w_in + pn * 256;
        unsigned vA0 = arow * LDA + achk, vA1 = vA0 + 64 * LDA, vB0 = brow * WROW + bchk, vB1 = vB0 + 32 * WROW, dA = 128 * LDA, dB = 64 * WROW;
        const int tb0 = 8 * pm + 2 * (wid & 3), fs0 = 8 * pn + 4 * grp;
        float bias[2];
        const int flz = F_L + (4 * x8 + (pn >> 1)) * 2 + (kk & 1);
        {
            f32x16 za[3];
            const int rg = 4 * x8 + (pn >> 1), hf = kk & 1, m0_ = wid & 1;
#pragma unroll
            for (int m = 0; m < 2; ++m) bias[m] = __uint_as_float((unsigned)bsp[(tb0 + m) * 32 + r] << 16);
            z2_half(lds, lds0, za, x, w_in, ws + (F_X + 8 * pm) * 8, rg, hf, tid, wid, lane
                );
            PIN(bias[0]); PIN(bias[1]);
            PROF(3);
            MAKE_TAG();
            if (wid == 0 && kk < 30) {
                const unsigned w = *(const LAS unsigned*)(lds + LDS_PARK + lane * 4);
                if (!__all(lane >= 16 || w == ((lane & 1) ? (unsigned)(tag >> 32) : (unsigned)tag))) poll_flags(ws, F_X + 8 * pm + (lane & 7), true, tag);
                ACQUIRE();
            }
            {
                const char* slab = (const char*)wsp + (size_t)x8 * 1048576 + pn * 131072;
                TOUCH_S(tid * 128, slab, lds0); TOUCH_S(tid * 128 + 65536, slab, lds0);
            }
            u32x4 zp[3][2];
#pragma unroll
            for (int i = 0; i < 3; ++i) { zp[i][0] = gelu8(za[i], 0); zp[i][1] = gelu8(za[i], 1); }
            const int nx = wid < 4 ? 8 : wid < 6 ? 6 : 0;
            LAS float* red = (LAS float*)lds;
            float s[2] = {0.f, 0.f}, mean[2];
#pragma unroll
            for (int i = 0; i < 24; ++i) if (i < 16 || i - 16 < nx) s[(i >> 3) & 1] = sum_pair(zp[i >> 3][(i >> 2) & 1][i & 3], s[(i >> 3) & 1]);
            red[wid * 128 + m0_ * 64 + lane] = s[0]; red[wid * 128 + (m0_ ^ 1) * 64 + lane] = s[1];
            WAIT_L(0); BAR();
#pragma unroll
            for (int m = 0; m < 2; ++m) { float t = 0.f;
#pragma unroll
                for (int w = 0; w < 8; ++w) t += red[w * 128 + (m0_ ^ m) * 64 + lane];
                mean[m] = swap_add(t) * (2.0f / FH); }
            f32x2 d[24], q2[2] = {{0.f, 0.f}, {0.f, 0.f}};
#pragma unroll
            for (int i = 0; i < 24; ++i) { const int m = (i >> 3) & 1; d[i] = unpk(zp[i >> 3][(i >> 2) & 1][i & 3]) - (f32x2){mean[m], mean[m]};
                if (i < 16 || i - 16 < nx) q2[m] = __builtin_elementwise_fma(d[i], d[i], q2[m]); }
            red[1024 + wid * 128 + m0_ * 64 + lane] = q2[0][0] + q2[0][1]; red[1024 + wid * 128 + (m0_ ^ 1) * 64 + lane] = q2[1][0] + q2[1][1];
            GP(9);
            WAIT_V(2); WAIT_L(0); BAR();
            LAS f32x2* tab = (LAS f32x2*)(lds + 160768);
            if (wid == 0) {
#pragma unroll
                for (int m = 0; m < 2; ++m) { float t = 0.f;
#pragma unroll
                    for (int w = 0; w < 8; ++w) t += red[1024 + w * 128 + m * 64 + lane];
                    s[m] = swap_add(t); }
                const float mo = hh ? mean[1] : mean[0], qo = hh ? s[1] : s[0];
                const unsigned t32 = (unsigned)tag;
                st16(rws, WS_G + (rg * 2 + hf) * 1024 + lane * 16, (u32x4){__float_as_uint(mo), t32, __float_as_uint(qo), t32});
                u32x4 o;
#pragma unroll 1
                for (unsigned spins = 0; spins < (1u << 17); ++spins) {
                    o = ld16(rws, WS_G + (rg * 2 + (hf ^ 1)) * 1024 + lane * 16);
                    if (__all(o[1] == t32 && o[3] == t32)) break;
                    __builtin_amdgcn_s_sleep(2);
                }
                const float mp = __uint_as_float(o[0]), dl = mo - mp;
                tab[lane] = (f32x2){0.5f * (mo + mp), 1.0f / sqrtf((qo + __uint_as_float(o[2]) + dl * dl * (FH * 0.25f)) * (1.0f / FH) + 1e-5f)};
            }
            WAIT_L(0); BAR();
            GP(10);
            if (kk < 30) {
                dma_pair<0, 8192>(vA0, vA1, Ap, ldsw); dma_pair<16384, 24576>(vA0, vA1, Ap + 128 * LDA, ldsw); dma_pair<RB, RB + 8192>(vB0, vB1, Bp, ldsw);
                dma_pair<32768, 32768 + 8192>(vA0, vA1, Ap + 128, ldsw); dma_pair<32768 + 16384, 32768 + 24576>(vA0, vA1, Ap + 128 + 128 * LDA, ldsw);
                dma_pair<RB + 16384, RB + 16384 + 8192>(vB0, vB1, Bp + 64 * WROW, ldsw); dma_pair<RB + 32768, RB + 32768 + 8192>(vB0, vB1, Bp + 128 * WROW, ldsw);
            }
            f32x2 rs[2], of[2];
#pragma unroll
            for (int m = 0; m < 2; ++m) { const f32x2 t = tab[(m0_ ^ m) * 32 + r]; const float o = (mean[m] - t[0]) * t[1]; rs[m] = (f32x2){t[1], t[1]}; of[m] = (f32x2){o, o}; }
            const lds_p tr = lds + 114688;
#pragma unroll
            for (int i = 0; i < 12; ++i)
                if (i < 8 || 2 * (i - 8) < nx) {
                    const int b = i >> 2, m = b & 1;
                    *(LAS u32x2*)(tr + ((m0_ ^ m) * 32 + r) * 720 + (b == 2 ? 8 + (wid >> 1) : wid) * 64 + (i & 3) * 16 + hh * 8) =
                        (u32x2){cvt2(__builtin_elementwise_fma(d[2 * i], rs[m], of[m])), cvt2(__builtin_elementwise_fma(d[2 * i + 1], rs[m], of[m]))};
                }
            WAIT_L(0); BAR();
#pragma unroll
            for (int i = 0; i < 6; ++i) { int c = tid + 512 * i; const bool ok = c < 2752; c = ok ? c : 0; const int zr = c / 43, zc = c - zr * 43;
                st16(rws, ok ? WS_Z2N + (rg * 64 + zr) * SLB + hf * 688 + zc * 16 : 0x80000000u, *(const LAS u32x4*)(tr + zr * 720 + zc * 16)); }
#pragma unroll
            for (int i = 0; i < 2; ++i) { const int c = tid + 512 * i, zr = c / 10;
                st16(rws, hf == 1 && c < 640 ? WS_Z2N + (rg * 64 + zr) * SLB + 1376 + (c - zr * 10) * 16 : 0x80000000u, (u32x4){0u, 0u, 0u, 0u}); }
        }
        PROF(1);
        if (kk < 30) {
            u32x4 z1[2][2][2];
#pragma unroll
            for (int k = 0; k < 4; ++k) aoff[k] = ((wid >> 1) & 1) * 16384 + ((wid & 1) * 64 + r) * 128 + (((2 * k + hh) ^ ((r >> 1) & 7)) << 4);
            const int bo0 = RB + (8 * hh + qq) * 256 + (((2 * grp) ^ qq) << 6) + blk * 32 + p * 8, bo1 = RB + (8 * hh + qq) * 256 + (((2 * grp + 1) ^ qq) << 6) + blk * 32 + p * 8;
            f32x16 acc[2][2];
#pragma unroll
            for (int c = 0; c < 4; ++c) acc[c >> 1][c & 1] = f32x16{};
#pragma unroll
            for (int c = 0; c < 8; ++c) z1[c >> 2][(c >> 1) & 1][c & 1] = (u32x4){0u, 0u, 0u, 0u};
            Ap += 256; Bp += 192 * WROW;
            unsigned sa = 0, sa1 = 32768, sa2 = RA2;
            unsigned la = ldsw + RA2, la1 = ldsw, la2 = ldsw + 32768, lb = 49152;
            const char* Aq = Ap; unsigned lq = la, lq1 = la1, lq2 = la2, vQ0 = vA0, vQ1 = vA1, dQ = dA;
#define SP_SRC(s_) do { if ((s_) == 6) { Ap = (const char*)wsp + (size_t)pm * 256 * SLA; vA1 = arow * SLA + achk + 64 * SLA; dA = 128 * SLA; } \
                if ((s_) == 5) { Bp = (const char*)ws + WS_Z2N + pn * 256; vB0 = brow * SLB + bchk; vB1 = vB0 + 32 * SLB; dB = 64 * SLB; } \
                if (t == 6) { Aq = (const char*)wsp + (size_t)pm * 256 * SLA; vQ0 = arow * SLA + achk; vQ1 = vQ0 + 64 * SLA; dQ = 128 * SLA; } } while (0)
#define SP_LATE() do { dma_pair<0, 8192>(vQ0, vQ1, Aq, lq); dma_one<16384>(vQ0, Aq + dQ, lq); Aq += 128; const unsigned l_ = lq; lq = lq1; lq1 = lq2; lq2 = l_; } while (0)
#define SP_A1() dma_one<24576>(vA1, Ap + dA, la)
#define SP_B0() dma_one<RB>(vB0, Bp, ldsw + lb)
#define SP_B1() dma_one<RB + 8192>(vB1, Bp, ldsw + lb)
#define SP_ADV() do { Ap += 128; Bp += dB; const unsigned l_ = la; la = la1; la1 = la2; la2 = l_; lb = (lb + 16384) & 49152; } while (0)
#define SP_GELU() do { _Pragma("unroll") for (int c = 0; c < 4; ++c) { f32x16& a = acc[c >> 1][c & 1]; z1[c >> 1][c & 1][0] = gelu8(a, 0); z1[c >> 1][c & 1][1] = gelu8(a, 1); a = f32x16{}; } } while (0)
#define SP_MMA(k) do { _Pragma("unroll") for (int c = 0; c < 4; ++c) acc[c >> 1][c & 1] = MFMA32(Bt[c & 1][k], At[c >> 1][k], acc[c >> 1][c & 1]); } while (0)
#define SP_STEP(PRE, LATE, WAIT0, WAIT1, D0, D1, D2, POST, LASTBAR) do { \
                bf16x8 At[2][4], Bt[2][4]; \
                PRE; \
                const lds_cp pa = lds + sa, pb = lds + (t & 3) * 16384; \
                _Pragma("unroll") for (int k = 0; k < 4; ++k) { Bt[0][k] = ldtr2(pb + bo0 + k * 4096, 1024); At[0][k] = ld128(pa + aoff[k]); At[1][k] = ld128(pa + aoff[k] + 4096); Bt[1][k] = ldtr2(pb + bo1 + k * 4096, 1024); } \
                LATE; \
                if (grp == 1) { WAIT1; } \
                WAIT_L(0); BAR(); SBAR(); \
                __builtin_amdgcn_s_setprio(1); \
                SP_MMA(0); SBAR(); D0; SBAR(); SP_MMA(1); SBAR(); D1; SBAR(); SP_MMA(2); SBAR(); D2; SBAR(); SP_MMA(3); \
                __builtin_amdgcn_s_setprio(0); \
                SBAR(); \
                POST; \
                if (grp == 0) { WAIT0; } \
                if (LASTBAR) BAR(); \
                const unsigned s_ = sa; sa = sa1; sa1 = sa2; sa2 = s_; } while (0)
            asm volatile("s_waitcnt vmcnt(16) lgkmcnt(0)\n\ts_barrier" ::: "memory");
            if (grp == 1) { SP_A1(); SP_B0(); SP_B1(); SP_ADV(); BAR(); }
            PROF(2);
            int t = 0;
#pragma unroll 1
            for (; t < 8; ++t)
                SP_STEP({ if (wid == 0) { if (t == 2 && lane == 0) { set_flag(ws, flz, tag); set_flag(ws, F_S + bid, tag); } if (t == 3) { poll_flags(ws, F_L + lane, true, tag); ACQUIRE(); } } SP_SRC(t + grp); },
                        SP_LATE(), { if (t == 0) WAIT_V(16); else WAIT_V(8); }, { if (t == 0) WAIT_V(16); else WAIT_V(6); }, SP_A1(), SP_B0(), SP_B1(), { SP_ADV(); if (t == 7 && grp == 1) SP_GELU(); }, true);
            PROF(11);
            if (grp == 0) SP_GELU();
            PROF(12);
#pragma unroll 1
            for (; t < 36; ++t) SP_STEP(NOP_, SP_LATE(), WAIT_V(8), WAIT_V(6), SP_A1(), SP_B0(), SP_B1(), SP_ADV(), true);
#pragma unroll 1
            for (; t < 40; ++t)
                SP_STEP({ if (wid == 0) { SP_CHECK(); } }, if (t < 38) SP_LATE(),
                        { if (t == 36) WAIT_V(8); else if (t == 37) WAIT_V(6); else WAIT_V(0); }, { if (t == 36) WAIT_V(6); else if (t == 37) WAIT_V(4); else WAIT_V(0); },
                        if (t + grp < 38) SP_A1(), if (t + grp < 37) SP_B0(), if (t + grp < 37) SP_B1(), SP_ADV(), !(t == 39 && grp == 1));
            PROF(4);
            Q1_LOAD();
            G_STAGE(G_SA(0, 0), rat.A, rat.vA0, rat.vA1); G_STAGE(G_SB(0, 0), rat.B, rat.vA0, rat.vA1); G_STAGE(G_SA(1, 0), rat.A + 8192, rat.vA0, rat.vA1); G_STAGE(G_SB(1, 0), rat.B + 8192, rat.vA0, rat.vA1);
            G_STAGE(G_SA(0, 1), rat.A + 65536, rat.vA0, rat.vA1); G_STAGE(G_SB(0, 1), rat.B + 65536, rat.vA0, rat.vA1); G_STAGE(G_SA(1, 1), rat.A + 73728, rat.vA0, rat.vA1); G_STAGE(G_SB(1, 1), rat.B + 73728, rat.vA0, rat.vA1);
            Q2_LOAD();
            SBAR();
#pragma unroll
            for (int m = 0; m < 2; ++m)
#pragma unroll
                for (int c = 0; c < 4; ++c) {
                    const f32x16& a = acc[m][c >> 1]; const u32x4 z = z1[m][c >> 1][c & 1]; const int s8 = 8 * (c & 1);
                    u32x4 o;
#pragma unroll
                    for (int e = 0; e < 4; ++e) o[e] = cvtpk(bflo(z[e]) * (a[s8 + 2 * e] + bias[m]), bfhi(z[e]) * (a[s8 + 2 * e + 1] + bias[m]));
                    st16(rws, fs0 + c < 43 ? WS_AF + ((256 + fs0 + c) * 64 + tb0 + m) * 1024 + lane * 16 : 0x80000000u, o);
                }
            pf = F_U + pm * 8 + pn;
            Q1_WAIT(20);
        } else {
            WAIT_V(0); WAIT_L(0); BAR();
            if (tid == 0) { set_flag(ws, flz, tag); set_flag(ws, F_S + bid, tag); }
            {
                const int aw = x8 * 2 + (kk - 30);
#pragma unroll
                for (int i = 0; i < 4; ++i) touch(tid * 128 + i * 65536, (const char*)wo + aw * 262144, lds0);
                if (tid < 344) touch(tid * 128, (const char*)wout + aw * 44032, lds0);
            }
            if (wid == 0) { poll_flags(ws, fatt + (lane & 31), lane < 32 && (lane & 15) < 12, tag); ACQUIRE(); }
            WAIT_V(0); BAR();
            PROF(4);
            g_prologue_b(rat, ldsw); g_prologue_rest(rat, ldsw); G_STAGE(G_SA(1, 1), rat.A + rat.dA1 + rat.dA2, rat.vA0, rat.vA1);
            Q1_LOAD();
            pf = -1;
            Q1_WAIT(0);
        }
    }
    BAR();
    PROF(5);
    {
        const int kh = wid >> 2, rg = wid & 3;
        const bool early = kk < 24 || kk >= 30;
        const rsrc_t rkv = __builtin_amdgcn_make_buffer_rsrc((void*)kvx, (short)0, 4 * 64 * 16384, 0x00020000);
        const unsigned dv0 = rg * 2048 + lane * 16 + kh * 32768, dv1 = dv0 + 8192, dv2 = dv0 + 16384, dv3 = dv0 + 24576;
        const unsigned wrapoff = (unsigned)(h1 * 16 + i1 + 1) * 65536u, wrapto = (unsigned)h2 << 20;
        unsigned sA_ = 0, sB_ = ((unsigned)h1 << 20) + 65536u, lA = lds0 + kh * 65536 + rg * 2048, lB = lA + 16384;
#define A_ADV() do { sA_ = sB_; sB_ += 65536u; if (sB_ == wrapoff) sB_ = wrapto; const unsigned t_ = lA; lA = lB; lB = t_; } while (0)
#define D_KO(h) bdma1<8192, (h) * 1024>(dv2, rkv, sA_, lA)
#define D_VE(h) bdma1<32768, (h) * 1024>(dv1, rkv, sA_, lA)
#define D_KE(h) bdma1<0, (h) * 1024>(dv0, rkv, sB_, lB)
#define D_VO(h) bdma1<40960, (h) * 1024>(dv3, rkv, sA_, lA)
#define PUBLISH() do { if (pf >= 0) { if (tid == 0) set_flag(ws, pf, tag); pf = -1; } } while (0)
        const lds_cp qrd = lds + 131072 + rg * 8192 + lane * 16;
        const lds_cp rbase = lds + kh * 65536 + lane * 16;
        int so = 0;
#pragma unroll 1
        for (int blkno = 0; blkno < 2; ++blkno) {
            const int ib = blkno ? i2 : i1, head = 4 * x8 + (blkno ? h2 : h1);
            const int qrow = 128 * ib + 32 * rg + r - 4 * hh - 64 * kh;
            f32x16 o[4], sA, sB, negm;
            bf16x8 fr[8];
            u32x4 pw0, pw1;
            float mref, l0 = 0.f, l1 = 0.f, resc = 1.0f;
            bool dead = false, pend = false;
            lds_cp pc = rbase + so, pn = rbase + (so ^ 16384);
#pragma unroll
            for (int d = 0; d < 8; ++d) fr[d] = ld128(pc + d * 1024);
            sA = f32x16{};
#pragma unroll
            for (int d = 0; d < 8; ++d) sA = MFMA32(fr[d], q[d], sA);
            asm volatile("s_waitcnt lgkmcnt(0)\n\ts_barrier" ::: "memory");
            A_ADV();
            D_KO(0); D_KO(1);
            if (blkno == 1 && wid < 5) {
                if (wid < 4) touch(kk * 32768 + tid * 128, (const char*)wo + (size_t)(x8 >> 1) * 1048576, lds0);
                else { const unsigned ur = 192 * (x8 >> 1) + 6 * kk + (lane >> 3); touch((ur > FH - 1 ? FH - 1 : ur) * 1024 + (lane & 7) * 128, wout, lds0); }
            }
#pragma unroll
            for (int d = 0; d < 4; ++d) fr[d] = ld128(pc + 8192 + d * 1024);
#pragma unroll
            for (int d = 0; d < 4; ++d) o[d] = f32x16{};
            if (ib == 0) att_mask(sA, qrow);
            {
                float m = MX3(sA[0], sA[1], sA[2]);
#pragma unroll
                for (int e = 3; e < 15; e += 2) m = MX3(m, sA[e], sA[e + 1]);
                m = swap_max(__builtin_fmaxf(m, sA[15]));
                dead = m == -__builtin_inff();
                mref = dead ? 0.f : m;
#pragma unroll
                for (int e = 0; e < 16; ++e) { sA[e] -= mref; negm[e] = -mref; }
                PIN(negm);
#pragma unroll
                for (int e = 0; e < 8; ++e) sA[e] = EX2(sA[e]);
                l0 = sA[0] + sA[2]; l1 = sA[1] + sA[3];
            }
#define A_RD(j, ptr) do { fr[j] = ld128(ptr); } while (0)
#define A_P1(SC, SN, KP, VP, E0, E1, E2) do { \
            A_RD(4, (KP) + 4096); SBAR(); SN = MFMA32(fr[0], q[0], negm); SC[8] = EX2(SC[8]); SC[9] = EX2(SC[9]); pw0[0] = cvtpk(SC[0], SC[1]); PIN(SC); PIN(pw0); SBAR(); \
            A_RD(5, (KP) + 5120); SBAR(); SN = MFMA32(fr[1], q[1], SN); SC[10] = EX2(SC[10]); SC[11] = EX2(SC[11]); pw0[1] = cvtpk(SC[2], SC[3]); PIN(SC); PIN(pw0); E0; SBAR(); \
            A_RD(6, (KP) + 6144); SBAR(); SN = MFMA32(fr[2], q[2], SN); SC[12] = EX2(SC[12]); SC[13] = EX2(SC[13]); pw0[2] = cvtpk(SC[4], SC[5]); PIN(SC); PIN(pw0); SBAR(); \
            A_RD(7, (KP) + 7168); SBAR(); SN = MFMA32(fr[3], q[3], SN); SC[14] = EX2(SC[14]); SC[15] = EX2(SC[15]); pw0[3] = cvtpk(SC[6], SC[7]); PIN(SC); PIN(pw0); SBAR(); \
            A_RD(0, (VP));        SBAR(); SN = MFMA32(fr[4], q[4], SN); l0 += SC[4]; l1 += SC[5]; l0 += SC[6]; l1 += SC[7]; PIN(l0); PIN(l1); E1; SBAR(); \
            A_RD(1, (VP) + 2048); SBAR(); SN = MFMA32(fr[5], q[5], SN); l0 += SC[8]; l1 += SC[9]; pw1[0] = cvtpk(SC[8], SC[9]); PIN(l0); PIN(l1); PIN(pw1); SBAR(); \
            A_RD(2, (VP) + 4096); SBAR(); SN = MFMA32(fr[6], q[6], SN); l0 += SC[10]; l1 += SC[11]; pw1[1] = cvtpk(SC[10], SC[11]); PIN(l0); PIN(l1); PIN(pw1); SBAR(); \
            A_RD(3, (VP) + 6144); SBAR(); SN = MFMA32(fr[7], q[7], SN); E2; SBAR(); } while (0)
#define PWF(x) __builtin_bit_cast(bf16x8, x)
#define A_P2A(SC, SN, VP, MASK, DQ) do { \
            A_RD(4, (VP) + 1024); A_RD(5, (VP) + 3072); SBAR(); o[0] = MFMA32(fr[0], PWF(pw0), o[0]); l0 += SC[12]; l1 += SC[13]; pw1[2] = cvtpk(SC[12], SC[13]); PIN(l0); PIN(l1); PIN(pw1); SBAR(); \
            A_RD(6, (VP) + 5120); A_RD(7, (VP) + 7168); SBAR(); o[1] = MFMA32(fr[1], PWF(pw0), o[1]); l0 += SC[14]; l1 += SC[15]; pw1[3] = cvtpk(SC[14], SC[15]); PIN(l0); PIN(l1); PIN(pw1); SBAR(); \
            float ma_ = 0.f, mb_ = 0.f; \
            o[2] = MFMA32(fr[2], PWF(pw0), o[2]); \
            if (MASK) att_mask(SN, DQ); \
            ma_ = MX3(SN[0], SN[1], SN[2]); mb_ = MX3(SN[3], SN[4], SN[5]); ma_ = MX3(ma_, SN[6], SN[7]); mb_ = MX3(mb_, SN[8], SN[9]); PIN(ma_); PIN(mb_); SBAR(); \
            o[3] = MFMA32(fr[3], PWF(pw0), o[3]); \
            ma_ = MX3(ma_, SN[10], SN[11]); mb_ = MX3(mb_, SN[12], SN[13]); ma_ = MX3(ma_, SN[14], SN[15]); ma_ = __builtin_fmaxf(ma_, mb_); \
            if (__builtin_expect(__any(ma_ > ATT_THR), 0)) {            \
                const float dl_ = __builtin_fmaxf(swap_max(ma_), 0.f); mref += dl_; resc = EX2(-dl_); l0 *= resc; l1 *= resc; pend = true; \
                _Pragma("unroll") for (int e = 0; e < 16; ++e) { SN[e] -= dl_; negm[e] = -mref; } PIN(negm); } SBAR(); } while (0)
#define A_RESC() do { if (__builtin_expect(pend, 0)) { pend = false;         \
            _Pragma("unroll") for (int d = 0; d < 4; ++d) _Pragma("unroll") for (int e = 0; e < 16; ++e) o[d][e] *= resc; } } while (0)
#define A_P2B(SN, KN, D0, D1) do { \
            A_RD(0, (KN));        SBAR(); o[0] = MFMA32(fr[4], PWF(pw1), o[0]); SN[0] = EX2(SN[0]); SN[1] = EX2(SN[1]); PIN(SN); D0; SBAR(); \
            A_RD(1, (KN) + 1024); SBAR(); o[1] = MFMA32(fr[5], PWF(pw1), o[1]); SN[2] = EX2(SN[2]); SN[3] = EX2(SN[3]); PIN(SN); SBAR(); \
            A_RD(2, (KN) + 2048); SBAR(); o[2] = MFMA32(fr[6], PWF(pw1), o[2]); SN[4] = EX2(SN[4]); SN[5] = EX2(SN[5]); l0 += SN[0]; l1 += SN[1]; PIN(SN); PIN(l0); PIN(l1); D1; SBAR(); \
            A_RD(3, (KN) + 3072); SBAR(); o[3] = MFMA32(fr[7], PWF(pw1), o[3]); SN[6] = EX2(SN[6]); SN[7] = EX2(SN[7]); l0 += SN[2]; l1 += SN[3]; PIN(SN); PIN(l0); PIN(l1); SBAR(); \
            A_RESC(); } while (0)
#pragma unroll 1
            for (int n = 0; n < ib; ++n) {
                A_P1(sA, sB, pc + 8192, pc + 32768, D_VE(0), D_VE(1), D_KE(0));
                A_P2A(sA, sB, pc + 32768, false, 0);
                A_P2B(sB, pn, D_KE(1), D_VO(0));
                A_P1(sB, sA, pn, pc + 32768 + 8192, D_VO(1), NOP_, NOP_);
                A_P2A(sB, sA, pc + 32768 + 8192, (n == ib - 1), qrow - 128 * ib);
                const int ev = blkno == 0 && (early || n == 0) ? n : 6;
                if (ev != 0) WAIT_V(0); else if (kk < 24) WAIT_V(24); else WAIT_V(16);
                asm volatile("s_waitcnt lgkmcnt(0)\n\ts_barrier" ::: "memory");
                A_ADV();
                if (ev != 0) PUBLISH();
                if (ev < 6) {
                    if (ev == 3 && wid == 0) dma_words(lane * 4, ws + (size_t)(fatt + 32) * 8, lds0 + 131072);
                    if (ev == 4 && wid == 0) { check_parked<0>(i2 >= 8 ? 8 : 12, lds + 131072, ws, fatt + 32, tag, lane); ACQUIRE(); }
                    if (ev == 5) Q2_LOAD();
                }
                A_P2B(sA, pn + 8192, D_KO(0), D_KO(1));
                so ^= 16384;
                const lds_cp t_ = pc; pc = pn; pn = t_;
            }
            if (blkno == 0) PROF(6);
            A_P1(sA, sB, pc + 8192, pc + 32768, D_VE(0); D_VE(1), D_KE(0); D_KE(1), D_VO(0); D_VO(1));
            if (blkno == 0) {
#pragma unroll
                for (int d = 0; d < 8; ++d) q[d] = ld128(qrd + d * 1024);
            }
            A_P2A(sA, sB, pc + 32768, true, qrow - 128 * ib - 32);
            {
                o[0] = MFMA32(fr[4], PWF(pw1), o[0]); sB[0] = EX2(sB[0]); sB[1] = EX2(sB[1]); PIN(sB); SBAR();
                o[1] = MFMA32(fr[5], PWF(pw1), o[1]); sB[2] = EX2(sB[2]); sB[3] = EX2(sB[3]); PIN(sB); SBAR();
                o[2] = MFMA32(fr[6], PWF(pw1), o[2]); sB[4] = EX2(sB[4]); sB[5] = EX2(sB[5]); l0 += sB[0]; l1 += sB[1]; PIN(sB); SBAR();
                o[3] = MFMA32(fr[7], PWF(pw1), o[3]); sB[6] = EX2(sB[6]); sB[7] = EX2(sB[7]); l0 += sB[2]; l1 += sB[3]; PIN(sB); SBAR();
                A_RESC();
            }
            {
                const lds_cp vp = pc + 32768 + 8192;
#pragma unroll
                for (int j = 0; j < 4; ++j) { fr[j] = ld128(vp + j * 2048); fr[4 + j] = ld128(vp + j * 2048 + 1024); }
#pragma unroll
                for (int e = 8; e < 16; ++e) sB[e] = EX2(sB[e]);
#pragma unroll
                for (int e = 4; e < 16; e += 2) { l0 += sB[e]; l1 += sB[e + 1]; }
#pragma unroll
                for (int e = 0; e < 4; ++e) { pw0[e] = cvtpk(sB[2 * e], sB[2 * e + 1]); pw1[e] = cvtpk(sB[8 + 2 * e], sB[9 + 2 * e]); }
                asm volatile("s_waitcnt vmcnt(0) lgkmcnt(0)\n\ts_barrier" ::: "memory");
                PUBLISH();
#pragma unroll
                for (int j = 0; j < 4; ++j) o[j] = MFMA32(fr[j], PWF(pw0), o[j]);
#pragma unroll
                for (int j = 0; j < 4; ++j) o[j] = MFMA32(fr[4 + j], PWF(pw1), o[j]);
            }
            {
                const int sb_own = kh == 0 ? 131072 + rg * 8192 : (rg >> 1) * 65536 + 32768 + so + (rg & 1) * 8192;
                const int sb_par = kh == 1 ? 131072 + rg * 8192 : (rg >> 1) * 65536 + 32768 + so + (rg & 1) * 8192;
                const float mown = dead ? -__builtin_inff() : mref, lown = l0 + l1;
                *(LAS f32x2*)(lds + kh * 65536 + so + 8192 + rg * 512 + lane * 8) = (f32x2){mown, lown};
                const f32x16 s0 = kh ? o[0] : o[2], s1 = kh ? o[1] : o[3], k0 = kh ? o[2] : o[0], k1 = kh ? o[3] : o[1];
#pragma unroll
                for (int qd = 0; qd < 4; ++qd) {
                    *(LAS f32x4*)(lds + sb_own + qd * 1024 + lane * 16) = (f32x4){s0[4 * qd], s0[4 * qd + 1], s0[4 * qd + 2], s0[4 * qd + 3]};
                    *(LAS f32x4*)(lds + sb_own + 4096 + qd * 1024 + lane * 16) = (f32x4){s1[4 * qd], s1[4 * qd + 1], s1[4 * qd + 2], s1[4 * qd + 3]};
                }
                asm volatile("s_waitcnt lgkmcnt(0)\n\ts_barrier" ::: "memory");
                const f32x2 ml = *(const LAS f32x2*)(lds + (kh ^ 1) * 65536 + so + 8192 + rg * 512 + lane * 8);
                f32x16 p0, p1;
#pragma unroll
                for (int qd = 0; qd < 4; ++qd) {
                    const f32x4 a = *(const LAS f32x4*)(lds + sb_par + qd * 1024 + lane * 16), b = *(const LAS f32x4*)(lds + sb_par + 4096 + qd * 1024 + lane * 16);
#pragma unroll
                    for (int e = 0; e < 4; ++e) { p0[4 * qd + e] = a[e]; p1[4 * qd + e] = b[e]; }
                }
                const float mf = __builtin_fmaxf(mown, ml[0]);
                const float fo = EX2(mown - mf), fp = EX2(ml[0] - mf);
                const float inv = 1.0f / swap_add(fo * lown + fp * ml[1]);
                const float ca = fo * inv, cb = fp * inv;
#pragma unroll
                for (int e = 0; e < 16; ++e) { p0[e] = ca * k0[e] + cb * p0[e]; p1[e] = ca * k1[e] + cb * p1[e]; }
                const unsigned dst = WS_AF + ((head * 8 + 4 * kh) * 64 + 4 * ib + rg) * 1024 + lane * 16;
                st16(rws, dst, pack8(p0, 0, 1.0f)); st16(rws, dst + 65536, pack8(p0, 1, 1.0f));
                st16(rws, dst + 2 * 65536, pack8(p1, 0, 1.0f)); st16(rws, dst + 3 * 65536, pack8(p1, 1, 1.0f));
                pf = F_C + ib * 32 + head;
            }
            so ^= 16384;
            if (blkno == 0) PROF(7);
        }
        PROF(8);
#undef A_RD
#undef A_P1
#undef A_P2A
#undef A_P2B
#undef PWF
    }

    {
        const int sq = x8 >> 1, tm = (x8 & 1) * 8 + (kk >> 2), tn = kk & 3, rb = wid & 3, kp = wid >> 2;
        OutArgs oa;
        oa.ra = __builtin_amdgcn_make_buffer_rsrc((void*)(ws + WS_AF), (short)0, NKS * 65536, 0x00020000);
        oa.wo = (const char*)wo + (size_t)sq * 1048576 + tn * 256; oa.wu = (const char*)wout + tn * 256;
        oa.wv = brow * 1024 + bchk;
#pragma unroll
        for (int j = 0; j < 6; ++j) { const unsigned ur = 192 * sq + 32 * j + brow; oa.uv[j] = (ur > FH - 1 ? FH - 1 : ur) * 1024 + bchk; }
        oa.va = kp * 131072 + (tm * 4 + rb) * 1024 + lane * 16; oa.sc = sq * 64 * 65536; oa.su = (256 + 12 * sq) * 65536; oa.ldsw = ldsw;
#pragma unroll
        for (int c = 0; c < 4; ++c) oa.rd[c] = lds + (kp * 32 + 4 * hh + qq) * 256 + ((c ^ qq) << 6) + blk * 32 + p * 8;
        WAIT_L(0); BAR();
        out_w<0>(oa); out_w<1>(oa); out_w<2>(oa); out_w<3>(oa);
        WAIT_V(8); BAR();
        if (tid == 0) set_flag(ws, pf, tag);
        if (wid == 1) {
            if (bid == 0) {
#pragma unroll 1
                for (int i = 0; i < 4; ++i) poll_flags(ws, F_S + i * 64 + lane, true, tag);
                if (lane == 0) set_flag(ws, F_E, tag);
            }
            poll_flags(ws, lane < 8 ? F_C + tm * 32 + 8 * sq + lane : F_U + (tm >> 1) * 8 + ((3 * sq) >> 1) + (lane & 1), lane < 10, tag);
        }
        out_w<4>(oa); out_w<5>(oa); out_w<6>(oa); out_w<7>(oa);
        WAIT_V(12); BAR();
        PROF(9);
        u32x4 af[6][2];
        bf16x8 wf[2][4];
        f32x16 acc[4] = {};
        out_a<0>(oa, af); out_a<1>(oa, af); out_a<2>(oa, af); out_a<3>(oa, af); out_a<4>(oa, af);
#pragma unroll
        for (int c = 0; c < 4; ++c) wf[0][c] = ldtr2(oa.rd[c], 2048);
#define OS(t) out_step<t>(oa, af, wf, acc)
        OS(0); OS(1); OS(2); OS(3); OS(4); OS(5); OS(6); OS(7); OS(8); OS(9); OS(10); OS(11); OS(12); OS(13); OS(14); OS(15); OS(16); OS(17); OS(18);
#undef OS
        PROF(10);
        const f32x16 s0 = kp ? acc[0] : acc[2], s1 = kp ? acc[1] : acc[3];
        f32x16 k0 = kp ? acc[2] : acc[0], k1 = kp ? acc[3] : acc[1];
#pragma unroll
        for (int g = 0; g < 4; ++g) {
            *(LAS f32x4*)(lds + 16384 + wid * 8192 + g * 1024 + lane * 16) = (f32x4){s0[4 * g], s0[4 * g + 1], s0[4 * g + 2], s0[4 * g + 3]};
            *(LAS f32x4*)(lds + 16384 + wid * 8192 + 4096 + g * 1024 + lane * 16) = (f32x4){s1[4 * g], s1[4 * g + 1], s1[4 * g + 2], s1[4 * g + 3]};
        }
        const size_t orow = (size_t)(tm * 128 + rb * 32 + r) * DM + tn * 128 + kp * 64 + 4 * hh;
        u32x2 xv[8];
        if (rb == sq) {
#pragma unroll
            for (int g = 0; g < 8; ++g) xv[g] = *(const u32x2*)(x + orow + 8 * g);
        }
        WAIT_L(0); BAR();
#pragma unroll
        for (int g = 0; g < 4; ++g) {
            const f32x4 a = *(const LAS f32x4*)(lds + 16384 + (wid ^ 4) * 8192 + g * 1024 + lane * 16), b = *(const LAS f32x4*)(lds + 16384 + (wid ^ 4) * 8192 + 4096 + g * 1024 + lane * 16);
#pragma unroll
            for (int e = 0; e < 4; ++e) { k0[4 * g + e] += a[e]; k1[4 * g + e] += b[e]; }
        }
        const unsigned pb = WS_P + ((tm * 4 + tn) * 4 + rb) * 65536 + kp * 8192 + lane * 16;
        const int fp = F_P + (tm * 4 + tn) * 4;
        if (rb != sq) {
#pragma unroll
            for (int g = 0; g < 4; ++g) {
                st16(rws, pb + sq * 16384 + g * 1024, __builtin_bit_cast(u32x4, (f32x4){k0[4 * g], k0[4 * g + 1], k0[4 * g + 2], k0[4 * g + 3]}));
                st16(rws, pb + sq * 16384 + 4096 + g * 1024, __builtin_bit_cast(u32x4, (f32x4){k1[4 * g], k1[4 * g + 1], k1[4 * g + 2], k1[4 * g + 3]}));
            }
            WAIT_V(0);
        }
        BAR();
        if (tid == 0) set_flag(ws, fp + sq, tag);
        PROF(11);
        if (rb == sq) {
            poll_flags(ws, fp + (lane & 3), (lane & 3) != sq, tag);
            PROF(12);
            u32x4 pp[3][8];
#pragma unroll
            for (int j = 0; j < 3; ++j)
#pragma unroll
                for (int g = 0; g < 8; ++g) pp[j][g] = ld16(rws, pb + ((sq + 1 + j) & 3) * 16384 + g * 1024);
#pragma unroll
            for (int g = 0; g < 8; ++g) {
                const f32x16& k = g < 4 ? k0 : k1; const int e0 = 4 * (g & 3);
                const f32x4 a = __builtin_bit_cast(f32x4, pp[0][g]), b = __builtin_bit_cast(f32x4, pp[1][g]), c = __builtin_bit_cast(f32x4, pp[2][g]);
                const f32x4 t = ((f32x4){k[e0], k[e0 + 1], k[e0 + 2], k[e0 + 3]} + a) + (b + c);
                *(u32x2*)(out + orow + 8 * g) = (u32x2){cvtpk(bflo(xv[g][0]) + t[0], bfhi(xv[g][0]) + t[1]), cvtpk(bflo(xv[g][1]) + t[2], bfhi(xv[g][1]) + t[3])};
            }
        }
    }
    PROF_END(ws);
}

extern "C" void kernel_launch(void* const* d_in, const int* in_sizes, int n_in, void* d_out, int out_size, void* d_ws, size_t ws_size, hipStream_t stream) {
    (void)in_sizes; (void)n_in; (void)out_size; (void)ws_size;
    fused_fwd<<<256, 512, 0, stream>>>((const bf16_t*)d_in[0], (const bf16_t*)d_in[1], (const bf16_t*)d_in[2], (const bf16_t*)d_in[3], (const bf16_t*)d_in[4], (const bf16_t*)d_in[5],
                                       (const bf16_t*)d_in[7], (const bf16_t*)d_in[8], (const bf16_t*)d_in[9], (const bf16_t*)d_in[10], (bf16_t*)d_out, (unsigned char*)d_ws);
}
```

```cpp
#include <hip/hip_runtime.h>
#include <hip/hip_bf16.h>
#include <stdint.h>

#define LAS __attribute__((address_space(3)))
#define DI __device__ __forceinline__
typedef unsigned short bf16_t;
typedef unsigned long long u64;
typedef __attribute__((address_space(1))) u64 gu64;
typedef short bf16x8 __attribute__((ext_vector_type(8)));
typedef short s16x4 __attribute__((ext_vector_type(4)));
typedef __bf16 bf16x2_t __attribute__((ext_vector_type(2)));
typedef float f32x16 __attribute__((ext_vector_type(16)));
typedef float f32x4 __attribute__((ext_vector_type(4)));
typedef float f32x2 __attribute__((ext_vector_type(2)));
typedef unsigned u32x4 __attribute__((ext_vector_type(4)));
typedef unsigned u32x2 __attribute__((ext_vector_type(2)));
typedef LAS unsigned char* lds_p;
typedef const LAS unsigned char* lds_cp;
typedef __amdgpu_buffer_rsrc_t rsrc_t;

constexpr int SEQ = 2048, DM = 512, NH = 32, FH = 688, FF = 1376;
constexpr int NKS = 299;
constexpr int F_E = 0;
constexpr int F_S = 512;
constexpr int F_X = 1024;
constexpr int F_L = 2048;
constexpr int F_U = 3072;
constexpr int F_K = 3584;
constexpr int F_C = 4096;
constexpr int F_P = 5120;
constexpr unsigned WS_G   = 65536;
constexpr unsigned WS_XN  = 131072;
constexpr unsigned WS_QF  = WS_XN + SEQ * DM * 2;
constexpr unsigned WS_KVF = WS_QF + NH * 64 * 8192;
constexpr unsigned WS_Z2N = WS_KVF + NH * 64 * 16384;
constexpr unsigned WS_AF  = WS_Z2N + SEQ * 768 * 2;
constexpr unsigned WS_P   = WS_AF + 304 * 64 * 1024;
constexpr unsigned WS_END = WS_P + 256 * 65536;

#define PROF_DECL
#define PROF(i)
#define PROF_END(wsp)
#define MFMA32(a, b, c) __builtin_amdgcn_mfma_f32_32x32x16_bf16((a), (b), (c), 0, 0, 0)
#define SBAR() __builtin_amdgcn_sched_barrier(0)
#define PIN(x) asm volatile("" : "+v"(x))
#define WAIT_V(n) asm volatile("s_waitcnt vmcnt(" #n ")" ::: "memory")
#define WAIT_L(n) asm volatile("s_waitcnt lgkmcnt(" #n ")" ::: "memory")
#define BAR() asm volatile("s_barrier" ::: "memory")
#define SC1 16

DI unsigned cvtpk(float lo, float hi) { f32x2 v = {lo, hi}; bf16x2_t b = __builtin_convertvector(v, bf16x2_t); return __builtin_bit_cast(unsigned, b); }
DI float bflo(unsigned w) { return __uint_as_float(w << 16); }
DI float bfhi(unsigned w) { return __uint_as_float(w & 0xffff0000u); }
DI u32x4 pack8(const f32x16& a, int s, float sc) {
    u32x4 w;
    w[0] = cvtpk(a[8 * s + 0] * sc, a[8 * s + 1] * sc); w[1] = cvtpk(a[8 * s + 2] * sc, a[8 * s + 3] * sc);
    w[2] = cvtpk(a[8 * s + 4] * sc, a[8 * s + 5] * sc); w[3] = cvtpk(a[8 * s + 6] * sc, a[8 * s + 7] * sc);
    return w;
}
DI bf16x8 ld128(lds_cp p) { return *(const LAS bf16x8*)p; }
typedef short v4i16_t __attribute__((ext_vector_type(4)));
DI s16x4 ldtr(lds_cp p) { return __builtin_bit_cast(s16x4, __builtin_amdgcn_ds_read_tr16_b64_v4i16((LAS v4i16_t*)p)); }
DI bf16x8 ldtr2(lds_cp p, int step) { const s16x4 lo = ldtr(p), hi = ldtr(p + step); return __builtin_shufflevector(lo, hi, 0, 1, 2, 3, 4, 5, 6, 7); }
template <int I0, int I1> DI void dma_pair(unsigned v0, unsigned v1, const void* sbase, unsigned l) {
    asm volatile("s_add_u32 m0, %3, %4\n\ts_nop 0\n\tglobal_load_lds_dwordx4 %0, %2 sc1\n\ts_add_u32 m0, %3, %5\n\ts_nop 0\n\tglobal_load_lds_dwordx4 %1, %2 sc1"
                 :: "v"(v0), "v"(v1), "s"(sbase), "s"(l), "i"(I0), "i"(I1) : "memory", "scc");
}
template <int I, int O> DI void bdma1(unsigned voff, rsrc_t rs, unsigned soff, unsigned l) {
    asm volatile("s_add_u32 m0, %3, %4\n\ts_nop 0\n\tbuffer_load_dwordx4 %0, %1, %2 offen offset:%5 sc1 lds" :: "v"(voff), "s"(rs), "s"(soff), "s"(l), "i"(I), "i"(O) : "memory", "scc");
}
DI void dma_words(unsigned voff, const void* sbase, unsigned l) {
    asm volatile("s_mov_b32 m0, %2\n\ts_nop 0\n\tglobal_load_lds_dword %0, %1 sc1" :: "v"(voff), "s"(sbase), "s"(l) : "memory");
}
DI void st16(rsrc_t rs, unsigned off, u32x4 v) { __builtin_amdgcn_raw_buffer_store_b128(v, rs, off, 0, SC1); }
DI u32x4 ld16(rsrc_t rs, unsigned off) { return __builtin_amdgcn_raw_buffer_load_b128(rs, off, 0, SC1); }
DI float swap_add(float v) { auto r = __builtin_amdgcn_permlane32_swap(__float_as_uint(v), __float_as_uint(v), false, false); return __uint_as_float(r[0]) + __uint_as_float(r[1]); }
DI float swap_max(float v) { auto r = __builtin_amdgcn_permlane32_swap(__float_as_uint(v), __float_as_uint(v), false, false); return __builtin_fmaxf(__uint_as_float(r[0]), __uint_as_float(r[1])); }
DI f32x2 unpk(unsigned w) { return (f32x2){bflo(w), bfhi(w)}; }
DI unsigned cvt2(f32x2 v) { return __builtin_bit_cast(unsigned, __builtin_convertvector(v, bf16x2_t)); }
DI float sum_pair(unsigned w, float acc) { return __builtin_amdgcn_fdot2_f32_bf16(__builtin_bit_cast(bf16x2_t, w), __builtin_bit_cast(bf16x2_t, 0x3f803f80u), acc, false); }
DI unsigned gelu2(float a, float b) {
    const f32x2 v = {a, b};
    const f32x2 t = v * __builtin_elementwise_fma(v * v, (f32x2){-2.302208198f * 0.044715f, -2.302208198f * 0.044715f}, (f32x2){-2.302208198f, -2.302208198f});
    const f32x2 d = (f32x2){__builtin_amdgcn_exp2f(t[0]), __builtin_amdgcn_exp2f(t[1])} + (f32x2){1.0f, 1.0f};
    return cvt2(v * (f32x2){__builtin_amdgcn_rcpf(d[0]), __builtin_amdgcn_rcpf(d[1])});
}
DI u32x4 gelu8(const f32x16& a, int s) { return (u32x4){gelu2(a[8 * s], a[8 * s + 1]), gelu2(a[8 * s + 2], a[8 * s + 3]), gelu2(a[8 * s + 4], a[8 * s + 5]), gelu2(a[8 * s + 6], a[8 * s + 7])}; }
DI void set_flag(unsigned char* ws, int idx, u64 tag) { __hip_atomic_store((gu64*)ws + idx, tag, __ATOMIC_RELAXED, __HIP_MEMORY_SCOPE_AGENT); }
DI void poll_flags(unsigned char* ws, int idx, bool active, u64 tag) {
#pragma unroll 1
    for (unsigned spins = 0; spins < (1u << 17); ++spins) {
        const u64 v = __hip_atomic_load((gu64*)ws + idx, __ATOMIC_RELAXED, __HIP_MEMORY_SCOPE_AGENT);
        if (__all(!active || v == tag)) break;
        __builtin_amdgcn_s_sleep(4);
    }
}
#define ACQUIRE() asm volatile("buffer_inv sc1" ::: "memory")

constexpr int LDS_TAG = 163584, LDS_TOUCH = 163328, LDS_PARK = 163072;
DI void touch(unsigned voff, const void* sbase, unsigned lds0) { dma_words(voff, sbase, lds0 + LDS_TOUCH); }
#define TOUCH_A(v, s, l) touch(v, s, l)
#define TOUCH_S(v, s, l) touch(v, s, l)
#define TOUCH_Z(v, s, l) touch(v, s, l)
template <int I> DI void dma_one(unsigned v0, const void* sbase, unsigned l) {
    asm volatile("s_add_u32 m0, %2, %3\n\ts_nop 0\n\tglobal_load_lds_dwordx4 %0, %1 sc1" :: "v"(v0), "s"(sbase), "s"(l), "i"(I) : "memory", "scc");
}
DI void z2_half(const lds_p lds, const unsigned lds0, f32x16 (&acc)[3], const bf16_t* __restrict__ x, const bf16_t* __restrict__ w_in, const unsigned char* fx, const int rg, const int hf,
                const int tid, const int wid, const int lane
#define GP(i)
                ) {
    constexpr int ZS = 704, ZST = 32 * ZS;
    constexpr unsigned WROW = FF * 2;
    const int row = tid >> 3, seg = tid & 7;
    const u32x4* src = (const u32x4*)(x + (size_t)(rg * 64 + row) * DM) + seg;
    u32x4 v[8];
#pragma unroll
    for (int j = 0; j < 8; ++j) asm volatile("global_load_dwordx4 %0, %1, off offset:%2" : "=v"(v[j]) : "v"(src), "i"(j * 128) : "memory");
    unsigned bv[3];
    const int p2 = wid < 6 ? wid + 16 : wid + 8;
#pragma unroll
    for (int i = 0; i < 3; ++i) { const int c = (i == 2 ? p2 : wid + 8 * i) * 64 + lane, zr = c / 44, zc = c - zr * 44; bv[i] = zr * WROW + (zc > 42 ? 42 : zc) * 16; }
    const unsigned lw = lds0 + 65536 + wid * 1024, lw2 = lds0 + 65536 + p2 * 1024;
    const char* wg = (const char*)w_in + FH * 2 + hf * FH;
#define Z_STAGE(slot, g_) do { dma_pair<(slot) * ZST, (slot) * ZST + 8192>(bv[0], bv[1], (g_), lw); dma_one<(slot) * ZST>(bv[2], (g_), lw2); } while (0)
    BAR();
    Z_STAGE(0, wg); Z_STAGE(1, wg + 32 * WROW); Z_STAGE(2, wg + 64 * WROW); Z_STAGE(3, wg + 96 * WROW);
#pragma unroll
    for (int i = 0; i < 2; ++i) { int c = (2 * wid + i) * 64 + lane; c = c > 895 ? 895 : c; const int zr = c / 7, zc = c - zr * 7; TOUCH_Z(((rg & 3) * 128 + zr) * WROW + (zc == 6 ? 684 : zc * 128), wg, lds0); }
    SBAR();
    GP(5);
    {
        asm volatile("s_waitcnt vmcnt(14)" : "+v"(v[0]), "+v"(v[1]), "+v"(v[2]), "+v"(v[3]), "+v"(v[4]), "+v"(v[5]), "+v"(v[6]), "+v"(v[7]));
        GP(6);
        float s = 0.f, s1 = 0.f;
#pragma unroll
        for (int j = 0; j < 8; ++j) { s = sum_pair(v[j][0], s); s1 = sum_pair(v[j][1], s1); s = sum_pair(v[j][2], s); s1 = sum_pair(v[j][3], s1); }
        s += s1;
        s += __shfl_xor(s, 1); s += __shfl_xor(s, 2); s += __shfl_xor(s, 4);
        const float mean = s * (1.0f / DM);
        f32x2 d[32], q2 = {0.f, 0.f}, q3 = {0.f, 0.f};
#pragma unroll
        for (int i = 0; i < 32; i += 2) { d[i] = unpk(v[i >> 2][i & 3]) - (f32x2){mean, mean}; d[i + 1] = unpk(v[i >> 2][(i & 3) + 1]) - (f32x2){mean, mean};
            q2 = __builtin_elementwise_fma(d[i], d[i], q2); q3 = __builtin_elementwise_fma(d[i + 1], d[i + 1], q3); }
        q2 += q3;
        float q = q2[0] + q2[1];
        q += __shfl_xor(q, 1); q += __shfl_xor(q, 2); q += __shfl_xor(q, 4);
        const float rstd = 1.0f / sqrtf(q * (1.0f / DM) + 1e-5f);
#pragma unroll
        for (int j = 0; j < 8; ++j) {
            u32x4 o;
#pragma unroll
            for (int e = 0; e < 4; ++e) o[e] = cvt2(d[4 * j + e] * (f32x2){rstd, rstd});
            *(LAS u32x4*)(lds + j * 8192 + row * 128 + ((seg ^ ((row >> 1) & 7)) << 4)) = o;
        }
    }
    const int r = lane & 31, hh = lane >> 5, blk = (lane >> 4) & 1, qq = (lane & 15) >> 2, p = lane & 3;
    const int asw = (r >> 1) & 7, oth = (wid & 1) ? -4096 : 4096;
    lds_cp ab[4];
#pragma unroll
    for (int k = 0; k < 4; ++k) ab[k] = lds + (wid & 1) * 4096 + r * 128 + (((2 * k + hh) ^ asw) << 4);
    const lds_cp bb = lds + 65536 + (8 * hh + qq) * ZS + wid * 64 + blk * 32 + p * 8;
    const lds_cp bx = bb + (8 + (wid >> 1) - wid) * 64;
    const bool w3 = wid < 6;
#pragma unroll
    for (int i = 0; i < 3; ++i) acc[i] = f32x16{};
    bf16x8 fa[2][2][2], fb[2][2][2];
#define Z_READ(set, slot, k0, aoff_) do { _Pragma("unroll") for (int k_ = 0; k_ < 2; ++k_) { \
        fa[set][k_][0] = ld128(ab[(k0) + k_] + (aoff_)); fa[set][k_][1] = ld128(ab[(k0) + k_] + oth + (aoff_)); \
        fb[set][k_][0] = ldtr2(bb + (slot) * ZST + k_ * 16 * ZS, 4 * ZS); if (w3) fb[set][k_][1] = ldtr2(bx + (slot) * ZST + k_ * 16 * ZS, 4 * ZS); } } while (0)
#define Z_MMA(set, k_) do { acc[0] = MFMA32(fb[set][k_][0], fa[set][k_][0], acc[0]); acc[1] = MFMA32(fb[set][k_][0], fa[set][k_][1], acc[1]); \
        if (w3) acc[2] = MFMA32(fb[set][k_][1], fa[set][k_][0], acc[2]); } while (0)
#define Z_SYNC(n) asm volatile("s_waitcnt vmcnt(" #n ") lgkmcnt(0)\n\ts_barrier" ::: "memory")
    GP(7);
    Z_SYNC(11);
    GP(8);
    Z_READ(0, 0, 0, 0);
    const char* g = wg + 128 * WROW;
#pragma unroll 1
    for (int kt = 0; kt < 3; ++kt) {
        Z_SYNC(6); Z_READ(1, 1, 2, 0);     SBAR(); Z_MMA(0, 0); SBAR(); Z_STAGE(0, g);             SBAR(); Z_MMA(0, 1); SBAR();
        Z_SYNC(6); Z_READ(0, 2, 0, 8192);  SBAR(); Z_MMA(1, 0); SBAR(); Z_STAGE(1, g + 32 * WROW); SBAR(); Z_MMA(1, 1); SBAR();
        Z_SYNC(6); Z_READ(1, 3, 2, 8192);  SBAR(); Z_MMA(0, 0); SBAR(); Z_STAGE(2, g + 64 * WROW); SBAR(); Z_MMA(0, 1); SBAR();
        Z_SYNC(6); Z_READ(0, 0, 0, 16384); SBAR(); Z_MMA(1, 0); SBAR(); Z_STAGE(3, g + 96 * WROW); SBAR(); Z_MMA(1, 1); SBAR();
        g += 128 * WROW;
#pragma unroll
        for (int k = 0; k < 4; ++k) ab[k] += 16384;
    }
    if (wid == 0) dma_words(lane * 4, fx, lds0 + LDS_PARK);
    Z_SYNC(6); Z_READ(1, 1, 2, 0);    SBAR(); Z_MMA(0, 0); Z_MMA(0, 1); SBAR();
    Z_SYNC(3); Z_READ(0, 2, 0, 8192); SBAR(); Z_MMA(1, 0); Z_MMA(1, 1); SBAR();
    Z_SYNC(0); Z_READ(1, 3, 2, 8192); SBAR(); Z_MMA(0, 0); Z_MMA(0, 1); SBAR();
    Z_SYNC(0);                                Z_MMA(1, 0); Z_MMA(1, 1);
#undef Z_STAGE
#undef Z_READ
#undef Z_MMA
#undef Z_SYNC
}

struct Run { const char* A; const char* B; unsigned dA1, dA2, dB1, dB2, vA0, vA1, vB0, vB1; };
constexpr int HTB = 16384;
constexpr unsigned LDA = DM * 2, LDB = 4096 * 2;
#define G_SA(b, h) (((b) * 2 + (h)) * HTB)
#define G_SB(b, h) ((4 + (b) * 2 + (h)) * HTB)
#define G_STAGE(bufoff, gb, v0, v1) dma_pair<(bufoff), (bufoff) + 8192>((v0), (v1), (gb), ldsw)
#define G_LDA(dst, b, h) do { _Pragma("unroll") for (int m_ = 0; m_ < 2; ++m_) _Pragma("unroll") for (int k_ = 0; k_ < 4; ++k_) dst[m_][k_] = ld128(lds + G_SA(b, h) + aoff[k_] + m_ * 4096); } while (0)
#define G_LDB(dst, b, h) do { _Pragma("unroll") for (int k_ = 0; k_ < 4; ++k_) dst[k_] = ldtr2(lds + G_SB(b, h) + boff + k_ * 4096, 1024); } while (0)
#define G_MMA(ai, bj, At, Bt) do { __builtin_amdgcn_s_setprio(1); _Pragma("unroll") for (int k_ = 0; k_ < 4; ++k_) _Pragma("unroll") for (int m_ = 0; m_ < 2; ++m_) \
        acc[ai][bj][m_] = SWAP ? MFMA32(At[m_][k_], Bt[k_], acc[ai][bj][m_]) : MFMA32(Bt[k_], At[m_][k_], acc[ai][bj][m_]); __builtin_amdgcn_s_setprio(0); } while (0)

DI void g_prologue_b(const Run& u, const unsigned ldsw) {
    G_STAGE(G_SB(0, 0), u.B, u.vB0, u.vB1); G_STAGE(G_SB(0, 1), u.B + u.dB1, u.vB0, u.vB1);
}
DI void g_prologue_rest(const Run& u, const unsigned ldsw) {
    G_STAGE(G_SA(0, 0), u.A, u.vA0, u.vA1); G_STAGE(G_SA(0, 1), u.A + u.dA1, u.vA0, u.vA1);
    G_STAGE(G_SB(1, 0), u.B + u.dB2, u.vB0, u.vB1); G_STAGE(G_SA(1, 0), u.A + u.dA2, u.vA0, u.vA1); G_STAGE(G_SB(1, 1), u.B + u.dB2 + u.dB1, u.vB0, u.vB1);
}
template <int LO, int HI> DI void check_parked(lds_cp parked, unsigned char* ws, const int fidx, const u64 tag, const int lane) {
    const unsigned w = *(const LAS unsigned*)(parked + lane * 4);
    const int sl = (lane >> 1) & 15;
    if (!__all(sl < LO || sl >= HI || w == ((lane & 1) ? (unsigned)(tag >> 32) : (unsigned)tag))) poll_flags(ws, fidx + (lane & 31), lane < 32 && (lane & 15) >= LO && (lane & 15) < HI, tag);
}

template <bool SWAP, int ROUND>
DI void g_kloop(const lds_p lds, const unsigned lds0, f32x16 (&acc)[2][2][2], const Run& cur, const Run& nxt, const int (&aoff)[4], const int boff, const unsigned ldsw,
                unsigned char* ws, const u64 tag, const int fl0, const int fatt, const int lane, const int wid, const unsigned tch) {
    bf16x8 At[2][4], B0[4], B1[4];
#pragma unroll
    for (int i = 0; i < 8; ++i) acc[i >> 2][(i >> 1) & 1][i & 1] = f32x16{};
#pragma unroll 1
    for (int t = 0; t < 8; t += 2) {
        const bool last = (t == 6);
        const char* a1 = cur.A + (t + 1) * 128 + 128 * LDA;
        const char* a2 = last ? nxt.A : cur.A + (t + 2) * 128;
        const char* b2 = last ? nxt.B : cur.B + (size_t)(t + 2) * 64 * LDB;
        const unsigned dA1 = last ? nxt.dA1 : cur.dA1, dA2 = last ? nxt.dA2 : cur.dA2, dB1 = last ? nxt.dB1 : cur.dB1, dB2 = last ? nxt.dB2 : cur.dB2;
        const unsigned vA0 = last ? nxt.vA0 : cur.vA0, vA1 = last ? nxt.vA1 : cur.vA1, vB0 = last ? nxt.vB0 : cur.vB0, vB1 = last ? nxt.vB1 : cur.vB1;
        if (ROUND == 1 && last && wid == 0) dma_words(lane * 4, ws + (size_t)fatt * 8, lds0 + 131072);
        if (t == 2 && wid == 0 && lane == 0) set_flag(ws, fl0, tag);
        G_LDB(B0, 0, 0); SBAR(); G_LDA(At, 0, 0); if (!(ROUND == 1 && t == 0)) G_STAGE(G_SA(1, 1), a1, cur.vA0, cur.vA1);
        if (ROUND == 0 && t == 2) touch(tch, nxt.A, lds0);
        if (ROUND == 1 && t == 4 && wid == 0) { check_parked<0, 8>(lds + 131072, ws, fatt, tag, lane); ACQUIRE(); }
        WAIT_L(8); BAR(); WAIT_L(0); SBAR(); G_MMA(0, 0, At, B0); SBAR(); BAR();
        G_LDB(B1, 0, 1);
        G_STAGE(G_SB(0, 0), b2, vB0, vB1);
        BAR(); WAIT_L(0); SBAR(); G_MMA(0, 1, At, B1); SBAR(); BAR();
        G_LDA(At, 0, 1); G_STAGE(G_SA(0, 0), a2, vA0, vA1);
        BAR(); WAIT_L(0); SBAR(); G_MMA(1, 0, At, B0); SBAR(); BAR();
        G_STAGE(G_SB(0, 1), b2 + dB1, vB0, vB1);
        if (ROUND == 1 && t == 0) WAIT_V(22); else if ((ROUND == 1 && t == 4 && wid == 0) || (ROUND == 0 && t == 2)) WAIT_V(7); else WAIT_V(6);
        BAR(); SBAR(); G_MMA(1, 1, At, B1); SBAR(); BAR();
        if (ROUND == 1 && wid == 0) {
            if (t == 2) dma_words(lane * 4, ws + (size_t)fatt * 8, lds0 + 131072);
            if (last) check_parked<8, 12>(lds + 131072, ws, fatt, tag, lane);
        }
        G_LDB(B0, 1, 0); SBAR(); G_LDA(At, 1, 0); G_STAGE(G_SA(0, 1), a2 + dA1, vA0, vA1);
        WAIT_L(8); BAR(); WAIT_L(0); SBAR(); G_MMA(0, 0, At, B0); SBAR(); BAR();
        G_LDB(B1, 1, 1); G_STAGE(G_SB(1, 0), b2 + dB2, vB0, vB1);
        BAR(); WAIT_L(0); SBAR(); G_MMA(0, 1, At, B1); SBAR(); BAR();
        G_LDA(At, 1, 1); G_STAGE(G_SA(1, 0), a2 + dA2, vA0, vA1);
        BAR(); WAIT_L(0); SBAR(); G_MMA(1, 0, At, B0); SBAR(); BAR();
        G_STAGE(G_SB(1, 1), b2 + dB2 + dB1, vB0, vB1);
        if (!(ROUND == 1 && last)) WAIT_V(6);
        BAR(); SBAR(); G_MMA(1, 1, At, B1); SBAR();
        if (!(last && wid >= 4)) BAR();
    }
}

DI void g_epilogue(const f32x16 (&acc)[2][2][2], const rsrc_t rws, const int type, const int pn, const int pm, const int wr, const int wc, const int lane) {
    constexpr float QS = 0.08838834764831845f * 1.4426950408889634f;
    const float sc = type == 2 ? QS : 1.0f;
    const unsigned base = (type == 2 ? WS_QF : WS_KVF + (type == 1 ? 8192 : 0)) + wc * 2048 + lane * 16;
    const unsigned hstride = type == 2 ? 8192 : 16384;
#pragma unroll
    for (int ai = 0; ai < 2; ++ai)
#pragma unroll
        for (int bj = 0; bj < 2; ++bj)
#pragma unroll
            for (int m = 0; m < 2; ++m) {
                const unsigned dst = base + (unsigned)((2 * pn + bj) * 64 + pm * 8 + ai * 4 + wr * 2 + m) * hstride;
                st16(rws, dst, pack8(acc[ai][bj][m], 0, sc)); st16(rws, dst + 1024, pack8(acc[ai][bj][m], 1, sc));
            }
}

#define NOP_ (void)0
#define SP_CHECK() if (t == 36) { poll_flags(ws, fatt + lane, (lane & 15) < 12, tag); ACQUIRE(); }
#define EX2(v) __builtin_amdgcn_exp2f(v)
#define MX3(a, b, c) __builtin_fmaxf(__builtin_fmaxf((a), (b)), (c))
constexpr float ATT_THR = 32.0f;
DI void att_mask(f32x16& s, int dq) {
#pragma unroll
    for (int e = 0; e < 16; ++e) if ((e & 3) + 8 * (e >> 2) > dq) s[e] = -__builtin_inff();
}

struct OutArgs { rsrc_t ra; const char* wo; const char* wu; lds_cp rd[4]; unsigned wv, uv[6], va, sc, su, ldsw; };
template <int T> DI void out_w(const OutArgs& o) {
    constexpr int S = (T % 9) * 16384;
    if constexpr (T < 16) dma_pair<S, S + 8192>(o.wv, o.wv + 32768, o.wo + T * 65536, o.ldsw);
    else if constexpr (T < 19) dma_pair<S, S + 8192>(o.uv[2 * (T - 16)], o.uv[2 * (T - 16) + 1], o.wu, o.ldsw);
}
template <int T> DI void out_a(const OutArgs& o, u32x4 (&af)[6][2]) {
    if constexpr (T < 19) {
        const unsigned so = T < 16 ? o.sc + T * 262144 : o.su + (T - 16) * 262144;
        asm volatile("buffer_load_dwordx4 %0, %1, %2, %3 offen sc1" : "=v"(af[T % 6][0]) : "v"(o.va), "s"(o.ra), "s"(so) : "memory");
        asm volatile("buffer_load_dwordx4 %0, %1, %2, %3 offen sc1" : "=v"(af[T % 6][1]) : "v"(o.va), "s"(o.ra), "s"(so + 65536) : "memory");
    }
}
constexpr int out_cnt(int i) { return (i + 8 < 19 ? 2 : 0) + (i + 5 < 19 ? 2 : 0); }
constexpr int out_allow(int t) { int n = t < 4 ? 2 * (4 - t) : 0; for (int i = t < 4 ? 0 : t - 4; i < t; ++i) n += out_cnt(i); return n; }
template <int T> DI void out_step(const OutArgs& o, u32x4 (&af)[6][2], bf16x8 (&wf)[2][4], f32x16 (&acc)[4]) {
    asm volatile("s_waitcnt vmcnt(%2)\n\ts_barrier" : "+v"(af[T % 6][0]), "+v"(af[T % 6][1]) : "n"(out_allow(T)) : "memory");
    const bf16x8 a0 = __builtin_bit_cast(bf16x8, af[T % 6][0]), a1 = __builtin_bit_cast(bf16x8, af[T % 6][1]);
#pragma unroll
    for (int c = 0; c < 4; ++c) wf[1][c] = ldtr2(o.rd[c] + (T % 9) * 16384 + 4096, 2048);
    SBAR();
    acc[0] = MFMA32(wf[0][0], a0, acc[0]); acc[1] = MFMA32(wf[0][1], a0, acc[1]); SBAR();
    out_w<T + 8>(o); SBAR();
    acc[2] = MFMA32(wf[0][2], a0, acc[2]); acc[3] = MFMA32(wf[0][3], a0, acc[3]); SBAR();
    if constexpr (T < 18) {
#pragma unroll
        for (int c = 0; c < 4; ++c) wf[0][c] = ldtr2(o.rd[c] + ((T + 1) % 9) * 16384, 2048);
    }
    SBAR();
    acc[0] = MFMA32(wf[1][0], a1, acc[0]); acc[1] = MFMA32(wf[1][1], a1, acc[1]); SBAR();
    out_a<T + 5>(o, af); SBAR();
    acc[2] = MFMA32(wf[1][2], a1, acc[2]); acc[3] = MFMA32(wf[1][3], a1, acc[3]); SBAR();
}

#define IT(h1, i1, h2, i2) ((h1) | (i1) << 2 | (h2) << 6 | (i2) << 8)
__device__ constexpr unsigned ITEMS[32] = {
    IT(0, 14, 0, 4), IT(1, 14, 1, 4), IT(2, 14, 2, 4), IT(3, 14, 3, 4), IT(0, 13, 0, 5), IT(1, 13, 1, 5), IT(2, 13, 2, 5), IT(3, 13, 3, 5),
    IT(0, 12, 0, 6), IT(1, 12, 1, 6), IT(2, 12, 2, 6), IT(3, 12, 3, 6), IT(0, 11, 0, 7), IT(1, 11, 1, 7), IT(2, 11, 2, 7), IT(3, 11, 3, 7),
    IT(0, 10, 0, 8), IT(1, 10, 1, 8), IT(2, 10, 2, 8), IT(3, 10, 3, 8), IT(0, 9, 1, 9), IT(2, 9, 3, 9), IT(0, 15, 2, 2), IT(1, 15, 3, 2),
    IT(0, 3, 0, 0), IT(1, 3, 1, 0), IT(2, 3, 2, 0), IT(3, 3, 3, 0), IT(0, 2, 0, 1), IT(1, 2, 1, 1), IT(2, 15, 2, 1), IT(3, 15, 3, 1)
};

__global__ __launch_bounds__(512, 2) void fused_fwd(const bf16_t* __restrict__ x, const bf16_t* __restrict__ ckv, const bf16_t* __restrict__ wq, const bf16_t* __restrict__ wk,
                                                    const bf16_t* __restrict__ wv, const bf16_t* __restrict__ wo, const bf16_t* __restrict__ w_in, const bf16_t* __restrict__ wsp,
                                                    const bf16_t* __restrict__ bsp, const bf16_t* __restrict__ wout, bf16_t* __restrict__ out, unsigned char* ws) {
    __shared__ __attribute__((aligned(16))) unsigned char smem[163840];
    const lds_p lds = (lds_p)smem;
    const unsigned lds0 = (unsigned)(uintptr_t)smem;
    const int tid = threadIdx.x, wid = __builtin_amdgcn_readfirstlane(tid >> 6), lane = tid & 63;
    const int bid = blockIdx.x, x8 = bid & 7, kk = bid >> 3;
    const int r = lane & 31, hh = lane >> 5, blk = (lane >> 4) & 1, qq = (lane & 15) >> 2, p = lane & 3;
    const rsrc_t rws = __builtin_amdgcn_make_buffer_rsrc((void*)ws, (short)0, WS_END, 0x00020000);
    const unsigned ldsw = lds0 + (unsigned)wid * 1024u;
    PROF_DECL;
    dma_words(lane * 4, ws, lds0 + LDS_TAG);
    u64 tag;
#define MAKE_TAG() do { const u32x2 e_ = *(const LAS u32x2*)(lds + LDS_TAG); \
        const u64 ep_ = ((u64)(unsigned)__builtin_amdgcn_readfirstlane(e_[1]) << 32) | (unsigned)__builtin_amdgcn_readfirstlane(e_[0]); \
        u64 z_ = ep_ + 0x9E3779B97F4A7C15ull; z_ = (z_ ^ (z_ >> 30)) * 0xBF58476D1CE4E5B9ull; z_ = (z_ ^ (z_ >> 27)) * 0x94D049BB133111EBull; z_ ^= z_ >> 31; tag = z_ == ep_ ? ~z_ : z_; } while (0)
    const unsigned item = ITEMS[kk];
    const int h1 = item & 3, i1 = (item >> 2) & 15, h2 = (item >> 6) & 3, i2 = (item >> 8) & 15;
    const unsigned char* kvx = ws + WS_KVF + (size_t)x8 * 4 * 64 * 16384;
    const unsigned char* kvh = kvx + (size_t)h1 * 64 * 16384;
    const char* q1src = (const char*)ws + WS_QF + (size_t)((4 * x8 + h1) * 64 + 4 * i1 + (wid & 3)) * 8192;
    bf16x8 q[8];
#define Q1_LOAD() do { _Pragma("unroll") for (int d = 0; d < 8; ++d) asm volatile("global_load_dwordx4 %0, %1, %2 offset:%3 sc1" : "=v"(q[d]) : "v"(lane * 16 + (d >> 2) * 4096), "s"(q1src), "i"((d & 3) * 1024) : "memory"); } while (0)
#define Q1_WAIT(n) asm volatile("s_waitcnt vmcnt(" #n ")" : "+v"(q[0]), "+v"(q[1]), "+v"(q[2]), "+v"(q[3]), "+v"(q[4]), "+v"(q[5]), "+v"(q[6]), "+v"(q[7]) :: "memory")
    const int fatt = F_K + x8 * 64;
#define Q2_LOAD() do { const char* q2src = (const char*)ws + WS_QF + (size_t)((4 * x8 + h2) * 64 + 4 * i2) * 8192 + wid * 4096; \
        const unsigned lq_ = __builtin_amdgcn_readfirstlane(lds0 + wid * 4096); \
        dma_pair<131072, 131072 + 1024>(lane * 16, lane * 16 + 1024, q2src, lq_); dma_pair<131072 + 2048, 131072 + 3072>(lane * 16 + 2048, lane * 16 + 3072, q2src, lq_); } while (0)
    Run rat;
    rat.A = (const char*)kvh; rat.B = (const char*)kvh + 32768; rat.dA1 = rat.dB1 = 65536; rat.dA2 = rat.dB2 = 8192;
    rat.vA0 = rat.vB0 = wid * 1024 + lane * 16; rat.vA1 = rat.vB1 = rat.vA0 + 16384;
    int pf;
    int aoff[4];
    const unsigned arow = 8 * wid + (lane >> 3), achk = ((lane & 7) ^ (((wid & 1) << 2) + (lane >> 4))) << 4;
    const unsigned brow = 4 * wid + (lane >> 4), bchk = ((lane & 15) ^ (((lane >> 4) & 3) << 2)) << 4;

    if (kk < 24) {
        const int type = kk >> 3, pn = 2 * x8 + ((kk >> 2) & 1), pm0 = (kk & 3) + (type == 2 ? 4 : 0), pm1 = pm0 ^ 4, wr = wid >> 2, wc = wid & 3;
        Run u0, u1;
        const char* Ab = type == 2 ? (const char*)ws + WS_XN : (const char*)ckv;
        u0.B = (const char*)(type == 0 ? wk : type == 1 ? wv : wq) + pn * 512; u0.A = Ab + (size_t)pm0 * 256 * LDA;
        u0.dA1 = 128 * LDA; u0.dA2 = 128; u0.dB1 = 256; u0.dB2 = 64 * LDB;
        u0.vA0 = arow * LDA + achk; u0.vA1 = u0.vA0 + 64 * LDA; u0.vB0 = brow * LDB + bchk; u0.vB1 = u0.vB0 + 32 * LDB;
        u1 = u0; u1.A = Ab + (size_t)pm1 * 256 * LDA;
        const unsigned tch = (type == 2 ? ((kk >> 2) & 1) * 2 : type * 2 + ((kk >> 2) & 1)) * 65536 + tid * 128, bl = ((kk & 3) * 128 + wid * 16 + (lane >> 2)) * LDB + (lane & 3) * 128;
        if (type < 2) {
            g_prologue_b(u0, ldsw); g_prologue_rest(u0, ldsw);
            PROF(1);
            WAIT_V(6);
            MAKE_TAG();
        } else {
            const int j = x8 * 8 + (kk - 16), row = 32 * j + 4 * wid + (lane >> 4);
            const u32x4* src = (const u32x4*)(x + (size_t)row * DM) + (lane & 15);
            u32x4 v[4];
#pragma unroll
            for (int i = 0; i < 4; ++i) v[i] = src[16 * i];
            SBAR();
            g_prologue_b(u0, ldsw); touch(bl, u0.B, lds0);
            SBAR();
            float s = 0.f;
#pragma unroll
            for (int i = 0; i < 4; ++i)
#pragma unroll
                for (int e = 0; e < 4; ++e) s += bflo(v[i][e]) + bfhi(v[i][e]);
            s += __shfl_xor(s, 1); s += __shfl_xor(s, 2); s += __shfl_xor(s, 4); s += __shfl_xor(s, 8);
            const float mean = s * (1.0f / DM);
            float q = 0.f;
#pragma unroll
            for (int i = 0; i < 4; ++i)
#pragma unroll
                for (int e = 0; e < 4; ++e) { const float a = bflo(v[i][e]) - mean, b = bfhi(v[i][e]) - mean; q += a * a + b * b; }
            q += __shfl_xor(q, 1); q += __shfl_xor(q, 2); q += __shfl_xor(q, 4); q += __shfl_xor(q, 8);
            const float rstd = 1.0f / sqrtf(q * (1.0f / DM) + 1e-5f);
#pragma unroll
            for (int i = 0; i < 4; ++i) {
                u32x4 o;
#pragma unroll
                for (int e = 0; e < 4; ++e) o[e] = cvtpk((bflo(v[i][e]) - mean) * rstd, (bfhi(v[i][e]) - mean) * rstd);
                st16(rws, WS_XN + row * 1024 + ((lane & 15) + 16 * i) * 16, o);
            }
            WAIT_V(0);
            MAKE_TAG();
            BAR();
            if (tid == 0) set_flag(ws, F_X + j, tag);
            if (wid == 0) { poll_flags(ws, F_X + ((lane & 8) ? pm1 : pm0) * 8 + (lane & 7), lane < 16, tag); ACQUIRE(); WAIT_V(0); }
            BAR();
            g_prologue_rest(u0, ldsw);
            PROF(1);
            WAIT_V(6);
        }
#pragma unroll
        for (int k = 0; k < 4; ++k) aoff[k] = (wr * 64 + r) * 128 + (((2 * k + hh) ^ ((r >> 1) & 7)) << 4);
        const int boff = (8 * hh + qq) * 256 + ((wc ^ qq) << 6) + blk * 32 + p * 8;
        BAR();
        if (wr == 1) BAR();
        PROF(2);
        f32x16 acc[2][2][2];
        const int fk = fatt + (pn & 1) * 16 + type * 4 + (kk & 3);
        if (type == 1) g_kloop<true, 0>(lds, lds0, acc, u0, u1, aoff, boff, ldsw, ws, tag, F_S + bid, fatt, lane, wid, tch);
        else           g_kloop<false, 0>(lds, lds0, acc, u0, u1, aoff, boff, ldsw, ws, tag, F_S + bid, fatt, lane, wid, tch);
        PROF(3);
        G_STAGE(G_SA(1, 1), u1.A + 128 + 128 * LDA, u1.vA0, u1.vA1);
        SBAR();
        g_epilogue(acc, rws, type, pn, pm0, wr, wc, lane);
        PROF(11);
        if (wr == 1) BAR();
        if (type == 1) g_kloop<true, 1>(lds, lds0, acc, u1, rat, aoff, boff, ldsw, ws, tag, fk, fatt, lane, wid, tch);
        else           g_kloop<false, 1>(lds, lds0, acc, u1, rat, aoff, boff, ldsw, ws, tag, fk, fatt, lane, wid, tch);
        PROF(4);
        Q1_LOAD();
        G_STAGE(G_SA(1, 1), rat.A + rat.dA1 + rat.dA2, rat.vA0, rat.vA1);
        SBAR();
        g_epilogue(acc, rws, type, pn, pm1, wr, wc, lane);
        PROF(12);
        pf = fk + 32;
        Q1_WAIT(18);
    } else {
        const int pm = x8, pn = kk - 24, grp = wid >> 2;
        constexpr unsigned SLA = SEQ * 2, SLB = 768 * 2, WROW = FF * 2;
        constexpr int RB = 65536, RA2 = 131072;
        const char* Ap = (const char*)ws + WS_XN + (size_t)pm * 256 * LDA;
        const char* Bp = (const char*)w_in + pn * 256;
        unsigned vA0 = arow * LDA + achk, vA1 = vA0 + 64 * LDA, vB0 = brow * WROW + bchk, vB1 = vB0 + 32 * WROW, dA = 128 * LDA, dB = 64 * WROW;
        const int tb0 = 8 * pm + 2 * (wid & 3), fs0 = 8 * pn + 4 * grp;
        float bias[2];
        const int flz = F_L + (4 * x8 + (pn >> 1)) * 2 + (kk & 1);
        {
            f32x16 za[3];
            const int rg = 4 * x8 + (pn >> 1), hf = kk & 1, m0_ = wid & 1;
#pragma unroll
            for (int m = 0; m < 2; ++m) bias[m] = __uint_as_float((unsigned)bsp[(tb0 + m) * 32 + r] << 16);
            z2_half(lds, lds0, za, x, w_in, ws + (F_X + 8 * pm) * 8, rg, hf, tid, wid, lane
                );
            PIN(bias[0]); PIN(bias[1]);
            PROF(3);
            MAKE_TAG();
            if (wid == 0 && kk < 30) {
                const unsigned w = *(const LAS unsigned*)(lds + LDS_PARK + lane * 4);
                if (!__all(lane >= 16 || w == ((lane & 1) ? (unsigned)(tag >> 32) : (unsigned)tag))) poll_flags(ws, F_X + 8 * pm + (lane & 7), true, tag);
                ACQUIRE();
            }
            {
                const char* slab = (const char*)wsp + (size_t)x8 * 1048576 + pn * 131072;
                TOUCH_S(tid * 128, slab, lds0); TOUCH_S(tid * 128 + 65536, slab, lds0);
            }
            u32x4 zp[3][2];
#pragma unroll
            for (int i = 0; i < 3; ++i) { zp[i][0] = gelu8(za[i], 0); zp[i][1] = gelu8(za[i], 1); }
            const int nx = wid < 4 ? 8 : wid < 6 ? 6 : 0;
            LAS float* red = (LAS float*)lds;
            float s[2] = {0.f, 0.f}, mean[2];
#pragma unroll
            for (int i = 0; i < 24; ++i) if (i < 16 || i - 16 < nx) s[(i >> 3) & 1] = sum_pair(zp[i >> 3][(i >> 2) & 1][i & 3], s[(i >> 3) & 1]);
            red[wid * 128 + m0_ * 64 + lane] = s[0]; red[wid * 128 + (m0_ ^ 1) * 64 + lane] = s[1];
            WAIT_L(0); BAR();
#pragma unroll
            for (int m = 0; m < 2; ++m) { float t = 0.f;
#pragma unroll
                for (int w = 0; w < 8; ++w) t += red[w * 128 + (m0_ ^ m) * 64 + lane];
                mean[m] = swap_add(t) * (2.0f / FH); }
            f32x2 d[24], q2[2] = {{0.f, 0.f}, {0.f, 0.f}};
#pragma unroll
            for (int i = 0; i < 24; ++i) { const int m = (i >> 3) & 1; d[i] = unpk(zp[i >> 3][(i >> 2) & 1][i & 3]) - (f32x2){mean[m], mean[m]};
                if (i < 16 || i - 16 < nx) q2[m] = __builtin_elementwise_fma(d[i], d[i], q2[m]); }
            red[1024 + wid * 128 + m0_ * 64 + lane] = q2[0][0] + q2[0][1]; red[1024 + wid * 128 + (m0_ ^ 1) * 64 + lane] = q2[1][0] + q2[1][1];
            GP(9);
            WAIT_V(2); WAIT_L(0); BAR();
            LAS f32x2* tab = (LAS f32x2*)(lds + 160768);
            if (wid == 0) {
#pragma unroll
                for (int m = 0; m < 2; ++m) { float t = 0.f;
#pragma unroll
                    for (int w = 0; w < 8; ++w) t += red[1024 + w * 128 + m * 64 + lane];
                    s[m] = swap_add(t); }
                const float mo = hh ? mean[1] : mean[0], qo = hh ? s[1] : s[0];
                const unsigned t32 = (unsigned)tag;
                st16(rws, WS_G + (rg * 2 + hf) * 1024 + lane * 16, (u32x4){__float_as_uint(mo), t32, __float_as_uint(qo), t32});
                u32x4 o;
#pragma unroll 1
                for (unsigned spins = 0; spins < (1u << 17); ++spins) {
                    o = ld16(rws, WS_G + (rg * 2 + (hf ^ 1)) * 1024 + lane * 16);
                    if (__all(o[1] == t32 && o[3] == t32)) break;
                    __builtin_amdgcn_s_sleep(2);
                }
                const float mp = __uint_as_float(o[0]), dl = mo - mp;
                tab[lane] = (f32x2){0.5f * (mo + mp), 1.0f / sqrtf((qo + __uint_as_float(o[2]) + dl * dl * (FH * 0.25f)) * (1.0f / FH) + 1e-5f)};
            }
            WAIT_L(0); BAR();
            GP(10);
            if (kk < 30) {
                dma_pair<0, 8192>(vA0, vA1, Ap, ldsw); dma_pair<16384, 24576>(vA0, vA1, Ap + 128 * LDA, ldsw); dma_pair<RB, RB + 8192>(vB0, vB1, Bp, ldsw);
                dma_pair<32768, 32768 + 8192>(vA0, vA1, Ap + 128, ldsw); dma_pair<32768 + 16384, 32768 + 24576>(vA0, vA1, Ap + 128 + 128 * LDA, ldsw);
                dma_pair<RB + 16384, RB + 16384 + 8192>(vB0, vB1, Bp + 64 * WROW, ldsw); dma_pair<RB + 32768, RB + 32768 + 8192>(vB0, vB1, Bp + 128 * WROW, ldsw);
            }
            f32x2 rs[2], of[2];
#pragma unroll
            for (int m = 0; m < 2; ++m) { const f32x2 t = tab[(m0_ ^ m) * 32 + r]; const float o = (mean[m] - t[0]) * t[1]; rs[m] = (f32x2){t[1], t[1]}; of[m] = (f32x2){o, o}; }
            const lds_p tr = lds + 114688;
#pragma unroll
            for (int i = 0; i < 12; ++i)
                if (i < 8 || 2 * (i - 8) < nx) {
                    const int b = i >> 2, m = b & 1;
                    *(LAS u32x2*)(tr + ((m0_ ^ m) * 32 + r) * 720 + (b == 2 ? 8 + (wid >> 1) : wid) * 64 + (i & 3) * 16 + hh * 8) =
                        (u32x2){cvt2(__builtin_elementwise_fma(d[2 * i], rs[m], of[m])), cvt2(__builtin_elementwise_fma(d[2 * i + 1], rs[m], of[m]))};
                }
            WAIT_L(0); BAR();
#pragma unroll
            for (int i = 0; i < 6; ++i) { int c = tid + 512 * i; const bool ok = c < 2752; c = ok ? c : 0; const int zr = c / 43, zc = c - zr * 43;
                st16(rws, ok ? WS_Z2N + (rg * 64 + zr) * SLB + hf * 688 + zc * 16 : 0x80000000u, *(const LAS u32x4*)(tr + zr * 720 + zc * 16)); }
#pragma unroll
            for (int i = 0; i < 2; ++i) { const int c = tid + 512 * i, zr = c / 10;
                st16(rws, hf == 1 && c < 640 ? WS_Z2N + (rg * 64 + zr) * SLB + 1376 + (c - zr * 10) * 16 : 0x80000000u, (u32x4){0u, 0u, 0u, 0u}); }
        }
        PROF(1);
        if (kk < 30) {
            u32x4 z1[2][2][2];
#pragma unroll
            for (int k = 0; k < 4; ++k) aoff[k] = ((wid >> 1) & 1) * 16384 + ((wid & 1) * 64 + r) * 128 + (((2 * k + hh) ^ ((r >> 1) & 7)) << 4);
            const int bo0 = RB + (8 * hh + qq) * 256 + (((2 * grp) ^ qq) << 6) + blk * 32 + p * 8, bo1 = RB + (8 * hh + qq) * 256 + (((2 * grp + 1) ^ qq) << 6) + blk * 32 + p * 8;
            f32x16 acc[2][2];
#pragma unroll
            for (int c = 0; c < 4; ++c) acc[c >> 1][c & 1] = f32x16{};
#pragma unroll
            for (int c = 0; c < 8; ++c) z1[c >> 2][(c >> 1) & 1][c & 1] = (u32x4){0u, 0u, 0u, 0u};
            Ap += 256; Bp += 192 * WROW;
            unsigned sa = 0, sa1 = 32768, sa2 = RA2;
            unsigned la = ldsw + RA2, la1 = ldsw, la2 = ldsw + 32768, lb = 49152;
            const char* Aq = Ap; unsigned lq = la, lq1 = la1, lq2 = la2, vQ0 = vA0, vQ1 = vA1, dQ = dA;
#define SP_SRC(s_) do { if ((s_) == 6) { Ap = (const char*)wsp + (size_t)pm * 256 * SLA; vA1 = arow * SLA + achk + 64 * SLA; dA = 128 * SLA; } \
                if ((s_) == 5) { Bp = (const char*)ws + WS_Z2N + pn * 256; vB0 = brow * SLB + bchk; vB1 = vB0 + 32 * SLB; dB = 64 * SLB; } \
                if (t == 6) { Aq = (const char*)wsp + (size_t)pm * 256 * SLA; vQ0 = arow * SLA + achk; vQ1 = vQ0 + 64 * SLA; dQ = 128 * SLA; } } while (0)
#define SP_LATE() do { dma_pair<0, 8192>(vQ0, vQ1, Aq, lq); dma_one<16384>(vQ0, Aq + dQ, lq); Aq += 128; const unsigned l_ = lq; lq = lq1; lq1 = lq2; lq2 = l_; } while (0)
#define SP_A1() dma_one<24576>(vA1, Ap + dA, la)
#define SP_B0() dma_one<RB>(vB0, Bp, ldsw + lb)
#define SP_B1() dma_one<RB + 8192>(vB1, Bp, ldsw + lb)
#define SP_ADV() do { Ap += 128; Bp += dB; const unsigned l_ = la; la = la1; la1 = la2; la2 = l_; lb = (lb + 16384) & 49152; } while (0)
#define SP_GELU() do { _Pragma("unroll") for (int c = 0; c < 4; ++c) { f32x16& a = acc[c >> 1][c & 1]; z1[c >> 1][c & 1][0] = gelu8(a, 0); z1[c >> 1][c & 1][1] = gelu8(a, 1); a = f32x16{}; } } while (0)
#define SP_MMA(k) do { _Pragma("unroll") for (int c = 0; c < 4; ++c) acc[c >> 1][c & 1] = MFMA32(Bt[c & 1][k], At[c >> 1][k], acc[c >> 1][c & 1]); } while (0)
#define SP_STEP(PRE, LATE, WAIT0, WAIT1, D0, D1, D2, POST, LASTBAR) do { \
                bf16x8 At[2][4], Bt[2][4]; \
                PRE; \
                const lds_cp pa = lds + sa, pb = lds + (t & 3) * 16384; \
                _Pragma("unroll") for (int k = 0; k < 4; ++k) { Bt[0][k] = ldtr2(pb + bo0 + k * 4096, 1024); At[0][k] = ld128(pa + aoff[k]); At[1][k] = ld128(pa + aoff[k] + 4096); Bt[1][k] = ldtr2(pb + bo1 + k * 4096, 1024); } \
                LATE; \
                if (grp == 1) { WAIT1; } \
                WAIT_L(0); BAR(); SBAR(); \
                __builtin_amdgcn_s_setprio(1); \
                SP_MMA(0); SBAR(); D0; SBAR(); SP_MMA(1); SBAR(); D1; SBAR(); SP_MMA(2); SBAR(); D2; SBAR(); SP_MMA(3); \
                __builtin_amdgcn_s_setprio(0); \
                SBAR(); \
                POST; \
                if (grp == 0) { WAIT0; } \
                if (LASTBAR) BAR(); \
                const unsigned s_ = sa; sa = sa1; sa1 = sa2; sa2 = s_; } while (0)
            asm volatile("s_waitcnt vmcnt(16) lgkmcnt(0)\n\ts_barrier" ::: "memory");
            if (grp == 1) { SP_A1(); SP_B0(); SP_B1(); SP_ADV(); BAR(); }
            PROF(2);
            int t = 0;
#pragma unroll 1
            for (; t < 8; ++t)
                SP_STEP({ if (wid == 0) { if (t == 2 && lane == 0) { set_flag(ws, flz, tag); set_flag(ws, F_S + bid, tag); } if (t == 3) { poll_flags(ws, F_L + lane, true, tag); ACQUIRE(); } } SP_SRC(t + grp); },
                        SP_LATE(), { if (t == 0) WAIT_V(16); else WAIT_V(8); }, { if (t == 0) WAIT_V(16); else WAIT_V(6); }, SP_A1(), SP_B0(), SP_B1(), { SP_ADV(); if (t == 7 && grp == 1) SP_GELU(); }, true);
            PROF(11);
            if (grp == 0) SP_GELU();
            PROF(12);
#pragma unroll 1
            for (; t < 36; ++t) SP_STEP(NOP_, SP_LATE(), WAIT_V(8), WAIT_V(6), SP_A1(), SP_B0(), SP_B1(), SP_ADV(), true);
#pragma unroll 1
            for (; t < 40; ++t)
                SP_STEP({ if (wid == 0) { SP_CHECK(); } }, if (t < 38) SP_LATE(),
                        { if (t == 36) WAIT_V(8); else if (t == 37) WAIT_V(6); else WAIT_V(0); }, { if (t == 36) WAIT_V(6); else if (t == 37) WAIT_V(4); else WAIT_V(0); },
                        if (t + grp < 38) SP_A1(), if (t + grp < 37) SP_B0(), if (t + grp < 37) SP_B1(), SP_ADV(), !(t == 39 && grp == 1));
            PROF(4);
            Q1_LOAD();
            G_STAGE(G_SA(0, 0), rat.A, rat.vA0, rat.vA1); G_STAGE(G_SB(0, 0), rat.B, rat.vA0, rat.vA1); G_STAGE(G_SA(1, 0), rat.A + 8192, rat.vA0, rat.vA1); G_STAGE(G_SB(1, 0), rat.B + 8192, rat.vA0, rat.vA1);
            G_STAGE(G_SA(0, 1), rat.A + 65536, rat.vA0, rat.vA1); G_STAGE(G_SB(0, 1), rat.B + 65536, rat.vA0, rat.vA1); G_STAGE(G_SA(1, 1), rat.A + 73728, rat.vA0, rat.vA1); G_STAGE(G_SB(1, 1), rat.B + 73728, rat.vA0, rat.vA1);
            Q2_LOAD();
            SBAR();
#pragma unroll
            for (int m = 0; m < 2; ++m)
#pragma unroll
                for (int c = 0; c < 4; ++c) {
                    const f32x16& a = acc[m][c >> 1]; const u32x4 z = z1[m][c >> 1][c & 1]; const int s8 = 8 * (c & 1);
                    u32x4 o;
#pragma unroll
                    for (int e = 0; e < 4; ++e) o[e] = cvtpk(bflo(z[e]) * (a[s8 + 2 * e] + bias[m]), bfhi(z[e]) * (a[s8 + 2 * e + 1] + bias[m]));
                    st16(rws, fs0 + c < 43 ? WS_AF + ((256 + fs0 + c) * 64 + tb0 + m) * 1024 + lane * 16 : 0x80000000u, o);
                }
            pf = F_U + pm * 8 + pn;
            Q1_WAIT(20);
        } else {
            WAIT_V(0); WAIT_L(0); BAR();
            if (tid == 0) { set_flag(ws, flz, tag); set_flag(ws, F_S + bid, tag); }
            if (wid == 0) { poll_flags(ws, fatt + (lane & 31), lane < 32 && (lane & 15) < 12, tag); ACQUIRE(); }
            WAIT_V(0); BAR();
            PROF(4);
            g_prologue_b(rat, ldsw); g_prologue_rest(rat, ldsw); G_STAGE(G_SA(1, 1), rat.A + rat.dA1 + rat.dA2, rat.vA0, rat.vA1);
            Q1_LOAD();
            pf = -1;
            Q1_WAIT(0);
        }
    }
    BAR();
    PROF(5);
    {
        const int kh = wid >> 2, rg = wid & 3;
        const bool early = kk < 24 || kk >= 30;
        const rsrc_t rkv = __builtin_amdgcn_make_buffer_rsrc((void*)kvx, (short)0, 4 * 64 * 16384, 0x00020000);
        const unsigned dv0 = rg * 2048 + lane * 16 + kh * 32768, dv1 = dv0 + 8192, dv2 = dv0 + 16384, dv3 = dv0 + 24576;
        const unsigned wrapoff = (unsigned)(h1 * 16 + i1 + 1) * 65536u, wrapto = (unsigned)h2 << 20;
        unsigned sA_ = 0, sB_ = ((unsigned)h1 << 20) + 65536u, lA = lds0 + kh * 65536 + rg * 2048, lB = lA + 16384;
#define A_ADV() do { sA_ = sB_; sB_ += 65536u; if (sB_ == wrapoff) sB_ = wrapto; const unsigned t_ = lA; lA = lB; lB = t_; } while (0)
#define D_KO(h) bdma1<8192, (h) * 1024>(dv2, rkv, sA_, lA)
#define D_VE(h) bdma1<32768, (h) * 1024>(dv1, rkv, sA_, lA)
#define D_KE(h) bdma1<0, (h) * 1024>(dv0, rkv, sB_, lB)
#define D_VO(h) bdma1<40960, (h) * 1024>(dv3, rkv, sA_, lA)
#define PUBLISH() do { if (pf >= 0) { if (tid == 0) set_flag(ws, pf, tag); pf = -1; } } while (0)
        const lds_cp qrd = lds + 131072 + rg * 8192 + lane * 16;
        const lds_cp rbase = lds + kh * 65536 + lane * 16;
        int so = 0;
#pragma unroll 1
        for (int blkno = 0; blkno < 2; ++blkno) {
            const int ib = blkno ? i2 : i1, head = 4 * x8 + (blkno ? h2 : h1);
            const int qrow = 128 * ib + 32 * rg + r - 4 * hh - 64 * kh;
            f32x16 o[4], sA, sB, negm;
            bf16x8 fr[8];
            u32x4 pw0, pw1;
            float mref, l0 = 0.f, l1 = 0.f, resc = 1.0f;
            bool dead = false, pend = false;
            lds_cp pc = rbase + so, pn = rbase + (so ^ 16384);
#pragma unroll
            for (int d = 0; d < 8; ++d) fr[d] = ld128(pc + d * 1024);
            sA = f32x16{};
#pragma unroll
            for (int d = 0; d < 8; ++d) sA = MFMA32(fr[d], q[d], sA);
            asm volatile("s_waitcnt lgkmcnt(0)\n\ts_barrier" ::: "memory");
            A_ADV();
            D_KO(0); D_KO(1);
#pragma unroll
            for (int d = 0; d < 4; ++d) fr[d] = ld128(pc + 8192 + d * 1024);
#pragma unroll
            for (int d = 0; d < 4; ++d) o[d] = f32x16{};
            if (ib == 0) att_mask(sA, qrow);
            {
                float m = MX3(sA[0], sA[1], sA[2]);
#pragma unroll
                for (int e = 3; e < 15; e += 2) m = MX3(m, sA[e], sA[e + 1]);
                m = swap_max(__builtin_fmaxf(m, sA[15]));
                dead = m == -__builtin_inff();
                mref = dead ? 0.f : m;
#pragma unroll
                for (int e = 0; e < 16; ++e) { sA[e] -= mref; negm[e] = -mref; }
                PIN(negm);
#pragma unroll
                for (int e = 0; e < 8; ++e) sA[e] = EX2(sA[e]);
                l0 = sA[0] + sA[2]; l1 = sA[1] + sA[3];
            }
#define A_RD(j, ptr) do { fr[j] = ld128(ptr); } while (0)
#define A_P1(SC, SN, KP, VP, E0, E1, E2) do { \
            A_RD(4, (KP) + 4096); SBAR(); SN = MFMA32(fr[0], q[0], negm); SC[8] = EX2(SC[8]); SC[9] = EX2(SC[9]); pw0[0] = cvtpk(SC[0], SC[1]); PIN(SC); PIN(pw0); SBAR(); \
            A_RD(5, (KP) + 5120); SBAR(); SN = MFMA32(fr[1], q[1], SN); SC[10] = EX2(SC[10]); SC[11] = EX2(SC[11]); pw0[1] = cvtpk(SC[2], SC[3]); PIN(SC); PIN(pw0); E0; SBAR(); \
            A_RD(6, (KP) + 6144); SBAR(); SN = MFMA32(fr[2], q[2], SN); SC[12] = EX2(SC[12]); SC[13] = EX2(SC[13]); pw0[2] = cvtpk(SC[4], SC[5]); PIN(SC); PIN(pw0); SBAR(); \
            A_RD(7, (KP) + 7168); SBAR(); SN = MFMA32(fr[3], q[3], SN); SC[14] = EX2(SC[14]); SC[15] = EX2(SC[15]); pw0[3] = cvtpk(SC[6], SC[7]); PIN(SC); PIN(pw0); SBAR(); \
            A_RD(0, (VP));        SBAR(); SN = MFMA32(fr[4], q[4], SN); l0 += SC[4]; l1 += SC[5]; l0 += SC[6]; l1 += SC[7]; PIN(l0); PIN(l1); E1; SBAR(); \
            A_RD(1, (VP) + 2048); SBAR(); SN = MFMA32(fr[5], q[5], SN); l0 += SC[8]; l1 += SC[9]; pw1[0] = cvtpk(SC[8], SC[9]); PIN(l0); PIN(l1); PIN(pw1); SBAR(); \
            A_RD(2, (VP) + 4096); SBAR(); SN = MFMA32(fr[6], q[6], SN); l0 += SC[10]; l1 += SC[11]; pw1[1] = cvtpk(SC[10], SC[11]); PIN(l0); PIN(l1); PIN(pw1); SBAR(); \
            A_RD(3, (VP) + 6144); SBAR(); SN = MFMA32(fr[7], q[7], SN); E2; SBAR(); } while (0)
#define PWF(x) __builtin_bit_cast(bf16x8, x)
#define A_P2A(SC, SN, VP, MASK, DQ) do { \
            A_RD(4, (VP) + 1024); A_RD(5, (VP) + 3072); SBAR(); o[0] = MFMA32(fr[0], PWF(pw0), o[0]); l0 += SC[12]; l1 += SC[13]; pw1[2] = cvtpk(SC[12], SC[13]); PIN(l0); PIN(l1); PIN(pw1); SBAR(); \
            A_RD(6, (VP) + 5120); A_RD(7, (VP) + 7168); SBAR(); o[1] = MFMA32(fr[1], PWF(pw0), o[1]); l0 += SC[14]; l1 += SC[15]; pw1[3] = cvtpk(SC[14], SC[15]); PIN(l0); PIN(l1); PIN(pw1); SBAR(); \
            float ma_ = 0.f, mb_ = 0.f; \
            o[2] = MFMA32(fr[2], PWF(pw0), o[2]); \
            if (MASK) att_mask(SN, DQ); \
            ma_ = MX3(SN[0], SN[1], SN[2]); mb_ = MX3(SN[3], SN[4], SN[5]); ma_ = MX3(ma_, SN[6], SN[7]); mb_ = MX3(mb_, SN[8], SN[9]); PIN(ma_); PIN(mb_); SBAR(); \
            o[3] = MFMA32(fr[3], PWF(pw0), o[3]); \
            ma_ = MX3(ma_, SN[10], SN[11]); mb_ = MX3(mb_, SN[12], SN[13]); ma_ = MX3(ma_, SN[14], SN[15]); ma_ = __builtin_fmaxf(ma_, mb_); \
            if (__builtin_expect(__any(ma_ > ATT_THR), 0)) {            \
                const float dl_ = __builtin_fmaxf(swap_max(ma_), 0.f); mref += dl_; resc = EX2(-dl_); l0 *= resc; l1 *= resc; pend = true; \
                _Pragma("unroll") for (int e = 0; e < 16; ++e) { SN[e] -= dl_; negm[e] = -mref; } PIN(negm); } SBAR(); } while (0)
#define A_RESC() do { if (__builtin_expect(pend, 0)) { pend = false;         \
            _Pragma("unroll") for (int d = 0; d < 4; ++d) _Pragma("unroll") for (int e = 0; e < 16; ++e) o[d][e] *= resc; } } while (0)
#define A_P2B(SN, KN, D0, D1) do { \
            A_RD(0, (KN));        SBAR(); o[0] = MFMA32(fr[4], PWF(pw1), o[0]); SN[0] = EX2(SN[0]); SN[1] = EX2(SN[1]); PIN(SN); D0; SBAR(); \
            A_RD(1, (KN) + 1024); SBAR(); o[1] = MFMA32(fr[5], PWF(pw1), o[1]); SN[2] = EX2(SN[2]); SN[3] = EX2(SN[3]); PIN(SN); SBAR(); \
            A_RD(2, (KN) + 2048); SBAR(); o[2] = MFMA32(fr[6], PWF(pw1), o[2]); SN[4] = EX2(SN[4]); SN[5] = EX2(SN[5]); l0 += SN[0]; l1 += SN[1]; PIN(SN); PIN(l0); PIN(l1); D1; SBAR(); \
            A_RD(3, (KN) + 3072); SBAR(); o[3] = MFMA32(fr[7], PWF(pw1), o[3]); SN[6] = EX2(SN[6]); SN[7] = EX2(SN[7]); l0 += SN[2]; l1 += SN[3]; PIN(SN); PIN(l0); PIN(l1); SBAR(); \
            A_RESC(); } while (0)
#pragma unroll 1
            for (int n = 0; n < ib; ++n) {
                A_P1(sA, sB, pc + 8192, pc + 32768, D_VE(0), D_VE(1), D_KE(0));
                A_P2A(sA, sB, pc + 32768, false, 0);
                A_P2B(sB, pn, D_KE(1), D_VO(0));
                A_P1(sB, sA, pn, pc + 32768 + 8192, D_VO(1), NOP_, NOP_);
                A_P2A(sB, sA, pc + 32768 + 8192, (n == ib - 1), qrow - 128 * ib);
                const int ev = blkno == 0 && (early || n == 0) ? n : 6;
                if (ev != 0) WAIT_V(0); else if (kk < 24) WAIT_V(24); else WAIT_V(16);
                asm volatile("s_waitcnt lgkmcnt(0)\n\ts_barrier" ::: "memory");
                A_ADV();
                if (ev != 0) PUBLISH();
                if (ev < 6) {
                    if (ev == 3 && wid == 0) dma_words(lane * 4, ws + (size_t)(fatt + 32) * 8, lds0 + 131072);
                    if (ev == 4 && wid == 0) { check_parked<0, 12>(lds + 131072, ws, fatt + 32, tag, lane); ACQUIRE(); }
                    if (ev == 5) Q2_LOAD();
                }
                A_P2B(sA, pn + 8192, D_KO(0), D_KO(1));
                so ^= 16384;
                const lds_cp t_ = pc; pc = pn; pn = t_;
            }
            if (blkno == 0) PROF(6);
            A_P1(sA, sB, pc + 8192, pc + 32768, D_VE(0); D_VE(1), D_KE(0); D_KE(1), D_VO(0); D_VO(1));
            if (blkno == 0) {
#pragma unroll
                for (int d = 0; d < 8; ++d) q[d] = ld128(qrd + d * 1024);
            }
            A_P2A(sA, sB, pc + 32768, true, qrow - 128 * ib - 32);
            {
                o[0] = MFMA32(fr[4], PWF(pw1), o[0]); sB[0] = EX2(sB[0]); sB[1] = EX2(sB[1]); PIN(sB); SBAR();
                o[1] = MFMA32(fr[5], PWF(pw1), o[1]); sB[2] = EX2(sB[2]); sB[3] = EX2(sB[3]); PIN(sB); SBAR();
                o[2] = MFMA32(fr[6], PWF(pw1), o[2]); sB[4] = EX2(sB[4]); sB[5] = EX2(sB[5]); l0 += sB[0]; l1 += sB[1]; PIN(sB); SBAR();
                o[3] = MFMA32(fr[7], PWF(pw1), o[3]); sB[6] = EX2(sB[6]); sB[7] = EX2(sB[7]); l0 += sB[2]; l1 += sB[3]; PIN(sB); SBAR();
                A_RESC();
            }
            {
                const lds_cp vp = pc + 32768 + 8192;
#pragma unroll
                for (int j = 0; j < 4; ++j) { fr[j] = ld128(vp + j * 2048); fr[4 + j] = ld128(vp + j * 2048 + 1024); }
#pragma unroll
                for (int e = 8; e < 16; ++e) sB[e] = EX2(sB[e]);
#pragma unroll
                for (int e = 4; e < 16; e += 2) { l0 += sB[e]; l1 += sB[e + 1]; }
#pragma unroll
                for (int e = 0; e < 4; ++e) { pw0[e] = cvtpk(sB[2 * e], sB[2 * e + 1]); pw1[e] = cvtpk(sB[8 + 2 * e], sB[9 + 2 * e]); }
                asm volatile("s_waitcnt vmcnt(0) lgkmcnt(0)\n\ts_barrier" ::: "memory");
                PUBLISH();
#pragma unroll
                for (int j = 0; j < 4; ++j) o[j] = MFMA32(fr[j], PWF(pw0), o[j]);
#pragma unroll
                for (int j = 0; j < 4; ++j) o[j] = MFMA32(fr[4 + j], PWF(pw1), o[j]);
            }
            {
                const int sb_own = kh == 0 ? 131072 + rg * 8192 : (rg >> 1) * 65536 + 32768 + so + (rg & 1) * 8192;
                const int sb_par = kh == 1 ? 131072 + rg * 8192 : (rg >> 1) * 65536 + 32768 + so + (rg & 1) * 8192;
                const float mown = dead ? -__builtin_inff() : mref, lown = l0 + l1;
                *(LAS f32x2*)(lds + kh * 65536 + so + 8192 + rg * 512 + lane * 8) = (f32x2){mown, lown};
                const f32x16 s0 = kh ? o[0] : o[2], s1 = kh ? o[1] : o[3], k0 = kh ? o[2] : o[0], k1 = kh ? o[3] : o[1];
#pragma unroll
                for (int qd = 0; qd < 4; ++qd) {
                    *(LAS f32x4*)(lds + sb_own + qd * 1024 + lane * 16) = (f32x4){s0[4 * qd], s0[4 * qd + 1], s0[4 * qd + 2], s0[4 * qd + 3]};
                    *(LAS f32x4*)(lds + sb_own + 4096 + qd * 1024 + lane * 16) = (f32x4){s1[4 * qd], s1[4 * qd + 1], s1[4 * qd + 2], s1[4 * qd + 3]};
                }
                asm volatile("s_waitcnt lgkmcnt(0)\n\ts_barrier" ::: "memory");
                const f32x2 ml = *(const LAS f32x2*)(lds + (kh ^ 1) * 65536 + so + 8192 + rg * 512 + lane * 8);
                f32x16 p0, p1;
#pragma unroll
                for (int qd = 0; qd < 4; ++qd) {
                    const f32x4 a = *(const LAS f32x4*)(lds + sb_par + qd * 1024 + lane * 16), b = *(const LAS f32x4*)(lds + sb_par + 4096 + qd * 1024 + lane * 16);
#pragma unroll
                    for (int e = 0; e < 4; ++e) { p0[4 * qd + e] = a[e]; p1[4 * qd + e] = b[e]; }
                }
                const float mf = __builtin_fmaxf(mown, ml[0]);
                const float fo = EX2(mown - mf), fp = EX2(ml[0] - mf);
                const float inv = 1.0f / swap_add(fo * lown + fp * ml[1]);
                const float ca = fo * inv, cb = fp * inv;
#pragma unroll
                for (int e = 0; e < 16; ++e) { p0[e] = ca * k0[e] + cb * p0[e]; p1[e] = ca * k1[e] + cb * p1[e]; }
                const unsigned dst = WS_AF + ((head * 8 + 4 * kh) * 64 + 4 * ib + rg) * 1024 + lane * 16;
                st16(rws, dst, pack8(p0, 0, 1.0f)); st16(rws, dst + 65536, pack8(p0, 1, 1.0f));
                st16(rws, dst + 2 * 65536, pack8(p1, 0, 1.0f)); st16(rws, dst + 3 * 65536, pack8(p1, 1, 1.0f));
                pf = F_C + ib * 32 + head;
            }
            so ^= 16384;
            if (blkno == 0) PROF(7);
        }
        PROF(8);
        asm volatile("s_waitcnt vmcnt(0) lgkmcnt(0)\n\ts_barrier" ::: "memory");
        PUBLISH();
#undef A_RD
#undef A_P1
#undef A_P2A
#undef A_P2B
#undef PWF
    }

    {
        const int sq = x8 >> 1, tm = (x8 & 1) * 8 + (kk >> 2), tn = kk & 3, rb = wid & 3, kp = wid >> 2;
        OutArgs oa;
        oa.ra = __builtin_amdgcn_make_buffer_rsrc((void*)(ws + WS_AF), (short)0, NKS * 65536, 0x00020000);
        oa.wo = (const char*)wo + (size_t)sq * 1048576 + tn * 256; oa.wu = (const char*)wout + tn * 256;
        oa.wv = brow * 1024 + bchk;
#pragma unroll
        for (int j = 0; j < 6; ++j) { const unsigned ur = 192 * sq + 32 * j + brow; oa.uv[j] = (ur > FH - 1 ? FH - 1 : ur) * 1024 + bchk; }
        oa.va = kp * 131072 + (tm * 4 + rb) * 1024 + lane * 16; oa.sc = sq * 64 * 65536; oa.su = (256 + 12 * sq) * 65536; oa.ldsw = ldsw;
#pragma unroll
        for (int c = 0; c < 4; ++c) oa.rd[c] = lds + (kp * 32 + 4 * hh + qq) * 256 + ((c ^ qq) << 6) + blk * 32 + p * 8;
        out_w<0>(oa); out_w<1>(oa); out_w<2>(oa); out_w<3>(oa); out_w<4>(oa); out_w<5>(oa); out_w<6>(oa); out_w<7>(oa);
        if (wid == 0) {
            if (bid == 0) {
#pragma unroll 1
                for (int i = 0; i < 4; ++i) poll_flags(ws, F_S + i * 64 + lane, true, tag);
                if (lane == 0) set_flag(ws, F_E, tag);
            }
            poll_flags(ws, lane < 8 ? F_C + tm * 32 + 8 * sq + lane : F_U + (tm >> 1) * 8 + ((3 * sq) >> 1) + (lane & 1), lane < 10, tag);
        }
        WAIT_V(12); BAR();
        PROF(9);
        u32x4 af[6][2];
        bf16x8 wf[2][4];
        f32x16 acc[4] = {};
        out_a<0>(oa, af); out_a<1>(oa, af); out_a<2>(oa, af); out_a<3>(oa, af); out_a<4>(oa, af);
#pragma unroll
        for (int c = 0; c < 4; ++c) wf[0][c] = ldtr2(oa.rd[c], 2048);
#define OS(t) out_step<t>(oa, af, wf, acc)
        OS(0); OS(1); OS(2); OS(3); OS(4); OS(5); OS(6); OS(7); OS(8); OS(9); OS(10); OS(11); OS(12); OS(13); OS(14); OS(15); OS(16); OS(17); OS(18);
#undef OS
        PROF(10);
        const f32x16 s0 = kp ? acc[0] : acc[2], s1 = kp ? acc[1] : acc[3];
        f32x16 k0 = kp ? acc[2] : acc[0], k1 = kp ? acc[3] : acc[1];
#pragma unroll
        for (int g = 0; g < 4; ++g) {
            *(LAS f32x4*)(lds + 16384 + wid * 8192 + g * 1024 + lane * 16) = (f32x4){s0[4 * g], s0[4 * g + 1], s0[4 * g + 2], s0[4 * g + 3]};
            *(LAS f32x4*)(lds + 16384 + wid * 8192 + 4096 + g * 1024 + lane * 16) = (f32x4){s1[4 * g], s1[4 * g + 1], s1[4 * g + 2], s1[4 * g + 3]};
        }
        const size_t orow = (size_t)(tm * 128 + rb * 32 + r) * DM + tn * 128 + kp * 64 + 4 * hh;
        u32x2 xv[8];
        if (rb == sq) {
#pragma unroll
            for (int g = 0; g < 8; ++g) xv[g] = *(const u32x2*)(x + orow + 8 * g);
        }
        WAIT_L(0); BAR();
#pragma unroll
        for (int g = 0; g < 4; ++g) {
            const f32x4 a = *(const LAS f32x4*)(lds + 16384 + (wid ^ 4) * 8192 + g * 1024 + lane * 16), b = *(const LAS f32x4*)(lds + 16384 + (wid ^ 4) * 8192 + 4096 + g * 1024 + lane * 16);
#pragma unroll
            for (int e = 0; e < 4; ++e) { k0[4 * g + e] += a[e]; k1[4 * g + e] += b[e]; }
        }
        const unsigned pb = WS_P + ((tm * 4 + tn) * 4 + rb) * 65536 + kp * 8192 + lane * 16;
        const int fp = F_P + (tm * 4 + tn) * 4;
        if (rb != sq) {
#pragma unroll
            for (int g = 0; g < 4; ++g) {
                st16(rws, pb + sq * 16384 + g * 1024, __builtin_bit_cast(u32x4, (f32x4){k0[4 * g], k0[4 * g + 1], k0[4 * g + 2], k0[4 * g + 3]}));
                st16(rws, pb + sq * 16384 + 4096 + g * 1024, __builtin_bit_cast(u32x4, (f32x4){k1[4 * g], k1[4 * g + 1], k1[4 * g + 2], k1[4 * g + 3]}));
            }
            WAIT_V(0);
        }
        BAR();
        if (tid == 0) set_flag(ws, fp + sq, tag);
        PROF(11);
        if (rb == sq) {
            poll_flags(ws, fp + (lane & 3), (lane & 3) != sq, tag);
            PROF(12);
            u32x4 pp[3][8];
#pragma unroll
            for (int j = 0; j < 3; ++j)
#pragma unroll
                for (int g = 0; g < 8; ++g) pp[j][g] = ld16(rws, pb + ((sq + 1 + j) & 3) * 16384 + g * 1024);
#pragma unroll
            for (int g = 0; g < 8; ++g) {
                const f32x16& k = g < 4 ? k0 : k1; const int e0 = 4 * (g & 3);
                const f32x4 a = __builtin_bit_cast(f32x4, pp[0][g]), b = __builtin_bit_cast(f32x4, pp[1][g]), c = __builtin_bit_cast(f32x4, pp[2][g]);
                const f32x4 t = ((f32x4){k[e0], k[e0 + 1], k[e0 + 2], k[e0 + 3]} + a) + (b + c);
                *(u32x2*)(out + orow + 8 * g) = (u32x2){cvtpk(bflo(xv[g][0]) + t[0], bfhi(xv[g][0]) + t[1]), cvtpk(bflo(xv[g][1]) + t[2], bfhi(xv[g][1]) + t[3])};
            }
        }
    }
    PROF_END(ws);
}

extern "C" void kernel_launch(void* const* d_in, const int* in_sizes, int n_in, void* d_out, int out_size, void* d_ws, size_t ws_size, hipStream_t stream) {
    (void)in_sizes; (void)n_in; (void)out_size; (void)ws_size;
    fused_fwd<<<256, 512, 0, stream>>>((const bf16_t*)d_in[0], (const bf16_t*)d_in[1], (const bf16_t*)d_in[2], (const bf16_t*)d_in[3], (const bf16_t*)d_in[4], (const bf16_t*)d_in[5],
                                       (const bf16_t*)d_in[7], (const bf16_t*)d_in[8], (const bf16_t*)d_in[9], (const bf16_t*)d_in[10], (bf16_t*)d_out, (unsigned char*)d_ws);
}
```

```cpp
#include <hip/hip_runtime.h>
#include <hip/hip_bf16.h>
#include <stdint.h>

#define LAS __attribute__((address_space(3)))
#define DI __device__ __forceinline__
typedef unsigned short bf16_t;
typedef unsigned long long u64;
typedef __attribute__((address_space(1))) u64 gu64;
typedef short bf16x8 __attribute__((ext_vector_type(8)));
typedef short s16x4 __attribute__((ext_vector_type(4)));
typedef __bf16 bf16x2_t __attribute__((ext_vector_type(2)));
typedef float f32x16 __attribute__((ext_vector_type(16)));
typedef float f32x4 __attribute__((ext_vector_type(4)));
typedef float f32x2 __attribute__((ext_vector_type(2)));
typedef unsigned u32x4 __attribute__((ext_vector_type(4)));
typedef unsigned u32x2 __attribute__((ext_vector_type(2)));
typedef LAS unsigned char* lds_p;
typedef const LAS unsigned char* lds_cp;
typedef __amdgpu_buffer_rsrc_t rsrc_t;

constexpr int SEQ = 2048, DM = 512, NH = 32, FH = 688, FF = 1376;
constexpr int NKS = 299;
constexpr int F_E = 0;
constexpr int F_S = 512;
constexpr int F_X = 1024;
constexpr int F_L = 2048;
constexpr int F_U = 3072;
constexpr int F_K = 3584;
constexpr int F_C = 4096;
constexpr int F_P = 5120;
constexpr unsigned WS_G   = 65536;
constexpr unsigned WS_XN  = 131072;
constexpr unsigned WS_QF  = WS_XN + SEQ * DM * 2;
constexpr unsigned WS_KVF = WS_QF + NH * 64 * 8192;
constexpr unsigned WS_Z2N = WS_KVF + NH * 64 * 16384;
constexpr unsigned WS_AF  = WS_Z2N + SEQ * 768 * 2;
constexpr unsigned WS_P   = WS_AF + 304 * 64 * 1024;
constexpr unsigned WS_END = WS_P + 256 * 65536;

#define PROF_DECL
#define PROF(i)
#define PROF_END(wsp)
#define MFMA32(a, b, c) __builtin_amdgcn_mfma_f32_32x32x16_bf16((a), (b), (c), 0, 0, 0)
#define SBAR() __builtin_amdgcn_sched_barrier(0)
#define PIN(x) asm volatile("" : "+v"(x))
#define WAIT_V(n) asm volatile("s_waitcnt vmcnt(" #n ")" ::: "memory")
#define WAIT_L(n) asm volatile("s_waitcnt lgkmcnt(" #n ")" ::: "memory")
#define BAR() asm volatile("s_barrier" ::: "memory")
#define SC1 16

DI unsigned cvtpk(float lo, float hi) { f32x2 v = {lo, hi}; bf16x2_t b = __builtin_convertvector(v, bf16x2_t); return __builtin_bit_cast(unsigned, b); }
DI float bflo(unsigned w) { return __uint_as_float(w << 16); }
DI float bfhi(unsigned w) { return __uint_as_float(w & 0xffff0000u); }
DI u32x4 pack8(const f32x16& a, int s, float sc) {
    u32x4 w;
    w[0] = cvtpk(a[8 * s + 0] * sc, a[8 * s + 1] * sc); w[1] = cvtpk(a[8 * s + 2] * sc, a[8 * s + 3] * sc);
    w[2] = cvtpk(a[8 * s + 4] * sc, a[8 * s + 5] * sc); w[3] = cvtpk(a[8 * s + 6] * sc, a[8 * s + 7] * sc);
    return w;
}
DI bf16x8 ld128(lds_cp p) { return *(const LAS bf16x8*)p; }
typedef short v4i16_t __attribute__((ext_vector_type(4)));
DI s16x4 ldtr(lds_cp p) { return __builtin_bit_cast(s16x4, __builtin_amdgcn_ds_read_tr16_b64_v4i16((LAS v4i16_t*)p)); }
DI bf16x8 ldtr2(lds_cp p, int step) { const s16x4 lo = ldtr(p), hi = ldtr(p + step); return __builtin_shufflevector(lo, hi, 0, 1, 2, 3, 4, 5, 6, 7); }
template <int I0, int I1> DI void dma_pair(unsigned v0, unsigned v1, const void* sbase, unsigned l) {
    asm volatile("s_add_u32 m0, %3, %4\n\ts_nop 0\n\tglobal_load_lds_dwordx4 %0, %2 sc1\n\ts_add_u32 m0, %3, %5\n\ts_nop 0\n\tglobal_load_lds_dwordx4 %1, %2 sc1"
                 :: "v"(v0), "v"(v1), "s"(sbase), "s"(l), "i"(I0), "i"(I1) : "memory", "scc");
}
template <int I, int O> DI void bdma1(unsigned voff, rsrc_t rs, unsigned soff, unsigned l) {
    asm volatile("s_add_u32 m0, %3, %4\n\ts_nop 0\n\tbuffer_load_dwordx4 %0, %1, %2 offen offset:%5 sc1 lds" :: "v"(voff), "s"(rs), "s"(soff), "s"(l), "i"(I), "i"(O) : "memory", "scc");
}
DI void dma_words(unsigned voff, const void* sbase, unsigned l) {
    asm volatile("s_mov_b32 m0, %2\n\ts_nop 0\n\tglobal_load_lds_dword %0, %1 sc1" :: "v"(voff), "s"(sbase), "s"(l) : "memory");
}
DI void st16(rsrc_t rs, unsigned off, u32x4 v) { __builtin_amdgcn_raw_buffer_store_b128(v, rs, off, 0, SC1); }
DI u32x4 ld16(rsrc_t rs, unsigned off) { return __builtin_amdgcn_raw_buffer_load_b128(rs, off, 0, SC1); }
DI float swap_add(float v) { auto r = __builtin_amdgcn_permlane32_swap(__float_as_uint(v), __float_as_uint(v), false, false); return __uint_as_float(r[0]) + __uint_as_float(r[1]); }
DI float swap_max(float v) { auto r = __builtin_amdgcn_permlane32_swap(__float_as_uint(v), __float_as_uint(v), false, false); return __builtin_fmaxf(__uint_as_float(r[0]), __uint_as_float(r[1])); }
DI f32x2 unpk(unsigned w) { return (f32x2){bflo(w), bfhi(w)}; }
DI unsigned cvt2(f32x2 v) { return __builtin_bit_cast(unsigned, __builtin_convertvector(v, bf16x2_t)); }
DI float sum_pair(unsigned w, float acc) { return __builtin_amdgcn_fdot2_f32_bf16(__builtin_bit_cast(bf16x2_t, w), __builtin_bit_cast(bf16x2_t, 0x3f803f80u), acc, false); }
DI unsigned gelu2(float a, float b) {
    const f32x2 v = {a, b};
    const f32x2 t = v * __builtin_elementwise_fma(v * v, (f32x2){-2.302208198f * 0.044715f, -2.302208198f * 0.044715f}, (f32x2){-2.302208198f, -2.302208198f});
    const f32x2 d = (f32x2){__builtin_amdgcn_exp2f(t[0]), __builtin_amdgcn_exp2f(t[1])} + (f32x2){1.0f, 1.0f};
    return cvt2(v * (f32x2){__builtin_amdgcn_rcpf(d[0]), __builtin_amdgcn_rcpf(d[1])});
}
DI u32x4 gelu8(const f32x16& a, int s) { return (u32x4){gelu2(a[8 * s], a[8 * s + 1]), gelu2(a[8 * s + 2], a[8 * s + 3]), gelu2(a[8 * s + 4], a[8 * s + 5]), gelu2(a[8 * s + 6], a[8 * s + 7])}; }
DI void set_flag(unsigned char* ws, int idx, u64 tag) { __hip_atomic_store((gu64*)ws + idx, tag, __ATOMIC_RELAXED, __HIP_MEMORY_SCOPE_AGENT); }
DI void poll_flags(unsigned char* ws, int idx, bool active, u64 tag) {
#pragma unroll 1
    for (unsigned spins = 0; spins < (1u << 17); ++spins) {
        const u64 v = __hip_atomic_load((gu64*)ws + idx, __ATOMIC_RELAXED, __HIP_MEMORY_SCOPE_AGENT);
        if (__all(!active || v == tag)) break;
        __builtin_amdgcn_s_sleep(4);
    }
}
#define ACQUIRE() asm volatile("buffer_inv sc1" ::: "memory")

constexpr int LDS_TAG = 163584, LDS_TOUCH = 163328, LDS_PARK = 163072;
DI void touch(unsigned voff, const void* sbase, unsigned lds0) { dma_words(voff, sbase, lds0 + LDS_TOUCH); }
#define TOUCH_A(v, s, l) touch(v, s, l)
#define TOUCH_S(v, s, l) touch(v, s, l)
#define TOUCH_Z(v, s, l) touch(v, s, l)
template <int I> DI void dma_one(unsigned v0, const void* sbase, unsigned l) {
    asm volatile("s_add_u32 m0, %2, %3\n\ts_nop 0\n\tglobal_load_lds_dwordx4 %0, %1 sc1" :: "v"(v0), "s"(sbase), "s"(l), "i"(I) : "memory", "scc");
}
DI void z2_half(const lds_p lds, const unsigned lds0, f32x16 (&acc)[3], const bf16_t* __restrict__ x, const bf16_t* __restrict__ w_in, const unsigned char* fx, const int rg, const int hf,
                const int tid, const int wid, const int lane
#define GP(i)
                ) {
    constexpr int ZS = 704, ZST = 32 * ZS;
    constexpr unsigned WROW = FF * 2;
    const int row = tid >> 3, seg = tid & 7;
    const u32x4* src = (const u32x4*)(x + (size_t)(rg * 64 + row) * DM) + seg;
    u32x4 v[8];
#pragma unroll
    for (int j = 0; j < 8; ++j) asm volatile("global_load_dwordx4 %0, %1, off offset:%2" : "=v"(v[j]) : "v"(src), "i"(j * 128) : "memory");
    unsigned bv[3];
    const int p2 = wid < 6 ? wid + 16 : wid + 8;
#pragma unroll
    for (int i = 0; i < 3; ++i) { const int c = (i == 2 ? p2 : wid + 8 * i) * 64 + lane, zr = c / 44, zc = c - zr * 44; bv[i] = zr * WROW + (zc > 42 ? 42 : zc) * 16; }
    const unsigned lw = lds0 + 65536 + wid * 1024, lw2 = lds0 + 65536 + p2 * 1024;
    const char* wg = (const char*)w_in + FH * 2 + hf * FH;
#define Z_STAGE(slot, g_) do { dma_pair<(slot) * ZST, (slot) * ZST + 8192>(bv[0], bv[1], (g_), lw); dma_one<(slot) * ZST>(bv[2], (g_), lw2); } while (0)
    BAR();
    Z_STAGE(0, wg); Z_STAGE(1, wg + 32 * WROW); Z_STAGE(2, wg + 64 * WROW); Z_STAGE(3, wg + 96 * WROW);
#pragma unroll
    for (int i = 0; i < 2; ++i) { int c = (2 * wid + i) * 64 + lane; c = c > 895 ? 895 : c; const int zr = c / 7, zc = c - zr * 7; TOUCH_Z(((rg & 3) * 128 + zr) * WROW + (zc == 6 ? 684 : zc * 128), wg, lds0); }
    SBAR();
    GP(5);
    {
        asm volatile("s_waitcnt vmcnt(14)" : "+v"(v[0]), "+v"(v[1]), "+v"(v[2]), "+v"(v[3]), "+v"(v[4]), "+v"(v[5]), "+v"(v[6]), "+v"(v[7]));
        GP(6);
        float s = 0.f, s1 = 0.f;
#pragma unroll
        for (int j = 0; j < 8; ++j) { s = sum_pair(v[j][0], s); s1 = sum_pair(v[j][1], s1); s = sum_pair(v[j][2], s); s1 = sum_pair(v[j][3], s1); }
        s += s1;
        s += __shfl_xor(s, 1); s += __shfl_xor(s, 2); s += __shfl_xor(s, 4);
        const float mean = s * (1.0f / DM);
        f32x2 d[32], q2 = {0.f, 0.f}, q3 = {0.f, 0.f};
#pragma unroll
        for (int i = 0; i < 32; i += 2) { d[i] = unpk(v[i >> 2][i & 3]) - (f32x2){mean, mean}; d[i + 1] = unpk(v[i >> 2][(i & 3) + 1]) - (f32x2){mean, mean};
            q2 = __builtin_elementwise_fma(d[i], d[i], q2); q3 = __builtin_elementwise_fma(d[i + 1], d[i + 1], q3); }
        q2 += q3;
        float q = q2[0] + q2[1];
        q += __shfl_xor(q, 1); q += __shfl_xor(q, 2); q += __shfl_xor(q, 4);
        const float rstd = 1.0f / sqrtf(q * (1.0f / DM) + 1e-5f);
#pragma unroll
        for (int j = 0; j < 8; ++j) {
            u32x4 o;
#pragma unroll
            for (int e = 0; e < 4; ++e) o[e] = cvt2(d[4 * j + e] * (f32x2){rstd, rstd});
            *(LAS u32x4*)(lds + j * 8192 + row * 128 + ((seg ^ ((row >> 1) & 7)) << 4)) = o;
        }
    }
    const int r = lane & 31, hh = lane >> 5, blk = (lane >> 4) & 1, qq = (lane & 15) >> 2, p = lane & 3;
    const int asw = (r >> 1) & 7, oth = (wid & 1) ? -4096 : 4096;
    lds_cp ab[4];
#pragma unroll
    for (int k = 0; k < 4; ++k) ab[k] = lds + (wid & 1) * 4096 + r * 128 + (((2 * k + hh) ^ asw) << 4);
    const lds_cp bb = lds + 65536 + (8 * hh + qq) * ZS + wid * 64 + blk * 32 + p * 8;
    const lds_cp bx = bb + (8 + (wid >> 1) - wid) * 64;
    const bool w3 = wid < 6;
#pragma unroll
    for (int i = 0; i < 3; ++i) acc[i] = f32x16{};
    bf16x8 fa[2][2][2], fb[2][2][2];
#define Z_READ(set, slot, k0, aoff_) do { _Pragma("unroll") for (int k_ = 0; k_ < 2; ++k_) { \
        fa[set][k_][0] = ld128(ab[(k0) + k_] + (aoff_)); fa[set][k_][1] = ld128(ab[(k0) + k_] + oth + (aoff_)); \
        fb[set][k_][0] = ldtr2(bb + (slot) * ZST + k_ * 16 * ZS, 4 * ZS); if (w3) fb[set][k_][1] = ldtr2(bx + (slot) * ZST + k_ * 16 * ZS, 4 * ZS); } } while (0)
#define Z_MMA(set, k_) do { acc[0] = MFMA32(fb[set][k_][0], fa[set][k_][0], acc[0]); acc[1] = MFMA32(fb[set][k_][0], fa[set][k_][1], acc[1]); \
        if (w3) acc[2] = MFMA32(fb[set][k_][1], fa[set][k_][0], acc[2]); } while (0)
#define Z_SYNC(n) asm volatile("s_waitcnt vmcnt(" #n ") lgkmcnt(0)\n\ts_barrier" ::: "memory")
    GP(7);
    Z_SYNC(11);
    GP(8);
    Z_READ(0, 0, 0, 0);
    const char* g = wg + 128 * WROW;
#pragma unroll 1
    for (int kt = 0; kt < 3; ++kt) {
        Z_SYNC(6); Z_READ(1, 1, 2, 0);     SBAR(); Z_MMA(0, 0); SBAR(); Z_STAGE(0, g);             SBAR(); Z_MMA(0, 1); SBAR();
        Z_SYNC(6); Z_READ(0, 2, 0, 8192);  SBAR(); Z_MMA(1, 0); SBAR(); Z_STAGE(1, g + 32 * WROW); SBAR(); Z_MMA(1, 1); SBAR();
        Z_SYNC(6); Z_READ(1, 3, 2, 8192);  SBAR(); Z_MMA(0, 0); SBAR(); Z_STAGE(2, g + 64 * WROW); SBAR(); Z_MMA(0, 1); SBAR();
        Z_SYNC(6); Z_READ(0, 0, 0, 16384); SBAR(); Z_MMA(1, 0); SBAR(); Z_STAGE(3, g + 96 * WROW); SBAR(); Z_MMA(1, 1); SBAR();
        g += 128 * WROW;
#pragma unroll
        for (int k = 0; k < 4; ++k) ab[k] += 16384;
    }
    if (wid == 0) dma_words(lane * 4, fx, lds0 + LDS_PARK);
    Z_SYNC(6); Z_READ(1, 1, 2, 0);    SBAR(); Z_MMA(0, 0); Z_MMA(0, 1); SBAR();
    Z_SYNC(3); Z_READ(0, 2, 0, 8192); SBAR(); Z_MMA(1, 0); Z_MMA(1, 1); SBAR();
    Z_SYNC(0); Z_READ(1, 3, 2, 8192); SBAR(); Z_MMA(0, 0); Z_MMA(0, 1); SBAR();
    Z_SYNC(0);                                Z_MMA(1, 0); Z_MMA(1, 1);
#undef Z_STAGE
#undef Z_READ
#undef Z_MMA
#undef Z_SYNC
}

struct Run { const char* A; const char* B; unsigned dA1, dA2, dB1, dB2, vA0, vA1, vB0, vB1; };
constexpr int HTB = 16384;
constexpr unsigned LDA = DM * 2, LDB = 4096 * 2;
#define G_SA(b, h) (((b) * 2 + (h)) * HTB)
#define G_SB(b, h) ((4 + (b) * 2 + (h)) * HTB)
#define G_STAGE(bufoff, gb, v0, v1) dma_pair<(bufoff), (bufoff) + 8192>((v0), (v1), (gb), ldsw)
#define G_LDA(dst, b, h) do { _Pragma("unroll") for (int m_ = 0; m_ < 2; ++m_) _Pragma("unroll") for (int k_ = 0; k_ < 4; ++k_) dst[m_][k_] = ld128(lds + G_SA(b, h) + aoff[k_] + m_ * 4096); } while (0)
#define G_LDB(dst, b, h) do { _Pragma("unroll") for (int k_ = 0; k_ < 4; ++k_) dst[k_] = ldtr2(lds + G_SB(b, h) + boff + k_ * 4096, 1024); } while (0)
#define G_MMA(ai, bj, At, Bt) do { __builtin_amdgcn_s_setprio(1); _Pragma("unroll") for (int k_ = 0; k_ < 4; ++k_) _Pragma("unroll") for (int m_ = 0; m_ < 2; ++m_) \
        acc[ai][bj][m_] = SWAP ? MFMA32(At[m_][k_], Bt[k_], acc[ai][bj][m_]) : MFMA32(Bt[k_], At[m_][k_], acc[ai][bj][m_]); __builtin_amdgcn_s_setprio(0); } while (0)

DI void g_prologue_b(const Run& u, const unsigned ldsw) {
    G_STAGE(G_SB(0, 0), u.B, u.vB0, u.vB1); G_STAGE(G_SB(0, 1), u.B + u.dB1, u.vB0, u.vB1);
}
DI void g_prologue_rest(const Run& u, const unsigned ldsw) {
    G_STAGE(G_SA(0, 0), u.A, u.vA0, u.vA1); G_STAGE(G_SA(0, 1), u.A + u.dA1, u.vA0, u.vA1);
    G_STAGE(G_SB(1, 0), u.B + u.dB2, u.vB0, u.vB1); G_STAGE(G_SA(1, 0), u.A + u.dA2, u.vA0, u.vA1); G_STAGE(G_SB(1, 1), u.B + u.dB2 + u.dB1, u.vB0, u.vB1);
}
template <int LO> DI void check_parked(const int HI, lds_cp parked, unsigned char* ws, const int fidx, const u64 tag, const int lane) {
    const unsigned w = *(const LAS unsigned*)(parked + lane * 4);
    const int sl = (lane >> 1) & 15;
    if (!__all(sl < LO || sl >= HI || w == ((lane & 1) ? (unsigned)(tag >> 32) : (unsigned)tag))) poll_flags(ws, fidx + (lane & 31), lane < 32 && (lane & 15) >= LO && (lane & 15) < HI, tag);
}

template <bool SWAP, int ROUND>
DI void g_kloop(const lds_p lds, const unsigned lds0, f32x16 (&acc)[2][2][2], const Run& cur, const Run& nxt, const int (&aoff)[4], const int boff, const unsigned ldsw,
                unsigned char* ws, const u64 tag, const int fl0, const int fatt, const int lane, const int wid, const unsigned tch) {
    bf16x8 At[2][4], B0[4], B1[4];
#pragma unroll
    for (int i = 0; i < 8; ++i) acc[i >> 2][(i >> 1) & 1][i & 1] = f32x16{};
#pragma unroll 1
    for (int t = 0; t < 8; t += 2) {
        const bool last = (t == 6);
        const char* a1 = cur.A + (t + 1) * 128 + 128 * LDA;
        const char* a2 = last ? nxt.A : cur.A + (t + 2) * 128;
        const char* b2 = last ? nxt.B : cur.B + (size_t)(t + 2) * 64 * LDB;
        const unsigned dA1 = last ? nxt.dA1 : cur.dA1, dA2 = last ? nxt.dA2 : cur.dA2, dB1 = last ? nxt.dB1 : cur.dB1, dB2 = last ? nxt.dB2 : cur.dB2;
        const unsigned vA0 = last ? nxt.vA0 : cur.vA0, vA1 = last ? nxt.vA1 : cur.vA1, vB0 = last ? nxt.vB0 : cur.vB0, vB1 = last ? nxt.vB1 : cur.vB1;
        if (ROUND == 1 && last && wid == 0) dma_words(lane * 4, ws + (size_t)fatt * 8, lds0 + 131072);
        if (t == 2 && wid == 0 && lane == 0) set_flag(ws, fl0, tag);
        G_LDB(B0, 0, 0); SBAR(); G_LDA(At, 0, 0); if (!(ROUND == 1 && t == 0)) G_STAGE(G_SA(1, 1), a1, cur.vA0, cur.vA1);
        if (ROUND == 0 && t == 2) touch(tch, nxt.A, lds0);
        if (ROUND == 1 && t == 4 && wid == 0) { check_parked<0>(8, lds + 131072, ws, fatt, tag, lane); ACQUIRE(); }
        WAIT_L(8); BAR(); WAIT_L(0); SBAR(); G_MMA(0, 0, At, B0); SBAR(); BAR();
        G_LDB(B1, 0, 1);
        G_STAGE(G_SB(0, 0), b2, vB0, vB1);
        BAR(); WAIT_L(0); SBAR(); G_MMA(0, 1, At, B1); SBAR(); BAR();
        G_LDA(At, 0, 1); G_STAGE(G_SA(0, 0), a2, vA0, vA1);
        BAR(); WAIT_L(0); SBAR(); G_MMA(1, 0, At, B0); SBAR(); BAR();
        G_STAGE(G_SB(0, 1), b2 + dB1, vB0, vB1);
        if (ROUND == 1 && t == 0) WAIT_V(22); else if ((ROUND == 1 && t == 4 && wid == 0) || (ROUND == 0 && t == 2)) WAIT_V(7); else WAIT_V(6);
        BAR(); SBAR(); G_MMA(1, 1, At, B1); SBAR(); BAR();
        if (ROUND == 1 && wid == 0) {
            if (t == 2) dma_words(lane * 4, ws + (size_t)fatt * 8, lds0 + 131072);
            if (last) check_parked<8>(12, lds + 131072, ws, fatt, tag, lane);
        }
        G_LDB(B0, 1, 0); SBAR(); G_LDA(At, 1, 0); G_STAGE(G_SA(0, 1), a2 + dA1, vA0, vA1);
        WAIT_L(8); BAR(); WAIT_L(0); SBAR(); G_MMA(0, 0, At, B0); SBAR(); BAR();
        G_LDB(B1, 1, 1); G_STAGE(G_SB(1, 0), b2 + dB2, vB0, vB1);
        BAR(); WAIT_L(0); SBAR(); G_MMA(0, 1, At, B1); SBAR(); BAR();
        G_LDA(At, 1, 1); G_STAGE(G_SA(1, 0), a2 + dA2, vA0, vA1);
        BAR(); WAIT_L(0); SBAR(); G_MMA(1, 0, At, B0); SBAR(); BAR();
        G_STAGE(G_SB(1, 1), b2 + dB2 + dB1, vB0, vB1);
        if (!(ROUND == 1 && last)) WAIT_V(6);
        BAR(); SBAR(); G_MMA(1, 1, At, B1); SBAR();
        if (!(last && wid >= 4)) BAR();
    }
}

DI void g_epilogue(const f32x16 (&acc)[2][2][2], const rsrc_t rws, const int type, const int pn, const int pm, const int wr, const int wc, const int lane) {
    constexpr float QS = 0.08838834764831845f * 1.4426950408889634f;
    const float sc = type == 2 ? QS : 1.0f;
    const unsigned base = (type == 2 ? WS_QF : WS_KVF + (type == 1 ? 8192 : 0)) + wc * 2048 + lane * 16;
    const unsigned hstride = type == 2 ? 8192 : 16384;
#pragma unroll
    for (int ai = 0; ai < 2; ++ai)
#pragma unroll
        for (int bj = 0; bj < 2; ++bj)
#pragma unroll
            for (int m = 0; m < 2; ++m) {
                const unsigned dst = base + (unsigned)((2 * pn + bj) * 64 + pm * 8 + ai * 4 + wr * 2 + m) * hstride;
                st16(rws, dst, pack8(acc[ai][bj][m], 0, sc)); st16(rws, dst + 1024, pack8(acc[ai][bj][m], 1, sc));
            }
}

#define NOP_ (void)0
#define SP_CHECK() if (t == 36) { poll_flags(ws, fatt + lane, (lane & 15) < 12, tag); ACQUIRE(); }
#define EX2(v) __builtin_amdgcn_exp2f(v)
#define MX3(a, b, c) __builtin_fmaxf(__builtin_fmaxf((a), (b)), (c))
constexpr float ATT_THR = 32.0f;
DI void att_mask(f32x16& s, int dq) {
#pragma unroll
    for (int e = 0; e < 16; ++e) if ((e & 3) + 8 * (e >> 2) > dq) s[e] = -__builtin_inff();
}

struct OutArgs { rsrc_t ra; const char* wo; const char* wu; lds_cp rd[4]; unsigned wv, uv[6], va, sc, su, ldsw; };
template <int T> DI void out_w(const OutArgs& o) {
    constexpr int S = (T % 9) * 16384;
    if constexpr (T < 16) dma_pair<S, S + 8192>(o.wv, o.wv + 32768, o.wo + T * 65536, o.ldsw);
    else if constexpr (T < 19) dma_pair<S, S + 8192>(o.uv[2 * (T - 16)], o.uv[2 * (T - 16) + 1], o.wu, o.ldsw);
}
template <int T> DI void out_a(const OutArgs& o, u32x4 (&af)[6][2]) {
    if constexpr (T < 19) {
        const unsigned so = T < 16 ? o.sc + T * 262144 : o.su + (T - 16) * 262144;
        asm volatile("buffer_load_dwordx4 %0, %1, %2, %3 offen sc1" : "=v"(af[T % 6][0]) : "v"(o.va), "s"(o.ra), "s"(so) : "memory");
        asm volatile("buffer_load_dwordx4 %0, %1, %2, %3 offen sc1" : "=v"(af[T % 6][1]) : "v"(o.va), "s"(o.ra), "s"(so + 65536) : "memory");
    }
}
constexpr int out_cnt(int i) { return (i + 8 < 19 ? 2 : 0) + (i + 5 < 19 ? 2 : 0); }
constexpr int out_allow(int t) { int n = t < 4 ? 2 * (4 - t) : 0; for (int i = t < 4 ? 0 : t - 4; i < t; ++i) n += out_cnt(i); return n; }
template <int T> DI void out_step(const OutArgs& o, u32x4 (&af)[6][2], bf16x8 (&wf)[2][4], f32x16 (&acc)[4]) {
    asm volatile("s_waitcnt vmcnt(%2)\n\ts_barrier" : "+v"(af[T % 6][0]), "+v"(af[T % 6][1]) : "n"(out_allow(T)) : "memory");
    const bf16x8 a0 = __builtin_bit_cast(bf16x8, af[T % 6][0]), a1 = __builtin_bit_cast(bf16x8, af[T % 6][1]);
#pragma unroll
    for (int c = 0; c < 4; ++c) wf[1][c] = ldtr2(o.rd[c] + (T % 9) * 16384 + 4096, 2048);
    SBAR();
    acc[0] = MFMA32(wf[0][0], a0, acc[0]); acc[1] = MFMA32(wf[0][1], a0, acc[1]); SBAR();
    out_w<T + 8>(o); SBAR();
    acc[2] = MFMA32(wf[0][2], a0, acc[2]); acc[3] = MFMA32(wf[0][3], a0, acc[3]); SBAR();
    if constexpr (T < 18) {
#pragma unroll
        for (int c = 0; c < 4; ++c) wf[0][c] = ldtr2(o.rd[c] + ((T + 1) % 9) * 16384, 2048);
    }
    SBAR();
    acc[0] = MFMA32(wf[1][0], a1, acc[0]); acc[1] = MFMA32(wf[1][1], a1, acc[1]); SBAR();
    out_a<T + 5>(o, af); SBAR();
    acc[2] = MFMA32(wf[1][2], a1, acc[2]); acc[3] = MFMA32(wf[1][3], a1, acc[3]); SBAR();
}

#define IT(h1, i1, h2, i2) ((h1) | (i1) << 2 | (h2) << 6 | (i2) << 8)
__device__ constexpr unsigned ITEMS[32] = {
    IT(0, 14, 0, 4), IT(1, 14, 1, 4), IT(2, 14, 2, 4), IT(3, 14, 3, 4), IT(0, 13, 0, 5), IT(1, 13, 1, 5), IT(2, 13, 2, 5), IT(3, 13, 3, 5),
    IT(0, 12, 0, 6), IT(1, 12, 1, 6), IT(2, 12, 2, 6), IT(3, 12, 3, 6), IT(0, 11, 0, 7), IT(1, 11, 1, 7), IT(2, 11, 2, 7), IT(3, 11, 3, 7),
    IT(0, 15, 0, 2), IT(1, 15, 1, 2), IT(2, 15, 2, 2), IT(3, 15, 3, 2), IT(0, 9, 0, 8), IT(1, 9, 1, 8), IT(2, 9, 2, 8), IT(3, 9, 3, 8),
    IT(0, 3, 0, 0), IT(1, 3, 1, 0), IT(2, 3, 2, 0), IT(3, 3, 3, 0), IT(0, 1, 1, 1), IT(2, 1, 3, 1), IT(0, 10, 1, 10), IT(2, 10, 3, 10)
};

__global__ __launch_bounds__(512, 2) void fused_fwd(const bf16_t* __restrict__ x, const bf16_t* __restrict__ ckv, const bf16_t* __restrict__ wq, const bf16_t* __restrict__ wk,
                                                    const bf16_t* __restrict__ wv, const bf16_t* __restrict__ wo, const bf16_t* __restrict__ w_in, const bf16_t* __restrict__ wsp,
                                                    const bf16_t* __restrict__ bsp, const bf16_t* __restrict__ wout, bf16_t* __restrict__ out, unsigned char* ws) {
    __shared__ __attribute__((aligned(16))) unsigned char smem[163840];
    const lds_p lds = (lds_p)smem;
    const unsigned lds0 = (unsigned)(uintptr_t)smem;
    const int tid = threadIdx.x, wid = __builtin_amdgcn_readfirstlane(tid >> 6), lane = tid & 63;
    const int bid = blockIdx.x, x8 = bid & 7, kk = bid >> 3;
    const int r = lane & 31, hh = lane >> 5, blk = (lane >> 4) & 1, qq = (lane & 15) >> 2, p = lane & 3;
    const rsrc_t rws = __builtin_amdgcn_make_buffer_rsrc((void*)ws, (short)0, WS_END, 0x00020000);
    const unsigned ldsw = lds0 + (unsigned)wid * 1024u;
    PROF_DECL;
    dma_words(lane * 4, ws, lds0 + LDS_TAG);
    u64 tag;
#define MAKE_TAG() do { const u32x2 e_ = *(const LAS u32x2*)(lds + LDS_TAG); \
        const u64 ep_ = ((u64)(unsigned)__builtin_amdgcn_readfirstlane(e_[1]) << 32) | (unsigned)__builtin_amdgcn_readfirstlane(e_[0]); \
        u64 z_ = ep_ + 0x9E3779B97F4A7C15ull; z_ = (z_ ^ (z_ >> 30)) * 0xBF58476D1CE4E5B9ull; z_ = (z_ ^ (z_ >> 27)) * 0x94D049BB133111EBull; z_ ^= z_ >> 31; tag = z_ == ep_ ? ~z_ : z_; } while (0)
    const unsigned item = ITEMS[kk];
    const int h1 = item & 3, i1 = (item >> 2) & 15, h2 = (item >> 6) & 3, i2 = (item >> 8) & 15;
    const unsigned char* kvx = ws + WS_KVF + (size_t)x8 * 4 * 64 * 16384;
    const unsigned char* kvh = kvx + (size_t)h1 * 64 * 16384;
    const char* q1src = (const char*)ws + WS_QF + (size_t)((4 * x8 + h1) * 64 + 4 * i1 + (wid & 3)) * 8192;
    bf16x8 q[8];
#define Q1_LOAD() do { _Pragma("unroll") for (int d = 0; d < 8; ++d) asm volatile("global_load_dwordx4 %0, %1, %2 offset:%3 sc1" : "=v"(q[d]) : "v"(lane * 16 + (d >> 2) * 4096), "s"(q1src), "i"((d & 3) * 1024) : "memory"); } while (0)
#define Q1_WAIT(n) asm volatile("s_waitcnt vmcnt(" #n ")" : "+v"(q[0]), "+v"(q[1]), "+v"(q[2]), "+v"(q[3]), "+v"(q[4]), "+v"(q[5]), "+v"(q[6]), "+v"(q[7]) :: "memory")
    const int fatt = F_K + x8 * 64;
#define Q2_LOAD() do { const char* q2src = (const char*)ws + WS_QF + (size_t)((4 * x8 + h2) * 64 + 4 * i2) * 8192 + wid * 4096; \
        const unsigned lq_ = __builtin_amdgcn_readfirstlane(lds0 + wid * 4096); \
        dma_pair<131072, 131072 + 1024>(lane * 16, lane * 16 + 1024, q2src, lq_); dma_pair<131072 + 2048, 131072 + 3072>(lane * 16 + 2048, lane * 16 + 3072, q2src, lq_); } while (0)
    Run rat;
    rat.A = (const char*)kvh; rat.B = (const char*)kvh + 32768; rat.dA1 = rat.dB1 = 65536; rat.dA2 = rat.dB2 = 8192;
    rat.vA0 = rat.vB0 = wid * 1024 + lane * 16; rat.vA1 = rat.vB1 = rat.vA0 + 16384;
    int pf;
    int aoff[4];
    const unsigned arow = 8 * wid + (lane >> 3), achk = ((lane & 7) ^ (((wid & 1) << 2) + (lane >> 4))) << 4;
    const unsigned brow = 4 * wid + (lane >> 4), bchk = ((lane & 15) ^ (((lane >> 4) & 3) << 2)) << 4;

    if (kk < 24) {
        const int type = kk >> 3, pn = 2 * x8 + ((kk >> 2) & 1), pm0 = (kk & 3) + (type == 2 ? 4 : 0), pm1 = pm0 ^ 4, wr = wid >> 2, wc = wid & 3;
        Run u0, u1;
        const char* Ab = type == 2 ? (const char*)ws + WS_XN : (const char*)ckv;
        u0.B = (const char*)(type == 0 ? wk : type == 1 ? wv : wq) + pn * 512; u0.A = Ab + (size_t)pm0 * 256 * LDA;
        u0.dA1 = 128 * LDA; u0.dA2 = 128; u0.dB1 = 256; u0.dB2 = 64 * LDB;
        u0.vA0 = arow * LDA + achk; u0.vA1 = u0.vA0 + 64 * LDA; u0.vB0 = brow * LDB + bchk; u0.vB1 = u0.vB0 + 32 * LDB;
        u1 = u0; u1.A = Ab + (size_t)pm1 * 256 * LDA;
        const unsigned tch = (type == 2 ? ((kk >> 2) & 1) * 2 : type * 2 + ((kk >> 2) & 1)) * 65536 + tid * 128, bl = ((kk & 3) * 128 + wid * 16 + (lane >> 2)) * LDB + (lane & 3) * 128;
        if (type < 2) {
            g_prologue_b(u0, ldsw); g_prologue_rest(u0, ldsw);
            PROF(1);
            WAIT_V(6);
            MAKE_TAG();
        } else {
            const int j = x8 * 8 + (kk - 16), row = 32 * j + 4 * wid + (lane >> 4);
            const u32x4* src = (const u32x4*)(x + (size_t)row * DM) + (lane & 15);
            u32x4 v[4];
#pragma unroll
            for (int i = 0; i < 4; ++i) v[i] = src[16 * i];
            SBAR();
            g_prologue_b(u0, ldsw); touch(bl, u0.B, lds0);
            SBAR();
            float s = 0.f;
#pragma unroll
            for (int i = 0; i < 4; ++i)
#pragma unroll
                for (int e = 0; e < 4; ++e) s += bflo(v[i][e]) + bfhi(v[i][e]);
            s += __shfl_xor(s, 1); s += __shfl_xor(s, 2); s += __shfl_xor(s, 4); s += __shfl_xor(s, 8);
            const float mean = s * (1.0f / DM);
            float q = 0.f;
#pragma unroll
            for (int i = 0; i < 4; ++i)
#pragma unroll
                for (int e = 0; e < 4; ++e) { const float a = bflo(v[i][e]) - mean, b = bfhi(v[i][e]) - mean; q += a * a + b * b; }
            q += __shfl_xor(q, 1); q += __shfl_xor(q, 2); q += __shfl_xor(q, 4); q += __shfl_xor(q, 8);
            const float rstd = 1.0f / sqrtf(q * (1.0f / DM) + 1e-5f);
#pragma unroll
            for (int i = 0; i < 4; ++i) {
                u32x4 o;
#pragma unroll
                for (int e = 0; e < 4; ++e) o[e] = cvtpk((bflo(v[i][e]) - mean) * rstd, (bfhi(v[i][e]) - mean) * rstd);
                st16(rws, WS_XN + row * 1024 + ((lane & 15) + 16 * i) * 16, o);
            }
            WAIT_V(0);
            MAKE_TAG();
            BAR();
            if (tid == 0) set_flag(ws, F_X + j, tag);
            if (wid == 0) { poll_flags(ws, F_X + ((lane & 8) ? pm1 : pm0) * 8 + (lane & 7), lane < 16, tag); ACQUIRE(); WAIT_V(0); }
            BAR();
            g_prologue_rest(u0, ldsw);
            PROF(1);
            WAIT_V(6);
        }
#pragma unroll
        for (int k = 0; k < 4; ++k) aoff[k] = (wr * 64 + r) * 128 + (((2 * k + hh) ^ ((r >> 1) & 7)) << 4);
        const int boff = (8 * hh + qq) * 256 + ((wc ^ qq) << 6) + blk * 32 + p * 8;
        BAR();
        if (wr == 1) BAR();
        PROF(2);
        f32x16 acc[2][2][2];
        const int fk = fatt + (pn & 1) * 16 + type * 4 + (kk & 3);
        if (type == 1) g_kloop<true, 0>(lds, lds0, acc, u0, u1, aoff, boff, ldsw, ws, tag, F_S + bid, fatt, lane, wid, tch);
        else           g_kloop<false, 0>(lds, lds0, acc, u0, u1, aoff, boff, ldsw, ws, tag, F_S + bid, fatt, lane, wid, tch);
        PROF(3);
        G_STAGE(G_SA(1, 1), u1.A + 128 + 128 * LDA, u1.vA0, u1.vA1);
        SBAR();
        g_epilogue(acc, rws, type, pn, pm0, wr, wc, lane);
        PROF(11);
        if (wr == 1) BAR();
        if (type == 1) g_kloop<true, 1>(lds, lds0, acc, u1, rat, aoff, boff, ldsw, ws, tag, fk, fatt, lane, wid, tch);
        else           g_kloop<false, 1>(lds, lds0, acc, u1, rat, aoff, boff, ldsw, ws, tag, fk, fatt, lane, wid, tch);
        PROF(4);
        Q1_LOAD();
        G_STAGE(G_SA(1, 1), rat.A + rat.dA1 + rat.dA2, rat.vA0, rat.vA1);
        SBAR();
        g_epilogue(acc, rws, type, pn, pm1, wr, wc, lane);
        PROF(12);
        pf = fk + 32;
        Q1_WAIT(18);
    } else {
        const int pm = x8, pn = kk - 24, grp = wid >> 2;
        constexpr unsigned SLA = SEQ * 2, SLB = 768 * 2, WROW = FF * 2;
        constexpr int RB = 65536, RA2 = 131072;
        const char* Ap = (const char*)ws + WS_XN + (size_t)pm * 256 * LDA;
        const char* Bp = (const char*)w_in + pn * 256;
        unsigned vA0 = arow * LDA + achk, vA1 = vA0 + 64 * LDA, vB0 = brow * WROW + bchk, vB1 = vB0 + 32 * WROW, dA = 128 * LDA, dB = 64 * WROW;
        const int tb0 = 8 * pm + 2 * (wid & 3), fs0 = 8 * pn + 4 * grp;
        float bias[2];
        const int flz = F_L + (4 * x8 + (pn >> 1)) * 2 + (kk & 1);
        {
            f32x16 za[3];
            const int rg = 4 * x8 + (pn >> 1), hf = kk & 1, m0_ = wid & 1;
#pragma unroll
            for (int m = 0; m < 2; ++m) bias[m] = __uint_as_float((unsigned)bsp[(tb0 + m) * 32 + r] << 16);
            z2_half(lds, lds0, za, x, w_in, ws + (F_X + 8 * pm) * 8, rg, hf, tid, wid, lane
                );
            PIN(bias[0]); PIN(bias[1]);
            PROF(3);
            MAKE_TAG();
            if (wid == 0 && kk < 30) {
                const unsigned w = *(const LAS unsigned*)(lds + LDS_PARK + lane * 4);
                if (!__all(lane >= 16 || w == ((lane & 1) ? (unsigned)(tag >> 32) : (unsigned)tag))) poll_flags(ws, F_X + 8 * pm + (lane & 7), true, tag);
                ACQUIRE();
            }
            {
                const char* slab = (const char*)wsp + (size_t)x8 * 1048576 + pn * 131072;
                TOUCH_S(tid * 128, slab, lds0); TOUCH_S(tid * 128 + 65536, slab, lds0);
            }
            u32x4 zp[3][2];
#pragma unroll
            for (int i = 0; i < 3; ++i) { zp[i][0] = gelu8(za[i], 0); zp[i][1] = gelu8(za[i], 1); }
            const int nx = wid < 4 ? 8 : wid < 6 ? 6 : 0;
            LAS float* red = (LAS float*)lds;
            float s[2] = {0.f, 0.f}, mean[2];
#pragma unroll
            for (int i = 0; i < 24; ++i) if (i < 16 || i - 16 < nx) s[(i >> 3) & 1] = sum_pair(zp[i >> 3][(i >> 2) & 1][i & 3], s[(i >> 3) & 1]);
            red[wid * 128 + m0_ * 64 + lane] = s[0]; red[wid * 128 + (m0_ ^ 1) * 64 + lane] = s[1];
            WAIT_L(0); BAR();
#pragma unroll
            for (int m = 0; m < 2; ++m) { float t = 0.f;
#pragma unroll
                for (int w = 0; w < 8; ++w) t += red[w * 128 + (m0_ ^ m) * 64 + lane];
                mean[m] = swap_add(t) * (2.0f / FH); }
            f32x2 d[24], q2[2] = {{0.f, 0.f}, {0.f, 0.f}};
#pragma unroll
            for (int i = 0; i < 24; ++i) { const int m = (i >> 3) & 1; d[i] = unpk(zp[i >> 3][(i >> 2) & 1][i & 3]) - (f32x2){mean[m], mean[m]};
                if (i < 16 || i - 16 < nx) q2[m] = __builtin_elementwise_fma(d[i], d[i], q2[m]); }
            red[1024 + wid * 128 + m0_ * 64 + lane] = q2[0][0] + q2[0][1]; red[1024 + wid * 128 + (m0_ ^ 1) * 64 + lane] = q2[1][0] + q2[1][1];
            GP(9);
            WAIT_V(2); WAIT_L(0); BAR();
            LAS f32x2* tab = (LAS f32x2*)(lds + 160768);
            if (wid == 0) {
#pragma unroll
                for (int m = 0; m < 2; ++m) { float t = 0.f;
#pragma unroll
                    for (int w = 0; w < 8; ++w) t += red[1024 + w * 128 + m * 64 + lane];
                    s[m] = swap_add(t); }
                const float mo = hh ? mean[1] : mean[0], qo = hh ? s[1] : s[0];
                const unsigned t32 = (unsigned)tag;
                st16(rws, WS_G + (rg * 2 + hf) * 1024 + lane * 16, (u32x4){__float_as_uint(mo), t32, __float_as_uint(qo), t32});
                u32x4 o;
#pragma unroll 1
                for (unsigned spins = 0; spins < (1u << 17); ++spins) {
                    o = ld16(rws, WS_G + (rg * 2 + (hf ^ 1)) * 1024 + lane * 16);
                    if (__all(o[1] == t32 && o[3] == t32)) break;
                    __builtin_amdgcn_s_sleep(2);
                }
                const float mp = __uint_as_float(o[0]), dl = mo - mp;
                tab[lane] = (f32x2){0.5f * (mo + mp), 1.0f / sqrtf((qo + __uint_as_float(o[2]) + dl * dl * (FH * 0.25f)) * (1.0f / FH) + 1e-5f)};
            }
            WAIT_L(0); BAR();
            GP(10);
            if (kk < 30) {
                dma_pair<0, 8192>(vA0, vA1, Ap, ldsw); dma_pair<16384, 24576>(vA0, vA1, Ap + 128 * LDA, ldsw); dma_pair<RB, RB + 8192>(vB0, vB1, Bp, ldsw);
                dma_pair<32768, 32768 + 8192>(vA0, vA1, Ap + 128, ldsw); dma_pair<32768 + 16384, 32768 + 24576>(vA0, vA1, Ap + 128 + 128 * LDA, ldsw);
                dma_pair<RB + 16384, RB + 16384 + 8192>(vB0, vB1, Bp + 64 * WROW, ldsw); dma_pair<RB + 32768, RB + 32768 + 8192>(vB0, vB1, Bp + 128 * WROW, ldsw);
            }
            f32x2 rs[2], of[2];
#pragma unroll
            for (int m = 0; m < 2; ++m) { const f32x2 t = tab[(m0_ ^ m) * 32 + r]; const float o = (mean[m] - t[0]) * t[1]; rs[m] = (f32x2){t[1], t[1]}; of[m] = (f32x2){o, o}; }
            const lds_p tr = lds + 114688;
#pragma unroll
            for (int i = 0; i < 12; ++i)
                if (i < 8 || 2 * (i - 8) < nx) {
                    const int b = i >> 2, m = b & 1;
                    *(LAS u32x2*)(tr + ((m0_ ^ m) * 32 + r) * 720 + (b == 2 ? 8 + (wid >> 1) : wid) * 64 + (i & 3) * 16 + hh * 8) =
                        (u32x2){cvt2(__builtin_elementwise_fma(d[2 * i], rs[m], of[m])), cvt2(__builtin_elementwise_fma(d[2 * i + 1], rs[m], of[m]))};
                }
            WAIT_L(0); BAR();
#pragma unroll
            for (int i = 0; i < 6; ++i) { int c = tid + 512 * i; const bool ok = c < 2752; c = ok ? c : 0; const int zr = c / 43, zc = c - zr * 43;
                st16(rws, ok ? WS_Z2N + (rg * 64 + zr) * SLB + hf * 688 + zc * 16 : 0x80000000u, *(const LAS u32x4*)(tr + zr * 720 + zc * 16)); }
#pragma unroll
            for (int i = 0; i < 2; ++i) { const int c = tid + 512 * i, zr = c / 10;
                st16(rws, hf == 1 && c < 640 ? WS_Z2N + (rg * 64 + zr) * SLB + 1376 + (c - zr * 10) * 16 : 0x80000000u, (u32x4){0u, 0u, 0u, 0u}); }
        }
        PROF(1);
        if (kk < 30) {
            u32x4 z1[2][2][2];
#pragma unroll
            for (int k = 0; k < 4; ++k) aoff[k] = ((wid >> 1) & 1) * 16384 + ((wid & 1) * 64 + r) * 128 + (((2 * k + hh) ^ ((r >> 1) & 7)) << 4);
            const int bo0 = RB + (8 * hh + qq) * 256 + (((2 * grp) ^ qq) << 6) + blk * 32 + p * 8, bo1 = RB + (8 * hh + qq) * 256 + (((2 * grp + 1) ^ qq) << 6) + blk * 32 + p * 8;
            f32x16 acc[2][2];
#pragma unroll
            for (int c = 0; c < 4; ++c) acc[c >> 1][c & 1] = f32x16{};
#pragma unroll
            for (int c = 0; c < 8; ++c) z1[c >> 2][(c >> 1) & 1][c & 1] = (u32x4){0u, 0u, 0u, 0u};
            Ap += 256; Bp += 192 * WROW;
            unsigned sa = 0, sa1 = 32768, sa2 = RA2;
            unsigned la = ldsw + RA2, la1 = ldsw, la2 = ldsw + 32768, lb = 49152;
            const char* Aq = Ap; unsigned lq = la, lq1 = la1, lq2 = la2, vQ0 = vA0, vQ1 = vA1, dQ = dA;
#define SP_SRC(s_) do { if ((s_) == 6) { Ap = (const char*)wsp + (size_t)pm * 256 * SLA; vA1 = arow * SLA + achk + 64 * SLA; dA = 128 * SLA; } \
                if ((s_) == 5) { Bp = (const char*)ws + WS_Z2N + pn * 256; vB0 = brow * SLB + bchk; vB1 = vB0 + 32 * SLB; dB = 64 * SLB; } \
                if (t == 6) { Aq = (const char*)wsp + (size_t)pm * 256 * SLA; vQ0 = arow * SLA + achk; vQ1 = vQ0 + 64 * SLA; dQ = 128 * SLA; } } while (0)
#define SP_LATE() do { dma_pair<0, 8192>(vQ0, vQ1, Aq, lq); dma_one<16384>(vQ0, Aq + dQ, lq); Aq += 128; const unsigned l_ = lq; lq = lq1; lq1 = lq2; lq2 = l_; } while (0)
#define SP_A1() dma_one<24576>(vA1, Ap + dA, la)
#define SP_B0() dma_one<RB>(vB0, Bp, ldsw + lb)
#define SP_B1() dma_one<RB + 8192>(vB1, Bp, ldsw + lb)
#define SP_ADV() do { Ap += 128; Bp += dB; const unsigned l_ = la; la = la1; la1 = la2; la2 = l_; lb = (lb + 16384) & 49152; } while (0)
#define SP_GELU() do { _Pragma("unroll") for (int c = 0; c < 4; ++c) { f32x16& a = acc[c >> 1][c & 1]; z1[c >> 1][c & 1][0] = gelu8(a, 0); z1[c >> 1][c & 1][1] = gelu8(a, 1); a = f32x16{}; } } while (0)
#define SP_MMA(k) do { _Pragma("unroll") for (int c = 0; c < 4; ++c) acc[c >> 1][c & 1] = MFMA32(Bt[c & 1][k], At[c >> 1][k], acc[c >> 1][c & 1]); } while (0)
#define SP_STEP(PRE, LATE, WAIT0, WAIT1, D0, D1, D2, POST, LASTBAR) do { \
                bf16x8 At[2][4], Bt[2][4]; \
                PRE; \
                const lds_cp pa = lds + sa, pb = lds + (t & 3) * 16384; \
                _Pragma("unroll") for (int k = 0; k < 4; ++k) { Bt[0][k] = ldtr2(pb + bo0 + k * 4096, 1024); At[0][k] = ld128(pa + aoff[k]); At[1][k] = ld128(pa + aoff[k] + 4096); Bt[1][k] = ldtr2(pb + bo1 + k * 4096, 1024); } \
                LATE; \
                if (grp == 1) { WAIT1; } \
                WAIT_L(0); BAR(); SBAR(); \
                __builtin_amdgcn_s_setprio(1); \
                SP_MMA(0); SBAR(); D0; SBAR(); SP_MMA(1); SBAR(); D1; SBAR(); SP_MMA(2); SBAR(); D2; SBAR(); SP_MMA(3); \
                __builtin_amdgcn_s_setprio(0); \
                SBAR(); \
                POST; \
                if (grp == 0) { WAIT0; } \
                if (LASTBAR) BAR(); \
                const unsigned s_ = sa; sa = sa1; sa1 = sa2; sa2 = s_; } while (0)
            asm volatile("s_waitcnt vmcnt(16) lgkmcnt(0)\n\ts_barrier" ::: "memory");
            if (grp == 1) { SP_A1(); SP_B0(); SP_B1(); SP_ADV(); BAR(); }
            PROF(2);
            int t = 0;
#pragma unroll 1
            for (; t < 8; ++t)
                SP_STEP({ if (wid == 0) { if (t == 2 && lane == 0) { set_flag(ws, flz, tag); set_flag(ws, F_S + bid, tag); } if (t == 3) { poll_flags(ws, F_L + lane, true, tag); ACQUIRE(); } } SP_SRC(t + grp); },
                        SP_LATE(), { if (t == 0) WAIT_V(16); else WAIT_V(8); }, { if (t == 0) WAIT_V(16); else WAIT_V(6); }, SP_A1(), SP_B0(), SP_B1(), { SP_ADV(); if (t == 7 && grp == 1) SP_GELU(); }, true);
            PROF(11);
            if (grp == 0) SP_GELU();
            PROF(12);
#pragma unroll 1
            for (; t < 36; ++t) SP_STEP(NOP_, SP_LATE(), WAIT_V(8), WAIT_V(6), SP_A1(), SP_B0(), SP_B1(), SP_ADV(), true);
#pragma unroll 1
            for (; t < 40; ++t)
                SP_STEP({ if (wid == 0) { SP_CHECK(); } }, if (t < 38) SP_LATE(),
                        { if (t == 36) WAIT_V(8); else if (t == 37) WAIT_V(6); else WAIT_V(0); }, { if (t == 36) WAIT_V(6); else if (t == 37) WAIT_V(4); else WAIT_V(0); },
                        if (t + grp < 38) SP_A1(), if (t + grp < 37) SP_B0(), if (t + grp < 37) SP_B1(), SP_ADV(), !(t == 39 && grp == 1));
            PROF(4);
            Q1_LOAD();
            G_STAGE(G_SA(0, 0), rat.A, rat.vA0, rat.vA1); G_STAGE(G_SB(0, 0), rat.B, rat.vA0, rat.vA1); G_STAGE(G_SA(1, 0), rat.A + 8192, rat.vA0, rat.vA1); G_STAGE(G_SB(1, 0), rat.B + 8192, rat.vA0, rat.vA1);
            G_STAGE(G_SA(0, 1), rat.A + 65536, rat.vA0, rat.vA1); G_STAGE(G_SB(0, 1), rat.B + 65536, rat.vA0, rat.vA1); G_STAGE(G_SA(1, 1), rat.A + 73728, rat.vA0, rat.vA1); G_STAGE(G_SB(1, 1), rat.B + 73728, rat.vA0, rat.vA1);
            Q2_LOAD();
            SBAR();
#pragma unroll
            for (int m = 0; m < 2; ++m)
#pragma unroll
                for (int c = 0; c < 4; ++c) {
                    const f32x16& a = acc[m][c >> 1]; const u32x4 z = z1[m][c >> 1][c & 1]; const int s8 = 8 * (c & 1);
                    u32x4 o;
#pragma unroll
                    for (int e = 0; e < 4; ++e) o[e] = cvtpk(bflo(z[e]) * (a[s8 + 2 * e] + bias[m]), bfhi(z[e]) * (a[s8 + 2 * e + 1] + bias[m]));
                    st16(rws, fs0 + c < 43 ? WS_AF + ((256 + fs0 + c) * 64 + tb0 + m) * 1024 + lane * 16 : 0x80000000u, o);
                }
            pf = F_U + pm * 8 + pn;
            Q1_WAIT(20);
        } else {
            WAIT_V(0); WAIT_L(0); BAR();
            if (tid == 0) { set_flag(ws, flz, tag); set_flag(ws, F_S + bid, tag); }
            {
                const int aw = x8 * 2 + (kk - 30);
#pragma unroll
                for (int i = 0; i < 4; ++i) touch(tid * 128 + i * 65536, (const char*)wo + aw * 262144, lds0);
                if (tid < 344) touch(tid * 128, (const char*)wout + aw * 44032, lds0);
            }
            if (wid == 0) { poll_flags(ws, fatt + (lane & 31), lane < 32 && (lane & 15) < 12, tag); ACQUIRE(); }
            WAIT_V(0); BAR();
            PROF(4);
            g_prologue_b(rat, ldsw); g_prologue_rest(rat, ldsw); G_STAGE(G_SA(1, 1), rat.A + rat.dA1 + rat.dA2, rat.vA0, rat.vA1);
            Q1_LOAD();
            pf = -1;
            Q1_WAIT(0);
        }
    }
    BAR();
    PROF(5);
    {
        const int kh = wid >> 2, rg = wid & 3;
        const bool early = kk < 24 || kk >= 30;
        const rsrc_t rkv = __builtin_amdgcn_make_buffer_rsrc((void*)kvx, (short)0, 4 * 64 * 16384, 0x00020000);
        const unsigned dv0 = rg * 2048 + lane * 16 + kh * 32768;
        const unsigned wrapoff = (unsigned)(h1 * 16 + i1 + 1) * 65536u, wrapto = (unsigned)h2 << 20;
        unsigned sA_ = 0, sB_ = ((unsigned)h1 << 20) + 65536u, lA = lds0 + kh * 65536 + rg * 2048, lB = lA + 16384;
#define A_ADV() do { sA_ = sB_; sB_ += 65536u; if (sB_ == wrapoff) sB_ = wrapto; const unsigned t_ = lA; lA = lB; lB = t_; } while (0)
#define D_KO(h) (bdma1<8192, (h) * 1024>(dv0, rkv, sA_ + 16384, lA))
#define D_VE(h) (bdma1<32768, (h) * 1024>(dv0, rkv, sA_ + 8192, lA))
#define D_KE(h) (bdma1<0, (h) * 1024>(dv0, rkv, sB_, lB))
#define D_VO(h) (bdma1<40960, (h) * 1024>(dv0, rkv, sA_ + 24576, lA))
#define PUBLISH() do { if (pf >= 0) { if (wid == 0 && lane == 0) set_flag(ws, pf, tag); pf = -1; } } while (0)
        const lds_cp qrd = lds + 131072 + rg * 8192 + lane * 16;
        const lds_cp rbase = lds + kh * 65536 + lane * 16;
        int so = 0;
#pragma unroll 1
        for (int blkno = 0; blkno < 2; ++blkno) {
            const int ib = blkno ? i2 : i1, head = 4 * x8 + (blkno ? h2 : h1);
            const int qrow = 128 * ib + 32 * rg + r - 4 * hh - 64 * kh;
            f32x16 o[4], sA, sB, negm;
            bf16x8 fr[8];
            u32x4 pw0, pw1;
            float mref, l0 = 0.f, l1 = 0.f, resc = 1.0f, ma, mb;
            bool dead = false, pend = false;
            lds_cp pc = rbase + so, pn = rbase + (so ^ 16384);
#pragma unroll
            for (int d = 0; d < 8; ++d) fr[d] = ld128(pc + d * 1024);
            sA = f32x16{};
#pragma unroll
            for (int d = 0; d < 8; ++d) sA = MFMA32(fr[d], q[d], sA);
            asm volatile("s_waitcnt lgkmcnt(0)\n\ts_barrier" ::: "memory");
            A_ADV();
            D_KO(0); D_KO(1);
            if (blkno == 1 && wid < 5) {
                if (wid < 4) touch(lane * 128, (const char*)wo + (size_t)(x8 >> 1) * 1048576 + kk * 32768 + wid * 8192, lds0);
                else { const unsigned ur = 192 * (x8 >> 1) + 6 * kk + (lane >> 3); touch((ur > FH - 1 ? FH - 1 : ur) * 1024 + (lane & 7) * 128, wout, lds0); }
            }
#pragma unroll
            for (int d = 0; d < 4; ++d) fr[d] = ld128(pc + 8192 + d * 1024);
#pragma unroll
            for (int d = 0; d < 4; ++d) o[d] = f32x16{};
            if (ib == 0) att_mask(sA, qrow);
            {
                float m = MX3(sA[0], sA[1], sA[2]);
#pragma unroll
                for (int e = 3; e < 15; e += 2) m = MX3(m, sA[e], sA[e + 1]);
                m = swap_max(__builtin_fmaxf(m, sA[15]));
                dead = m == -__builtin_inff();
                mref = dead ? 0.f : m;
#pragma unroll
                for (int e = 0; e < 16; ++e) { sA[e] -= mref; negm[e] = -mref; }
                PIN(negm);
#pragma unroll
                for (int e = 0; e < 6; ++e) sA[e] = EX2(sA[e]);
                pw0[0] = cvtpk(sA[0], sA[1]); pw0[1] = cvtpk(sA[2], sA[3]);
            }
#define A_RD(j, ptr) do { fr[j] = ld128(ptr); } while (0)
#define ANYCHK(x) (x)
#define XE(S, i) S[i] = EX2(S[i])
#define A_P1(SC, SN, KP, VP, E0, E1, E2) do { \
            A_RD(4, (KP) + 4096); SBAR(); SN = MFMA32(fr[0], q[0], negm); XE(SC, 6); XE(SC, 7); pw0[2] = cvtpk(SC[4], SC[5]); PIN(SC); PIN(pw0); SBAR(); \
            A_RD(5, (KP) + 5120); SBAR(); SN = MFMA32(fr[1], q[1], SN); XE(SC, 8); XE(SC, 9); pw0[3] = cvtpk(SC[6], SC[7]); PIN(SC); PIN(pw0); E0; SBAR(); \
            A_RD(6, (KP) + 6144); SBAR(); SN = MFMA32(fr[2], q[2], SN); XE(SC, 10); XE(SC, 11); l0 += SC[0]; PIN(SC); PIN(l0); SBAR(); \
            A_RD(7, (KP) + 7168); SBAR(); SN = MFMA32(fr[3], q[3], SN); XE(SC, 12); XE(SC, 13); l1 += SC[1]; PIN(SC); PIN(l1); SBAR(); \
            A_RD(0, (VP));        SBAR(); SN = MFMA32(fr[4], q[4], SN); XE(SC, 14); XE(SC, 15); pw1[0] = cvtpk(SC[8], SC[9]); PIN(SC); PIN(pw1); E1; SBAR(); \
            A_RD(1, (VP) + 2048); SBAR(); SN = MFMA32(fr[5], q[5], SN); l0 += SC[2]; l1 += SC[3]; l0 += SC[4]; l1 += SC[5]; l0 += SC[6]; PIN(l0); PIN(l1); SBAR(); \
            A_RD(2, (VP) + 4096); SBAR(); SN = MFMA32(fr[6], q[6], SN); l1 += SC[7]; l0 += SC[8]; l1 += SC[9]; l0 += SC[10]; pw1[1] = cvtpk(SC[10], SC[11]); PIN(l0); PIN(l1); PIN(pw1); SBAR(); \
            A_RD(3, (VP) + 6144); SBAR(); SN = MFMA32(fr[7], q[7], SN); l1 += SC[11]; l0 += SC[12]; l1 += SC[13]; pw1[2] = cvtpk(SC[12], SC[13]); PIN(l0); PIN(l1); PIN(pw1); E2; SBAR(); } while (0)
#define PWF(x) __builtin_bit_cast(bf16x8, x)
#define A_P2A(SC, SN, VP, MASK, DQ) do { \
            A_RD(4, (VP) + 1024); A_RD(5, (VP) + 3072); SBAR(); o[0] = MFMA32(fr[0], PWF(pw0), o[0]); l0 += SC[14]; l1 += SC[15]; pw1[3] = cvtpk(SC[14], SC[15]); PIN(l0); PIN(l1); PIN(pw1); SBAR(); \
            A_RD(6, (VP) + 5120); A_RD(7, (VP) + 7168); SBAR(); o[1] = MFMA32(fr[1], PWF(pw0), o[1]); \
            if (MASK) att_mask(SN, DQ); \
            ma = MX3(SN[0], SN[1], SN[2]); mb = MX3(SN[3], SN[4], SN[5]); PIN(ma); PIN(mb); SBAR(); \
            o[2] = MFMA32(fr[2], PWF(pw0), o[2]); ma = MX3(ma, SN[6], SN[7]); mb = MX3(mb, SN[8], SN[9]); PIN(ma); PIN(mb); ma = MX3(ma, SN[10], SN[11]); PIN(ma); SBAR(); \
            o[3] = MFMA32(fr[3], PWF(pw0), o[3]); mb = MX3(mb, SN[12], SN[13]); ma = MX3(ma, SN[14], SN[15]); PIN(ma); PIN(mb); SBAR(); } while (0)
#define A_CHECK(SN) do { if (__builtin_expect(ANYCHK((__builtin_amdgcn_fcmpf(ma, ATT_THR, 2) | __builtin_amdgcn_fcmpf(mb, ATT_THR, 2)) != 0), 0)) {            \
                const float dl_ = __builtin_fmaxf(swap_max(__builtin_fmaxf(ma, mb)), 0.f); mref += dl_; resc = EX2(-dl_); l0 *= resc; l1 *= resc; pend = true; \
                _Pragma("unroll") for (int e = 0; e < 16; ++e) { SN[e] -= dl_; negm[e] = -mref; } PIN(negm); } SBAR(); } while (0)
#define A_RESC() do { if (__builtin_expect(pend, 0)) { pend = false;         \
            _Pragma("unroll") for (int d = 0; d < 4; ++d) _Pragma("unroll") for (int e = 0; e < 16; ++e) o[d][e] *= resc; } } while (0)
#define A_P2B(SN, RD0, RD1, RD2, RD3, D0, D1) do { \
            RD0; SBAR(); o[0] = MFMA32(fr[4], PWF(pw1), o[0]); SBAR(); A_CHECK(SN); D0; SBAR(); \
            RD1; SBAR(); o[1] = MFMA32(fr[5], PWF(pw1), o[1]); XE(SN, 0); XE(SN, 1); PIN(SN); SBAR(); \
            RD2; SBAR(); o[2] = MFMA32(fr[6], PWF(pw1), o[2]); XE(SN, 2); XE(SN, 3); pw0[0] = cvtpk(SN[0], SN[1]); PIN(SN); PIN(pw0); D1; SBAR(); \
            RD3; SBAR(); o[3] = MFMA32(fr[7], PWF(pw1), o[3]); XE(SN, 4); XE(SN, 5); pw0[1] = cvtpk(SN[2], SN[3]); PIN(SN); PIN(pw0); SBAR(); \
            A_RESC(); } while (0)
#define A_P2K(SN, KN, D0, D1) A_P2B(SN, A_RD(0, (KN)), A_RD(1, (KN) + 1024), A_RD(2, (KN) + 2048), A_RD(3, (KN) + 3072), D0, D1)
#define UNP(...) __VA_ARGS__
#define PROFO(i) PROF(i)
#pragma unroll 1
            for (int n = 0; n < ib; ++n) {
                A_P1(sA, sB, pc + 8192, pc + 32768, D_VE(0), D_VE(1), D_KE(0));
                A_P2A(sA, sB, pc + 32768, false, 0);
                A_P2K(sB, pn, D_KE(1), D_VO(0));
                A_P1(sB, sA, pn, pc + 32768 + 8192, D_VO(1), NOP_, NOP_);
                A_P2A(sB, sA, pc + 32768 + 8192, (n == ib - 1), qrow - 128 * ib);
                const int ev = blkno == 0 && (early || n == 0) ? n : 6;
                if (ev != 0) WAIT_V(0); else if (kk < 24) WAIT_V(24); else WAIT_V(16);
                asm volatile("s_waitcnt lgkmcnt(0)\n\ts_barrier" ::: "memory");
#define A_EV() do { A_ADV(); if (ev != 0) PUBLISH(); \
                if (ev < 6) { \
                    if (ev == 3 && wid == 0) dma_words(lane * 4, ws + (size_t)(fatt + 32) * 8, lds0 + 131072); \
                    if (ev == 4 && wid == 0) { check_parked<0>(i2 >= 8 ? 8 : 12, lds + 131072, ws, fatt + 32, tag, lane); ACQUIRE(); } \
                    if (ev == 5) Q2_LOAD(); } \
                D_KO(0); } while (0)
                A_P2B(sA, A_RD(0, pn + 8192), A_RD(1, pn + 9216), A_RD(2, pn + 10240), A_RD(3, pn + 11264), A_EV(), D_KO(1));
                so ^= 16384;
                const lds_cp t_ = pc; pc = pn; pn = t_;
            }
            if (blkno == 0) PROF(6);
            lA = __builtin_amdgcn_readfirstlane(lA); lB = __builtin_amdgcn_readfirstlane(lB); sA_ = __builtin_amdgcn_readfirstlane(sA_); sB_ = __builtin_amdgcn_readfirstlane(sB_);
            A_P1(sA, sB, pc + 8192, pc + 32768, D_VE(0); D_VE(1), D_KE(0); D_KE(1), D_VO(0); D_VO(1));
            if (blkno == 0) {
#pragma unroll
                for (int d = 0; d < 8; ++d) q[d] = ld128(qrd + d * 1024);
            }
            A_P2A(sA, sB, pc + 32768, true, qrow - 128 * ib - 32);
            {
                const lds_cp vp = pc + 32768 + 8192;
                A_P2B(sB, A_RD(0, vp), A_RD(1, vp + 2048), A_RD(2, vp + 4096), A_RD(3, vp + 6144), NOP_, NOP_);
#pragma unroll
                for (int j = 0; j < 4; ++j) fr[4 + j] = ld128(vp + j * 2048 + 1024);
#pragma unroll
                for (int e = 6; e < 16; ++e) sB[e] = EX2(sB[e]);
#pragma unroll
                for (int e = 0; e < 16; e += 2) { l0 += sB[e]; l1 += sB[e + 1]; }
#pragma unroll
                for (int e = 0; e < 4; ++e) { if (e >= 2) pw0[e] = cvtpk(sB[2 * e], sB[2 * e + 1]); pw1[e] = cvtpk(sB[8 + 2 * e], sB[9 + 2 * e]); }
                asm volatile("s_waitcnt vmcnt(0) lgkmcnt(0)\n\ts_barrier" ::: "memory");
                PUBLISH();
#pragma unroll
                for (int j = 0; j < 4; ++j) o[j] = MFMA32(fr[j], PWF(pw0), o[j]);
#pragma unroll
                for (int j = 0; j < 4; ++j) o[j] = MFMA32(fr[4 + j], PWF(pw1), o[j]);
            }
            {
                const int sb_own = kh == 0 ? 131072 + rg * 8192 : (rg >> 1) * 65536 + 32768 + so + (rg & 1) * 8192;
                const int sb_par = kh == 1 ? 131072 + rg * 8192 : (rg >> 1) * 65536 + 32768 + so + (rg & 1) * 8192;
                const float mown = dead ? -__builtin_inff() : mref, lown = l0 + l1;
                *(LAS f32x2*)(lds + kh * 65536 + so + 8192 + rg * 512 + lane * 8) = (f32x2){mown, lown};
                const f32x16 s0 = kh ? o[0] : o[2], s1 = kh ? o[1] : o[3], k0 = kh ? o[2] : o[0], k1 = kh ? o[3] : o[1];
#pragma unroll
                for (int qd = 0; qd < 4; ++qd) {
                    *(LAS f32x4*)(lds + sb_own + qd * 1024 + lane * 16) = (f32x4){s0[4 * qd], s0[4 * qd + 1], s0[4 * qd + 2], s0[4 * qd + 3]};
                    *(LAS f32x4*)(lds + sb_own + 4096 + qd * 1024 + lane * 16) = (f32x4){s1[4 * qd], s1[4 * qd + 1], s1[4 * qd + 2], s1[4 * qd + 3]};
                }
                asm volatile("s_waitcnt lgkmcnt(0)\n\ts_barrier" ::: "memory");
                const f32x2 ml = *(const LAS f32x2*)(lds + (kh ^ 1) * 65536 + so + 8192 + rg * 512 + lane * 8);
                f32x16 p0, p1;
#pragma unroll
                for (int qd = 0; qd < 4; ++qd) {
                    const f32x4 a = *(const LAS f32x4*)(lds + sb_par + qd * 1024 + lane * 16), b = *(const LAS f32x4*)(lds + sb_par + 4096 + qd * 1024 + lane * 16);
#pragma unroll
                    for (int e = 0; e < 4; ++e) { p0[4 * qd + e] = a[e]; p1[4 * qd + e] = b[e]; }
                }
                const float mf = __builtin_fmaxf(mown, ml[0]);
                const float fo = EX2(mown - mf), fp = EX2(ml[0] - mf);
                const float inv = 1.0f / swap_add(fo * lown + fp * ml[1]);
                const float ca = fo * inv, cb = fp * inv;
#pragma unroll
                for (int e = 0; e < 16; ++e) { p0[e] = ca * k0[e] + cb * p0[e]; p1[e] = ca * k1[e] + cb * p1[e]; }
                const unsigned dst = WS_AF + ((head * 8 + 4 * kh) * 64 + 4 * ib + rg) * 1024 + lane * 16;
                st16(rws, dst, pack8(p0, 0, 1.0f)); st16(rws, dst + 65536, pack8(p0, 1, 1.0f));
                st16(rws, dst + 2 * 65536, pack8(p1, 0, 1.0f)); st16(rws, dst + 3 * 65536, pack8(p1, 1, 1.0f));
                pf = F_C + ib * 32 + head;
            }
            so ^= 16384;
            if (blkno == 0) PROF(7);
        }
        PROF(8);
#undef A_RD
#undef A_P1
#undef A_P2A
#undef A_P2B
#undef A_P2K
#undef A_CHECK
#undef XE
#undef PWF
    }

    {
        int lane;
        asm volatile("v_mbcnt_lo_u32_b32 %0, -1, 0\n\tv_mbcnt_hi_u32_b32 %0, -1, %0" : "=v"(lane));
        const int r = lane & 31, hh = lane >> 5, blk = (lane >> 4) & 1, qq = (lane & 15) >> 2, p = lane & 3;
        const unsigned brow = 4 * wid + (lane >> 4), bchk = ((lane & 15) ^ (((lane >> 4) & 3) << 2)) << 4;
        const int sq = x8 >> 1, tm = (x8 & 1) * 8 + (kk >> 2), tn = kk & 3, rb = wid & 3, kp = wid >> 2;
        OutArgs oa;
        oa.ra = __builtin_amdgcn_make_buffer_rsrc((void*)(ws + WS_AF), (short)0, NKS * 65536, 0x00020000);
        oa.wo = (const char*)wo + (size_t)sq * 1048576 + tn * 256; oa.wu = (const char*)wout + tn * 256;
        oa.wv = brow * 1024 + bchk;
#pragma unroll
        for (int j = 0; j < 6; ++j) { const unsigned ur = 192 * sq + 32 * j + brow; oa.uv[j] = (ur > FH - 1 ? FH - 1 : ur) * 1024 + bchk; }
        oa.va = kp * 131072 + (tm * 4 + rb) * 1024 + lane * 16; oa.sc = sq * 64 * 65536; oa.su = (256 + 12 * sq) * 65536; oa.ldsw = ldsw;
#pragma unroll
        for (int c = 0; c < 4; ++c) oa.rd[c] = lds + (kp * 32 + 4 * hh + qq) * 256 + ((c ^ qq) << 6) + blk * 32 + p * 8;
        WAIT_L(0); BAR();
        out_w<0>(oa); out_w<1>(oa); out_w<2>(oa); out_w<3>(oa);
        WAIT_V(8); BAR();
        if (wid == 0 && lane == 0) set_flag(ws, pf, tag);
        if (wid == 1) {
            if (bid == 0) {
#pragma unroll 1
                for (int i = 0; i < 4; ++i) poll_flags(ws, F_S + i * 64 + lane, true, tag);
                if (lane == 0) set_flag(ws, F_E, tag);
            }
            poll_flags(ws, lane < 8 ? F_C + tm * 32 + 8 * sq + lane : F_U + (tm >> 1) * 8 + ((3 * sq) >> 1) + (lane & 1), lane < 10, tag);
        }
        out_w<4>(oa); out_w<5>(oa); out_w<6>(oa); out_w<7>(oa);
        WAIT_V(12); BAR();
        PROFO(9);
        u32x4 af[6][2];
        bf16x8 wf[2][4];
        f32x16 acc[4] = {};
        out_a<0>(oa, af); out_a<1>(oa, af); out_a<2>(oa, af); out_a<3>(oa, af); out_a<4>(oa, af);
#pragma unroll
        for (int c = 0; c < 4; ++c) wf[0][c] = ldtr2(oa.rd[c], 2048);
#define OS(t) out_step<t>(oa, af, wf, acc)
        OS(0); OS(1); OS(2); OS(3); OS(4); OS(5); OS(6); OS(7); OS(8); OS(9); OS(10); OS(11); OS(12); OS(13); OS(14); OS(15); OS(16); OS(17); OS(18);
#undef OS
        PROFO(10);
        const f32x16 s0 = kp ? acc[0] : acc[2], s1 = kp ? acc[1] : acc[3];
        f32x16 k0 = kp ? acc[2] : acc[0], k1 = kp ? acc[3] : acc[1];
#pragma unroll
        for (int g = 0; g < 4; ++g) {
            *(LAS f32x4*)(lds + 16384 + wid * 8192 + g * 1024 + lane * 16) = (f32x4){s0[4 * g], s0[4 * g + 1], s0[4 * g + 2], s0[4 * g + 3]};
            *(LAS f32x4*)(lds + 16384 + wid * 8192 + 4096 + g * 1024 + lane * 16) = (f32x4){s1[4 * g], s1[4 * g + 1], s1[4 * g + 2], s1[4 * g + 3]};
        }
        const size_t orow = (size_t)(tm * 128 + rb * 32 + r) * DM + tn * 128 + kp * 64 + 4 * hh;
        u32x2 xv[8];
        if (rb == sq) {
#pragma unroll
            for (int g = 0; g < 8; ++g) xv[g] = *(const u32x2*)(x + orow + 8 * g);
        }
        WAIT_L(0); BAR();
#pragma unroll
        for (int g = 0; g < 4; ++g) {
            const f32x4 a = *(const LAS f32x4*)(lds + 16384 + (wid ^ 4) * 8192 + g * 1024 + lane * 16), b = *(const LAS f32x4*)(lds + 16384 + (wid ^ 4) * 8192 + 4096 + g * 1024 + lane * 16);
#pragma unroll
            for (int e = 0; e < 4; ++e) { k0[4 * g + e] += a[e]; k1[4 * g + e] += b[e]; }
        }
        const unsigned pb = WS_P + ((tm * 4 + tn) * 4 + rb) * 65536 + kp * 8192 + lane * 16;
        const int fp = F_P + (tm * 4 + tn) * 4;
        if (rb != sq) {
#pragma unroll
            for (int g = 0; g < 4; ++g) {
                st16(rws, pb + sq * 16384 + g * 1024, __builtin_bit_cast(u32x4, (f32x4){k0[4 * g], k0[4 * g + 1], k0[4 * g + 2], k0[4 * g + 3]}));
                st16(rws, pb + sq * 16384 + 4096 + g * 1024, __builtin_bit_cast(u32x4, (f32x4){k1[4 * g], k1[4 * g + 1], k1[4 * g + 2], k1[4 * g + 3]}));
            }
            WAIT_V(0);
        }
        BAR();
        if (wid == 0 && lane == 0) set_flag(ws, fp + sq, tag);
        PROFO(11);
        if (rb == sq) {
            poll_flags(ws, fp + (lane & 3), (lane & 3) != sq, tag);
            PROFO(12);
            u32x4 pp[3][8];
#pragma unroll
            for (int j = 0; j < 3; ++j)
#pragma unroll
                for (int g = 0; g < 8; ++g) pp[j][g] = ld16(rws, pb + ((sq + 1 + j) & 3) * 16384 + g * 1024);
#pragma unroll
            for (int g = 0; g < 8; ++g) {
                const f32x16& k = g < 4 ? k0 : k1; const int e0 = 4 * (g & 3);
                const f32x4 a = __builtin_bit_cast(f32x4, pp[0][g]), b = __builtin_bit_cast(f32x4, pp[1][g]), c = __builtin_bit_cast(f32x4, pp[2][g]);
                const f32x4 t = ((f32x4){k[e0], k[e0 + 1], k[e0 + 2], k[e0 + 3]} + a) + (b + c);
                *(u32x2*)(out + orow + 8 * g) = (u32x2){cvtpk(bflo(xv[g][0]) + t[0], bfhi(xv[g][0]) + t[1]), cvtpk(bflo(xv[g][1]) + t[2], bfhi(xv[g][1]) + t[3])};
            }
        }
    }
    PROF_END(ws);
}

extern "C" void kernel_launch(void* const* d_in, const int* in_sizes, int n_in, void* d_out, int out_size, void* d_ws, size_t ws_size, hipStream_t stream) {
    (void)in_sizes; (void)n_in; (void)out_size; (void)ws_size;
    fused_fwd<<<256, 512, 0, stream>>>((const bf16_t*)d_in[0], (const bf16_t*)d_in[1], (const bf16_t*)d_in[2], (const bf16_t*)d_in[3], (const bf16_t*)d_in[4], (const bf16_t*)d_in[5],
                                       (const bf16_t*)d_in[7], (const bf16_t*)d_in[8], (const bf16_t*)d_in[9], (const bf16_t*)d_in[10], (bf16_t*)d_out, (unsigned char*)d_ws);
}
```

```cpp
#include <hip/hip_runtime.h>
#include <hip/hip_bf16.h>
#include <stdint.h>

#define LAS __attribute__((address_space(3)))
#define DI __device__ __forceinline__
typedef unsigned short bf16_t;
typedef unsigned long long u64;
typedef __attribute__((address_space(1))) u64 gu64;
typedef short bf16x8 __attribute__((ext_vector_type(8)));
typedef short s16x4 __attribute__((ext_vector_type(4)));
typedef __bf16 bf16x2_t __attribute__((ext_vector_type(2)));
typedef float f32x16 __attribute__((ext_vector_type(16)));
typedef float f32x4 __attribute__((ext_vector_type(4)));
typedef float f32x2 __attribute__((ext_vector_type(2)));
typedef unsigned u32x4 __attribute__((ext_vector_type(4)));
typedef unsigned u32x2 __attribute__((ext_vector_type(2)));
typedef LAS unsigned char* lds_p;
typedef const LAS unsigned char* lds_cp;
typedef __amdgpu_buffer_rsrc_t rsrc_t;

constexpr int SEQ = 2048, DM = 512, NH = 32, FH = 688, FF = 1376;
constexpr int NKS = 299;
constexpr int F_E = 0;
constexpr int F_S = 512;
constexpr int F_X = 1024;
constexpr int F_L = 2048;
constexpr int F_U = 3072;
constexpr int F_K = 3584;
constexpr int F_C = 4096;
constexpr int F_P = 5120;
constexpr unsigned WS_G   = 65536;
constexpr unsigned WS_XN  = 131072;
constexpr unsigned WS_QF  = WS_XN + SEQ * DM * 2;
constexpr unsigned WS_KVF = WS_QF + NH * 64 * 8192;
constexpr unsigned WS_Z2N = WS_KVF + NH * 64 * 16384;
constexpr unsigned WS_AF  = WS_Z2N + SEQ * 768 * 2;
constexpr unsigned WS_P   = WS_AF + 304 * 64 * 1024;
constexpr unsigned WS_END = WS_P + 256 * 65536;

#define MFMA32(a, b, c) __builtin_amdgcn_mfma_f32_32x32x16_bf16((a), (b), (c), 0, 0, 0)
#define SBAR() __builtin_amdgcn_sched_barrier(0)
#define PIN(x) asm volatile("" : "+v"(x))
#define WAIT_V(n) asm volatile("s_waitcnt vmcnt(" #n ")" ::: "memory")
#define WAIT_L(n) asm volatile("s_waitcnt lgkmcnt(" #n ")" ::: "memory")
#define BAR() asm volatile("s_barrier" ::: "memory")
#define SC1 16

DI unsigned cvtpk(float lo, float hi) { f32x2 v = {lo, hi}; bf16x2_t b = __builtin_convertvector(v, bf16x2_t); return __builtin_bit_cast(unsigned, b); }
DI float bflo(unsigned w) { return __uint_as_float(w << 16); }
DI float bfhi(unsigned w) { return __uint_as_float(w & 0xffff0000u); }
DI u32x4 pack8(const f32x16& a, int s, float sc) {
    u32x4 w;
    w[0] = cvtpk(a[8 * s + 0] * sc, a[8 * s + 1] * sc); w[1] = cvtpk(a[8 * s + 2] * sc, a[8 * s + 3] * sc);
    w[2] = cvtpk(a[8 * s + 4] * sc, a[8 * s + 5] * sc); w[3] = cvtpk(a[8 * s + 6] * sc, a[8 * s + 7] * sc);
    return w;
}
DI bf16x8 ld128(lds_cp p) { return *(const LAS bf16x8*)p; }
typedef short v4i16_t __attribute__((ext_vector_type(4)));
DI s16x4 ldtr(lds_cp p) { return __builtin_bit_cast(s16x4, __builtin_amdgcn_ds_read_tr16_b64_v4i16((LAS v4i16_t*)p)); }
DI bf16x8 ldtr2(lds_cp p, int step) { const s16x4 lo = ldtr(p), hi = ldtr(p + step); return __builtin_shufflevector(lo, hi, 0, 1, 2, 3, 4, 5, 6, 7); }
template <int I0, int I1> DI void dma_pair(unsigned v0, unsigned v1, const void* sbase, unsigned l) {
    asm volatile("s_add_u32 m0, %3, %4\n\ts_nop 0\n\tglobal_load_lds_dwordx4 %0, %2 sc1\n\ts_add_u32 m0, %3, %5\n\ts_nop 0\n\tglobal_load_lds_dwordx4 %1, %2 sc1"
                 :: "v"(v0), "v"(v1), "s"(sbase), "s"(l), "i"(I0), "i"(I1) : "memory", "scc");
}
template <int I, int O> DI void bdma1(unsigned voff, rsrc_t rs, unsigned soff, unsigned l) {
    asm volatile("s_add_u32 m0, %3, %4\n\ts_nop 0\n\tbuffer_load_dwordx4 %0, %1, %2 offen offset:%5 sc1 lds" :: "v"(voff), "s"(rs), "s"(soff), "s"(l), "i"(I), "i"(O) : "memory", "scc");
}
DI void dma_words(unsigned voff, const void* sbase, unsigned l) {
    asm volatile("s_mov_b32 m0, %2\n\ts_nop 0\n\tglobal_load_lds_dword %0, %1 sc1" :: "v"(voff), "s"(sbase), "s"(l) : "memory");
}
DI void st16(rsrc_t rs, unsigned off, u32x4 v) { __builtin_amdgcn_raw_buffer_store_b128(v, rs, off, 0, SC1); }
DI u32x4 ld16(rsrc_t rs, unsigned off) { return __builtin_amdgcn_raw_buffer_load_b128(rs, off, 0, SC1); }
DI float swap_add(float v) { auto r = __builtin_amdgcn_permlane32_swap(__float_as_uint(v), __float_as_uint(v), false, false); return __uint_as_float(r[0]) + __uint_as_float(r[1]); }
DI float swap_max(float v) { auto r = __builtin_amdgcn_permlane32_swap(__float_as_uint(v), __float_as_uint(v), false, false); return __builtin_fmaxf(__uint_as_float(r[0]), __uint_as_float(r[1])); }
DI f32x2 unpk(unsigned w) { return (f32x2){bflo(w), bfhi(w)}; }
DI unsigned cvt2(f32x2 v) { return __builtin_bit_cast(unsigned, __builtin_convertvector(v, bf16x2_t)); }
DI float sum_pair(unsigned w, float acc) { return __builtin_amdgcn_fdot2_f32_bf16(__builtin_bit_cast(bf16x2_t, w), __builtin_bit_cast(bf16x2_t, 0x3f803f80u), acc, false); }
DI unsigned gelu2(float a, float b) {
    const f32x2 v = {a, b};
    const f32x2 t = v * __builtin_elementwise_fma(v * v, (f32x2){-2.302208198f * 0.044715f, -2.302208198f * 0.044715f}, (f32x2){-2.302208198f, -2.302208198f});
    const f32x2 d = (f32x2){__builtin_amdgcn_exp2f(t[0]), __builtin_amdgcn_exp2f(t[1])} + (f32x2){1.0f, 1.0f};
    return cvt2(v * (f32x2){__builtin_amdgcn_rcpf(d[0]), __builtin_amdgcn_rcpf(d[1])});
}
DI u32x4 gelu8(const f32x16& a, int s) { return (u32x4){gelu2(a[8 * s], a[8 * s + 1]), gelu2(a[8 * s + 2], a[8 * s + 3]), gelu2(a[8 * s + 4], a[8 * s + 5]), gelu2(a[8 * s + 6], a[8 * s + 7])}; }
DI void set_flag(unsigned char* ws, int idx, u64 tag) { __hip_atomic_store((gu64*)ws + idx, tag, __ATOMIC_RELAXED, __HIP_MEMORY_SCOPE_AGENT); }
DI void poll_flags(unsigned char* ws, int idx, bool active, u64 tag) {
#pragma unroll 1
    for (unsigned spins = 0; spins < (1u << 17); ++spins) {
        const u64 v = __hip_atomic_load((gu64*)ws + idx, __ATOMIC_RELAXED, __HIP_MEMORY_SCOPE_AGENT);
        if (__all(!active || v == tag)) break;
        __builtin_amdgcn_s_sleep(4);
    }
}
#define ACQUIRE() asm volatile("buffer_inv sc1" ::: "memory")

constexpr int LDS_TAG = 163584, LDS_TOUCH = 163328, LDS_PARK = 163072;
DI void touch(unsigned voff, const void* sbase, unsigned lds0) { dma_words(voff, sbase, lds0 + LDS_TOUCH); }
template <int I> DI void dma_one(unsigned v0, const void* sbase, unsigned l) {
    asm volatile("s_add_u32 m0, %2, %3\n\ts_nop 0\n\tglobal_load_lds_dwordx4 %0, %1 sc1" :: "v"(v0), "s"(sbase), "s"(l), "i"(I) : "memory", "scc");
}
DI void z2_half(const lds_p lds, const unsigned lds0, f32x16 (&acc)[3], const bf16_t* __restrict__ x, const bf16_t* __restrict__ w_in, const unsigned char* fx, const int rg, const int hf,
                const int tid, const int wid, const int lane
                ) {
    constexpr int ZS = 704, ZST = 32 * ZS;
    constexpr unsigned WROW = FF * 2;
    const int row = tid >> 3, seg = tid & 7;
    const u32x4* src = (const u32x4*)(x + (size_t)(rg * 64 + row) * DM) + seg;
    u32x4 v[8];
#pragma unroll
    for (int j = 0; j < 8; ++j) asm volatile("global_load_dwordx4 %0, %1, off offset:%2" : "=v"(v[j]) : "v"(src), "i"(j * 128) : "memory");
    unsigned bv[3];
    const int p2 = wid < 6 ? wid + 16 : wid + 8;
#pragma unroll
    for (int i = 0; i < 3; ++i) { const int c = (i == 2 ? p2 : wid + 8 * i) * 64 + lane, zr = c / 44, zc = c - zr * 44; bv[i] = zr * WROW + (zc > 42 ? 42 : zc) * 16; }
    const unsigned lw = lds0 + 65536 + wid * 1024, lw2 = lds0 + 65536 + p2 * 1024;
    const char* wg = (const char*)w_in + FH * 2 + hf * FH;
#define Z_STAGE(slot, g_) do { dma_pair<(slot) * ZST, (slot) * ZST + 8192>(bv[0], bv[1], (g_), lw); dma_one<(slot) * ZST>(bv[2], (g_), lw2); } while (0)
    BAR();
    Z_STAGE(0, wg); Z_STAGE(1, wg + 32 * WROW); Z_STAGE(2, wg + 64 * WROW); Z_STAGE(3, wg + 96 * WROW);
#pragma unroll
    for (int i = 0; i < 2; ++i) { int c = (2 * wid + i) * 64 + lane; c = c > 895 ? 895 : c; const int zr = c / 7, zc = c - zr * 7; touch(((rg & 3) * 128 + zr) * WROW + (zc == 6 ? 684 : zc * 128), wg, lds0); }
    SBAR();
    {
        asm volatile("s_waitcnt vmcnt(14)" : "+v"(v[0]), "+v"(v[1]), "+v"(v[2]), "+v"(v[3]), "+v"(v[4]), "+v"(v[5]), "+v"(v[6]), "+v"(v[7]));
        float s = 0.f, s1 = 0.f;
#pragma unroll
        for (int j = 0; j < 8; ++j) { s = sum_pair(v[j][0], s); s1 = sum_pair(v[j][1], s1); s = sum_pair(v[j][2], s); s1 = sum_pair(v[j][3], s1); }
        s += s1;
        s += __shfl_xor(s, 1); s += __shfl_xor(s, 2); s += __shfl_xor(s, 4);
        const float mean = s * (1.0f / DM);
        f32x2 d[32], q2 = {0.f, 0.f}, q3 = {0.f, 0.f};
#pragma unroll
        for (int i = 0; i < 32; i += 2) { d[i] = unpk(v[i >> 2][i & 3]) - (f32x2){mean, mean}; d[i + 1] = unpk(v[i >> 2][(i & 3) + 1]) - (f32x2){mean, mean};
            q2 = __builtin_elementwise_fma(d[i], d[i], q2); q3 = __builtin_elementwise_fma(d[i + 1], d[i + 1], q3); }
        q2 += q3;
        float q = q2[0] + q2[1];
        q += __shfl_xor(q, 1); q += __shfl_xor(q, 2); q += __shfl_xor(q, 4);
        const float rstd = 1.0f / sqrtf(q * (1.0f / DM) + 1e-5f);
#pragma unroll
        for (int j = 0; j < 8; ++j) {
            u32x4 o;
#pragma unroll
            for (int e = 0; e < 4; ++e) o[e] = cvt2(d[4 * j + e] * (f32x2){rstd, rstd});
            *(LAS u32x4*)(lds + j * 8192 + row * 128 + ((seg ^ ((row >> 1) & 7)) << 4)) = o;
        }
    }
    const int r = lane & 31, hh = lane >> 5, blk = (lane >> 4) & 1, qq = (lane & 15) >> 2, p = lane & 3;
    const int asw = (r >> 1) & 7, oth = (wid & 1) ? -4096 : 4096;
    lds_cp ab[4];
#pragma unroll
    for (int k = 0; k < 4; ++k) ab[k] = lds + (wid & 1) * 4096 + r * 128 + (((2 * k + hh) ^ asw) << 4);
    const lds_cp bb = lds + 65536 + (8 * hh + qq) * ZS + wid * 64 + blk * 32 + p * 8;
    const lds_cp bx = bb + (8 + (wid >> 1) - wid) * 64;
    const bool w3 = wid < 6;
#pragma unroll
    for (int i = 0; i < 3; ++i) acc[i] = f32x16{};
    bf16x8 fa[2][2][2], fb[2][2][2];
#define Z_READ(set, slot, k0, aoff_) do { _Pragma("unroll") for (int k_ = 0; k_ < 2; ++k_) { \
        fa[set][k_][0] = ld128(ab[(k0) + k_] + (aoff_)); fa[set][k_][1] = ld128(ab[(k0) + k_] + oth + (aoff_)); \
        fb[set][k_][0] = ldtr2(bb + (slot) * ZST + k_ * 16 * ZS, 4 * ZS); if (w3) fb[set][k_][1] = ldtr2(bx + (slot) * ZST + k_ * 16 * ZS, 4 * ZS); } } while (0)
#define Z_MMA(set, k_) do { acc[0] = MFMA32(fb[set][k_][0], fa[set][k_][0], acc[0]); acc[1] = MFMA32(fb[set][k_][0], fa[set][k_][1], acc[1]); \
        if (w3) acc[2] = MFMA32(fb[set][k_][1], fa[set][k_][0], acc[2]); } while (0)
#define Z_SYNC(n) asm volatile("s_waitcnt vmcnt(" #n ") lgkmcnt(0)\n\ts_barrier" ::: "memory")
    Z_SYNC(11);
    Z_READ(0, 0, 0, 0);
    const char* g = wg + 128 * WROW;
#pragma unroll 1
    for (int kt = 0; kt < 3; ++kt) {
        Z_SYNC(6); Z_READ(1, 1, 2, 0);     SBAR(); Z_MMA(0, 0); SBAR(); Z_STAGE(0, g);             SBAR(); Z_MMA(0, 1); SBAR();
        Z_SYNC(6); Z_READ(0, 2, 0, 8192);  SBAR(); Z_MMA(1, 0); SBAR(); Z_STAGE(1, g + 32 * WROW); SBAR(); Z_MMA(1, 1); SBAR();
        Z_SYNC(6); Z_READ(1, 3, 2, 8192);  SBAR(); Z_MMA(0, 0); SBAR(); Z_STAGE(2, g + 64 * WROW); SBAR(); Z_MMA(0, 1); SBAR();
        Z_SYNC(6); Z_READ(0, 0, 0, 16384); SBAR(); Z_MMA(1, 0); SBAR(); Z_STAGE(3, g + 96 * WROW); SBAR(); Z_MMA(1, 1); SBAR();
        g += 128 * WROW;
#pragma unroll
        for (int k = 0; k < 4; ++k) ab[k] += 16384;
    }
    if (wid == 0) dma_words(lane * 4, fx, lds0 + LDS_PARK);
    Z_SYNC(6); Z_READ(1, 1, 2, 0);    SBAR(); Z_MMA(0, 0); Z_MMA(0, 1); SBAR();
    Z_SYNC(3); Z_READ(0, 2, 0, 8192); SBAR(); Z_MMA(1, 0); Z_MMA(1, 1); SBAR();
    Z_SYNC(0); Z_READ(1, 3, 2, 8192); SBAR(); Z_MMA(0, 0); Z_MMA(0, 1); SBAR();
    Z_SYNC(0);                                Z_MMA(1, 0); Z_MMA(1, 1);
#undef Z_STAGE
#undef Z_READ
#undef Z_MMA
#undef Z_SYNC
}

struct Run { const char* A; const char* B; unsigned dA1, dA2, dB1, dB2, vA0, vA1, vB0, vB1; };
constexpr int HTB = 16384;
constexpr unsigned LDA = DM * 2, LDB = 4096 * 2;
#define G_SA(b, h) (((b) * 2 + (h)) * HTB)
#define G_SB(b, h) ((4 + (b) * 2 + (h)) * HTB)
#define G_STAGE(bufoff, gb, v0, v1) dma_pair<(bufoff), (bufoff) + 8192>((v0), (v1), (gb), ldsw)
#define G_LDA(dst, b, h) do { _Pragma("unroll") for (int m_ = 0; m_ < 2; ++m_) _Pragma("unroll") for (int k_ = 0; k_ < 4; ++k_) dst[m_][k_] = ld128(lds + G_SA(b, h) + aoff[k_] + m_ * 4096); } while (0)
#define G_LDB(dst, b, h) do { _Pragma("unroll") for (int k_ = 0; k_ < 4; ++k_) dst[k_] = ldtr2(lds + G_SB(b, h) + boff + k_ * 4096, 1024); } while (0)
#define G_MMA(ai, bj, At, Bt) do { __builtin_amdgcn_s_setprio(1); _Pragma("unroll") for (int k_ = 0; k_ < 4; ++k_) _Pragma("unroll") for (int m_ = 0; m_ < 2; ++m_) \
        acc[ai][bj][m_] = SWAP ? MFMA32(At[m_][k_], Bt[k_], acc[ai][bj][m_]) : MFMA32(Bt[k_], At[m_][k_], acc[ai][bj][m_]); __builtin_amdgcn_s_setprio(0); } while (0)

DI void g_prologue_b(const Run& u, const unsigned ldsw) {
    G_STAGE(G_SB(0, 0), u.B, u.vB0, u.vB1); G_STAGE(G_SB(0, 1), u.B + u.dB1, u.vB0, u.vB1);
}
DI void g_prologue_rest(const Run& u, const unsigned ldsw) {
    G_STAGE(G_SA(0, 0), u.A, u.vA0, u.vA1); G_STAGE(G_SA(0, 1), u.A + u.dA1, u.vA0, u.vA1);
    G_STAGE(G_SB(1, 0), u.B + u.dB2, u.vB0, u.vB1); G_STAGE(G_SA(1, 0), u.A + u.dA2, u.vA0, u.vA1); G_STAGE(G_SB(1, 1), u.B + u.dB2 + u.dB1, u.vB0, u.vB1);
}
template <int LO> DI void check_parked(const int HI, lds_cp parked, unsigned char* ws, const int fidx, const u64 tag, const int lane) {
    const unsigned w = *(const LAS unsigned*)(parked + lane * 4);
    const int sl = (lane >> 1) & 15;
    if (!__all(sl < LO || sl >= HI || w == ((lane & 1) ? (unsigned)(tag >> 32) : (unsigned)tag))) poll_flags(ws, fidx + (lane & 31), lane < 32 && (lane & 15) >= LO && (lane & 15) < HI, tag);
}

template <bool SWAP, int ROUND>
DI void g_kloop(const lds_p lds, const unsigned lds0, f32x16 (&acc)[2][2][2], const Run& cur, const Run& nxt, const int (&aoff)[4], const int boff, const unsigned ldsw,
                unsigned char* ws, const u64 tag, const int fl0, const int fatt, const int lane, const int wid, const unsigned tch) {
    bf16x8 At[2][4], B0[4], B1[4];
#pragma unroll
    for (int i = 0; i < 8; ++i) acc[i >> 2][(i >> 1) & 1][i & 1] = f32x16{};
#pragma unroll 1
    for (int t = 0; t < 8; t += 2) {
        const bool last = (t == 6);
        const char* a1 = cur.A + (t + 1) * 128 + 128 * LDA;
        const char* a2 = last ? nxt.A : cur.A + (t + 2) * 128;
        const char* b2 = last ? nxt.B : cur.B + (size_t)(t + 2) * 64 * LDB;
        const unsigned dA1 = last ? nxt.dA1 : cur.dA1, dA2 = last ? nxt.dA2 : cur.dA2, dB1 = last ? nxt.dB1 : cur.dB1, dB2 = last ? nxt.dB2 : cur.dB2;
        const unsigned vA0 = last ? nxt.vA0 : cur.vA0, vA1 = last ? nxt.vA1 : cur.vA1, vB0 = last ? nxt.vB0 : cur.vB0, vB1 = last ? nxt.vB1 : cur.vB1;
        if (ROUND == 1 && last && wid == 0) dma_words(lane * 4, ws + (size_t)fatt * 8, lds0 + 131072);
        if (t == 2 && wid == 0 && lane == 0) set_flag(ws, fl0, tag);
        if (t == 0) G_LDB(B0, 0, 0);
        SBAR(); G_LDA(At, 0, 0); if (!(ROUND == 1 && t == 0)) G_STAGE(G_SA(1, 1), a1, cur.vA0, cur.vA1);
        if (ROUND == 0 && t == 2) touch(tch, nxt.A, lds0);
        if (ROUND == 1 && t == 4 && wid == 0) { check_parked<0>(8, lds + 131072, ws, fatt, tag, lane); ACQUIRE(); }
        WAIT_L(8); BAR(); WAIT_L(0); SBAR(); G_MMA(0, 0, At, B0); SBAR(); BAR();
        G_LDB(B1, 0, 1);
        G_STAGE(G_SB(0, 0), b2, vB0, vB1);
        BAR(); WAIT_L(0); SBAR(); G_MMA(0, 1, At, B1); SBAR(); BAR();
        G_LDA(At, 0, 1); G_STAGE(G_SA(0, 0), a2, vA0, vA1);
        if (ROUND == 1 && t == 0) WAIT_V(26); else WAIT_V(10);
        BAR(); WAIT_L(0); SBAR(); G_MMA(1, 0, At, B0); SBAR(); BAR();
        G_LDB(B0, 1, 0); G_STAGE(G_SB(0, 1), b2 + dB1, vB0, vB1);
        if (ROUND == 1 && t == 0) WAIT_V(22); else if ((ROUND == 1 && t == 4 && wid == 0) || (ROUND == 0 && t == 2)) WAIT_V(7); else WAIT_V(6);
        BAR(); SBAR(); G_MMA(1, 1, At, B1); SBAR(); BAR();
        if (ROUND == 1 && wid == 0) {
            if (t == 2) dma_words(lane * 4, ws + (size_t)fatt * 8, lds0 + 131072);
            if (last) check_parked<8>(12, lds + 131072, ws, fatt, tag, lane);
        }
        G_LDA(At, 1, 0); G_STAGE(G_SA(0, 1), a2 + dA1, vA0, vA1);
        BAR(); WAIT_L(0); SBAR(); G_MMA(0, 0, At, B0); SBAR(); BAR();
        G_LDB(B1, 1, 1); G_STAGE(G_SB(1, 0), b2 + dB2, vB0, vB1);
        BAR(); WAIT_L(0); SBAR(); G_MMA(0, 1, At, B1); SBAR(); BAR();
        G_LDA(At, 1, 1); G_STAGE(G_SA(1, 0), a2 + dA2, vA0, vA1);
        if (!last) WAIT_V(10);
        BAR(); WAIT_L(0); SBAR(); G_MMA(1, 0, At, B0); SBAR(); BAR();
        if (!last) G_LDB(B0, 0, 0);
        G_STAGE(G_SB(1, 1), b2 + dB2 + dB1, vB0, vB1);
        if (!(ROUND == 1 && last)) WAIT_V(6);
        BAR(); SBAR(); G_MMA(1, 1, At, B1); SBAR();
        if (!(last && wid >= 4)) BAR();
    }
}

DI void g_epilogue(const f32x16 (&acc)[2][2][2], const rsrc_t rws, const int type, const int pn, const int pm, const int wr, const int wc, const int lane) {
    constexpr float QS = 0.08838834764831845f * 1.4426950408889634f;
    const float sc = type == 2 ? QS : 1.0f;
    const unsigned base = (type == 2 ? WS_QF : WS_KVF + (type == 1 ? 8192 : 0)) + wc * 2048 + lane * 16;
    const unsigned hstride = type == 2 ? 8192 : 16384;
#pragma unroll
    for (int ai = 0; ai < 2; ++ai)
#pragma unroll
        for (int bj = 0; bj < 2; ++bj)
#pragma unroll
            for (int m = 0; m < 2; ++m) {
                const unsigned dst = base + (unsigned)((2 * pn + bj) * 64 + pm * 8 + ai * 4 + wr * 2 + m) * hstride;
                st16(rws, dst, pack8(acc[ai][bj][m], 0, sc)); st16(rws, dst + 1024, pack8(acc[ai][bj][m], 1, sc));
            }
}

#define NOP_ (void)0
#define SP_CHECK() if (t == 36) { poll_flags(ws, fatt + lane, (lane & 15) < 12, tag); ACQUIRE(); }
#define EX2(v) __builtin_amdgcn_exp2f(v)
#define MX3(a, b, c) __builtin_fmaxf(__builtin_fmaxf((a), (b)), (c))
constexpr float ATT_THR = 32.0f;
DI void att_mask(f32x16& s, int dq) {
#pragma unroll
    for (int e = 0; e < 16; ++e) if ((e & 3) + 8 * (e >> 2) > dq) s[e] = -__builtin_inff();
}

struct OutArgs { rsrc_t ra; const char* wo; const char* wu; lds_cp rd[4]; unsigned wv, uv[6], va, sc, su, ldsw; };
template <int T> DI void out_w(const OutArgs& o) {
    constexpr int S = (T % 9) * 16384;
    if constexpr (T < 16) dma_pair<S, S + 8192>(o.wv, o.wv + 32768, o.wo + T * 65536, o.ldsw);
    else if constexpr (T < 19) dma_pair<S, S + 8192>(o.uv[2 * (T - 16)], o.uv[2 * (T - 16) + 1], o.wu, o.ldsw);
}
template <int T> DI void out_a(const OutArgs& o, u32x4 (&af)[6][2]) {
    if constexpr (T < 19) {
        const unsigned so = T < 16 ? o.sc + T * 262144 : o.su + (T - 16) * 262144;
        asm volatile("buffer_load_dwordx4 %0, %1, %2, %3 offen sc1" : "=v"(af[T % 6][0]) : "v"(o.va), "s"(o.ra), "s"(so) : "memory");
        asm volatile("buffer_load_dwordx4 %0, %1, %2, %3 offen sc1" : "=v"(af[T % 6][1]) : "v"(o.va), "s"(o.ra), "s"(so + 65536) : "memory");
    }
}
constexpr int out_cnt(int i) { return (i + 7 < 19 ? 2 : 0) + (i + 5 < 19 ? 2 : 0); }
constexpr int out_allow(int t) { int n = t < 4 ? 2 * (4 - t) : 0; for (int i = t < 4 ? 0 : t - 4; i < t; ++i) n += out_cnt(i); return n; }
template <int T> DI void out_step(const OutArgs& o, u32x4 (&af)[6][2], bf16x8 (&wf)[2][4], f32x16 (&acc)[4]) {
    asm volatile("s_waitcnt vmcnt(%2)" : "+v"(af[T % 6][0]), "+v"(af[T % 6][1]) : "n"(out_allow(T)) : "memory");
    if constexpr (T % 2 == 0) BAR();
    const bf16x8 a0 = __builtin_bit_cast(bf16x8, af[T % 6][0]), a1 = __builtin_bit_cast(bf16x8, af[T % 6][1]);
#pragma unroll
    for (int c = 0; c < 4; ++c) wf[1][c] = ldtr2(o.rd[c] + (T % 9) * 16384 + 4096, 2048);
    SBAR();
    acc[0] = MFMA32(wf[0][0], a0, acc[0]); acc[1] = MFMA32(wf[0][1], a0, acc[1]); SBAR();
    out_w<T + 7>(o); SBAR();
    acc[2] = MFMA32(wf[0][2], a0, acc[2]); acc[3] = MFMA32(wf[0][3], a0, acc[3]); SBAR();
    if constexpr (T < 18) {
#pragma unroll
        for (int c = 0; c < 4; ++c) wf[0][c] = ldtr2(o.rd[c] + ((T + 1) % 9) * 16384, 2048);
    }
    SBAR();
    acc[0] = MFMA32(wf[1][0], a1, acc[0]); acc[1] = MFMA32(wf[1][1], a1, acc[1]); SBAR();
    out_a<T + 5>(o, af); SBAR();
    acc[2] = MFMA32(wf[1][2], a1, acc[2]); acc[3] = MFMA32(wf[1][3], a1, acc[3]); SBAR();
}

#define IT(h1, i1, h2, i2) ((h1) | (i1) << 2 | (h2) << 6 | (i2) << 8)
__device__ constexpr unsigned ITEMS[32] = {
    IT(0, 15, 0, 4), IT(1, 15, 1, 4), IT(2, 15, 2, 4), IT(3, 15, 3, 4), IT(0, 13, 0, 5), IT(1, 13, 1, 5), IT(2, 13, 2, 5), IT(3, 13, 3, 5),
    IT(0, 12, 0, 6), IT(1, 12, 1, 6), IT(2, 12, 2, 6), IT(3, 12, 3, 6), IT(0, 11, 0, 7), IT(1, 11, 1, 7), IT(2, 11, 2, 7), IT(3, 11, 3, 7),
    IT(0, 14, 0, 3), IT(1, 14, 1, 3), IT(2, 14, 2, 3), IT(3, 14, 3, 3), IT(0, 9, 0, 8), IT(1, 9, 1, 8), IT(2, 9, 2, 8), IT(3, 9, 3, 8),
    IT(0, 2, 0, 0), IT(1, 2, 1, 0), IT(2, 2, 2, 0), IT(3, 2, 3, 0), IT(0, 1, 1, 1), IT(2, 1, 3, 1), IT(0, 10, 1, 10), IT(2, 10, 3, 10)
};

__global__ __launch_bounds__(512, 2) void fused_fwd(const bf16_t* __restrict__ x, const bf16_t* __restrict__ ckv, const bf16_t* __restrict__ wq, const bf16_t* __restrict__ wk,
                                                    const bf16_t* __restrict__ wv, const bf16_t* __restrict__ wo, const bf16_t* __restrict__ w_in, const bf16_t* __restrict__ wsp,
                                                    const bf16_t* __restrict__ bsp, const bf16_t* __restrict__ wout, bf16_t* __restrict__ out, unsigned char* ws) {
    __shared__ __attribute__((aligned(16))) unsigned char smem[163840];
    const lds_p lds = (lds_p)smem;
    const unsigned lds0 = (unsigned)(uintptr_t)smem;
    const int tid = threadIdx.x, wid = __builtin_amdgcn_readfirstlane(tid >> 6), lane = tid & 63;
    const int bid = blockIdx.x, x8 = bid & 7, kk = bid >> 3;
    const int r = lane & 31, hh = lane >> 5, blk = (lane >> 4) & 1, qq = (lane & 15) >> 2, p = lane & 3;
    const rsrc_t rws = __builtin_amdgcn_make_buffer_rsrc((void*)ws, (short)0, WS_END, 0x00020000);
    const unsigned ldsw = lds0 + (unsigned)wid * 1024u;
    dma_words(lane * 4, ws, lds0 + LDS_TAG);
    u64 tag;
#define MAKE_TAG() do { const u32x2 e_ = *(const LAS u32x2*)(lds + LDS_TAG); \
        const u64 ep_ = ((u64)(unsigned)__builtin_amdgcn_readfirstlane(e_[1]) << 32) | (unsigned)__builtin_amdgcn_readfirstlane(e_[0]); \
        u64 z_ = ep_ + 0x9E3779B97F4A7C15ull; z_ = (z_ ^ (z_ >> 30)) * 0xBF58476D1CE4E5B9ull; z_ = (z_ ^ (z_ >> 27)) * 0x94D049BB133111EBull; z_ ^= z_ >> 31; tag = z_ == ep_ ? ~z_ : z_; } while (0)
    const unsigned item = ITEMS[kk];
    const int h1 = item & 3, i1 = (item >> 2) & 15, h2 = (item >> 6) & 3, i2 = (item >> 8) & 15;
    const unsigned char* kvx = ws + WS_KVF + (size_t)x8 * 4 * 64 * 16384;
    const unsigned char* kvh = kvx + (size_t)h1 * 64 * 16384;
    const char* q1src = (const char*)ws + WS_QF + (size_t)((4 * x8 + h1) * 64 + 4 * i1 + (wid & 3)) * 8192;
    bf16x8 q[8];
#define Q1_LOAD() do { _Pragma("unroll") for (int d = 0; d < 8; ++d) asm volatile("global_load_dwordx4 %0, %1, %2 offset:%3 sc1" : "=v"(q[d]) : "v"(lane * 16 + (d >> 2) * 4096), "s"(q1src), "i"((d & 3) * 1024) : "memory"); } while (0)
#define Q1_WAIT(n) asm volatile("s_waitcnt vmcnt(" #n ")" : "+v"(q[0]), "+v"(q[1]), "+v"(q[2]), "+v"(q[3]), "+v"(q[4]), "+v"(q[5]), "+v"(q[6]), "+v"(q[7]) :: "memory")
    const int fatt = F_K + x8 * 64;
#define Q2_LOAD() do { const char* q2src = (const char*)ws + WS_QF + (size_t)((4 * x8 + h2) * 64 + 4 * i2) * 8192 + wid * 4096; \
        const unsigned lq_ = __builtin_amdgcn_readfirstlane(lds0 + wid * 4096); \
        dma_pair<131072, 131072 + 1024>(lane * 16, lane * 16 + 1024, q2src, lq_); dma_pair<131072 + 2048, 131072 + 3072>(lane * 16 + 2048, lane * 16 + 3072, q2src, lq_); } while (0)
    Run rat;
    rat.A = (const char*)kvh; rat.B = (const char*)kvh + 32768; rat.dA1 = rat.dB1 = 65536; rat.dA2 = rat.dB2 = 8192;
    rat.vA0 = rat.vB0 = wid * 1024 + lane * 16; rat.vA1 = rat.vB1 = rat.vA0 + 16384;
    int pf;
    int aoff[4];
    const unsigned arow = 8 * wid + (lane >> 3), achk = ((lane & 7) ^ (((wid & 1) << 2) + (lane >> 4))) << 4;
    const unsigned brow = 4 * wid + (lane >> 4), bchk = ((lane & 15) ^ (((lane >> 4) & 3) << 2)) << 4;

    if (kk < 24) {
        const int type = kk >> 3, pn = 2 * x8 + ((kk >> 2) & 1), pm0 = (kk & 3) + (type == 2 ? 4 : 0), pm1 = pm0 ^ 4, wr = wid >> 2, wc = wid & 3;
        Run u0, u1;
        const char* Ab = type == 2 ? (const char*)ws + WS_XN : (const char*)ckv;
        u0.B = (const char*)(type == 0 ? wk : type == 1 ? wv : wq) + pn * 512; u0.A = Ab + (size_t)pm0 * 256 * LDA;
        u0.dA1 = 128 * LDA; u0.dA2 = 128; u0.dB1 = 256; u0.dB2 = 64 * LDB;
        u0.vA0 = arow * LDA + achk; u0.vA1 = u0.vA0 + 64 * LDA; u0.vB0 = brow * LDB + bchk; u0.vB1 = u0.vB0 + 32 * LDB;
        u1 = u0; u1.A = Ab + (size_t)pm1 * 256 * LDA;
        const unsigned tch = (type == 2 ? ((kk >> 2) & 1) * 2 : type * 2 + ((kk >> 2) & 1)) * 65536 + tid * 128, bl = ((kk & 3) * 128 + wid * 16 + (lane >> 2)) * LDB + (lane & 3) * 128;
        if (type < 2) {
            g_prologue_b(u0, ldsw); g_prologue_rest(u0, ldsw);
            WAIT_V(6);
            MAKE_TAG();
        } else {
            const int j = x8 * 8 + (kk - 16), row = 32 * j + 4 * wid + (lane >> 4);
            const u32x4* src = (const u32x4*)(x + (size_t)row * DM) + (lane & 15);
            u32x4 v[4];
#pragma unroll
            for (int i = 0; i < 4; ++i) v[i] = src[16 * i];
            SBAR();
            g_prologue_b(u0, ldsw); touch(bl, u0.B, lds0);
            SBAR();
            float s = 0.f;
#pragma unroll
            for (int i = 0; i < 4; ++i)
#pragma unroll
                for (int e = 0; e < 4; ++e) s += bflo(v[i][e]) + bfhi(v[i][e]);
            s += __shfl_xor(s, 1); s += __shfl_xor(s, 2); s += __shfl_xor(s, 4); s += __shfl_xor(s, 8);
            const float mean = s * (1.0f / DM);
            float q = 0.f;
#pragma unroll
            for (int i = 0; i < 4; ++i)
#pragma unroll
                for (int e = 0; e < 4; ++e) { const float a = bflo(v[i][e]) - mean, b = bfhi(v[i][e]) - mean; q += a * a + b * b; }
            q += __shfl_xor(q, 1); q += __shfl_xor(q, 2); q += __shfl_xor(q, 4); q += __shfl_xor(q, 8);
            const float rstd = 1.0f / sqrtf(q * (1.0f / DM) + 1e-5f);
#pragma unroll
            for (int i = 0; i < 4; ++i) {
                u32x4 o;
#pragma unroll
                for (int e = 0; e < 4; ++e) o[e] = cvtpk((bflo(v[i][e]) - mean) * rstd, (bfhi(v[i][e]) - mean) * rstd);
                st16(rws, WS_XN + row * 1024 + ((lane & 15) + 16 * i) * 16, o);
            }
            WAIT_V(0);
            MAKE_TAG();
            BAR();
            if (tid == 0) set_flag(ws, F_X + j, tag);
            if (wid == 0) { poll_flags(ws, F_X + ((lane & 8) ? pm1 : pm0) * 8 + (lane & 7), lane < 16, tag); ACQUIRE(); WAIT_V(0); }
            BAR();
            g_prologue_rest(u0, ldsw);
            WAIT_V(6);
        }
#pragma unroll
        for (int k = 0; k < 4; ++k) aoff[k] = (wr * 64 + r) * 128 + (((2 * k + hh) ^ ((r >> 1) & 7)) << 4);
        const int boff = (8 * hh + qq) * 256 + ((wc ^ qq) << 6) + blk * 32 + p * 8;
        BAR();
        if (wr == 1) BAR();
        f32x16 acc[2][2][2];
        const int fk = fatt + (pn & 1) * 16 + type * 4 + (kk & 3);
        if (type == 1) g_kloop<true, 0>(lds, lds0, acc, u0, u1, aoff, boff, ldsw, ws, tag, F_S + bid, fatt, lane, wid, tch);
        else           g_kloop<false, 0>(lds, lds0, acc, u0, u1, aoff, boff, ldsw, ws, tag, F_S + bid, fatt, lane, wid, tch);
        G_STAGE(G_SA(1, 1), u1.A + 128 + 128 * LDA, u1.vA0, u1.vA1);
        SBAR();
        g_epilogue(acc, rws, type, pn, pm0, wr, wc, lane);
        if (wr == 1) BAR();
        if (type == 1) g_kloop<true, 1>(lds, lds0, acc, u1, rat, aoff, boff, ldsw, ws, tag, fk, fatt, lane, wid, tch);
        else           g_kloop<false, 1>(lds, lds0, acc, u1, rat, aoff, boff, ldsw, ws, tag, fk, fatt, lane, wid, tch);
        Q1_LOAD();
        G_STAGE(G_SA(1, 1), rat.A + rat.dA1 + rat.dA2, rat.vA0, rat.vA1);
        SBAR();
        g_epilogue(acc, rws, type, pn, pm1, wr, wc, lane);
        pf = fk + 32;
        Q1_WAIT(18);
    } else {
        const int pm = x8, pn = kk - 24, grp = wid >> 2;
        constexpr unsigned SLA = SEQ * 2, SLB = 768 * 2, WROW = FF * 2;
        constexpr int RB = 65536, RA2 = 131072;
        const char* Ap = (const char*)ws + WS_XN + (size_t)pm * 256 * LDA;
        const char* Bp = (const char*)w_in + pn * 256;
        unsigned vA0 = arow * LDA + achk, vA1 = vA0 + 64 * LDA, vB0 = brow * WROW + bchk, vB1 = vB0 + 32 * WROW, dA = 128 * LDA, dB = 64 * WROW;
        const int tb0 = 8 * pm + 2 * (wid & 3), fs0 = 8 * pn + 4 * grp;
        float bias[2];
        const int flz = F_L + (4 * x8 + (pn >> 1)) * 2 + (kk & 1);
        {
            f32x16 za[3];
            const int rg = 4 * x8 + (pn >> 1), hf = kk & 1, m0_ = wid & 1;
#pragma unroll
            for (int m = 0; m < 2; ++m) bias[m] = __uint_as_float((unsigned)bsp[(tb0 + m) * 32 + r] << 16);
            z2_half(lds, lds0, za, x, w_in, ws + (F_X + 8 * pm) * 8, rg, hf, tid, wid, lane
                );
            PIN(bias[0]); PIN(bias[1]);
            MAKE_TAG();
            if (wid == 0 && kk < 30) {
                const unsigned w = *(const LAS unsigned*)(lds + LDS_PARK + lane * 4);
                if (!__all(lane >= 16 || w == ((lane & 1) ? (unsigned)(tag >> 32) : (unsigned)tag))) poll_flags(ws, F_X + 8 * pm + (lane & 7), true, tag);
                ACQUIRE();
            }
            {
                const char* slab = (const char*)wsp + (size_t)x8 * 1048576 + pn * 131072;
                touch(tid * 128, slab, lds0); touch(tid * 128 + 65536, slab, lds0);
            }
            u32x4 zp[3][2];
#pragma unroll
            for (int i = 0; i < 3; ++i) { zp[i][0] = gelu8(za[i], 0); zp[i][1] = gelu8(za[i], 1); }
            const int nx = wid < 4 ? 8 : wid < 6 ? 6 : 0;
            LAS float* red = (LAS float*)lds;
            float s[2] = {0.f, 0.f}, mean[2];
#pragma unroll
            for (int i = 0; i < 24; ++i) if (i < 16 || i - 16 < nx) s[(i >> 3) & 1] = sum_pair(zp[i >> 3][(i >> 2) & 1][i & 3], s[(i >> 3) & 1]);
            red[wid * 128 + m0_ * 64 + lane] = s[0]; red[wid * 128 + (m0_ ^ 1) * 64 + lane] = s[1];
            WAIT_L(0); BAR();
#pragma unroll
            for (int m = 0; m < 2; ++m) { float t = 0.f;
#pragma unroll
                for (int w = 0; w < 8; ++w) t += red[w * 128 + (m0_ ^ m) * 64 + lane];
                mean[m] = swap_add(t) * (2.0f / FH); }
            f32x2 d[24], q2[2] = {{0.f, 0.f}, {0.f, 0.f}};
#pragma unroll
            for (int i = 0; i < 24; ++i) { const int m = (i >> 3) & 1; d[i] = unpk(zp[i >> 3][(i >> 2) & 1][i & 3]) - (f32x2){mean[m], mean[m]};
                if (i < 16 || i - 16 < nx) q2[m] = __builtin_elementwise_fma(d[i], d[i], q2[m]); }
            red[1024 + wid * 128 + m0_ * 64 + lane] = q2[0][0] + q2[0][1]; red[1024 + wid * 128 + (m0_ ^ 1) * 64 + lane] = q2[1][0] + q2[1][1];
            WAIT_V(2); WAIT_L(0); BAR();
            LAS f32x2* tab = (LAS f32x2*)(lds + 160768);
            if (wid == 0) {
#pragma unroll
                for (int m = 0; m < 2; ++m) { float t = 0.f;
#pragma unroll
                    for (int w = 0; w < 8; ++w) t += red[1024 + w * 128 + m * 64 + lane];
                    s[m] = swap_add(t); }
                const float mo = hh ? mean[1] : mean[0], qo = hh ? s[1] : s[0];
                const unsigned t32 = (unsigned)tag;
                st16(rws, WS_G + (rg * 2 + hf) * 1024 + lane * 16, (u32x4){__float_as_uint(mo), t32, __float_as_uint(qo), t32});
                u32x4 o;
#pragma unroll 1
                for (unsigned spins = 0; spins < (1u << 17); ++spins) {
                    o = ld16(rws, WS_G + (rg * 2 + (hf ^ 1)) * 1024 + lane * 16);
                    if (__all(o[1] == t32 && o[3] == t32)) break;
                    __builtin_amdgcn_s_sleep(2);
                }
                const float mp = __uint_as_float(o[0]), dl = mo - mp;
                tab[lane] = (f32x2){0.5f * (mo + mp), 1.0f / sqrtf((qo + __uint_as_float(o[2]) + dl * dl * (FH * 0.25f)) * (1.0f / FH) + 1e-5f)};
            }
            WAIT_L(0); BAR();
            if (kk < 30) {
                dma_pair<0, 8192>(vA0, vA1, Ap, ldsw); dma_pair<16384, 24576>(vA0, vA1, Ap + 128 * LDA, ldsw); dma_pair<RB, RB + 8192>(vB0, vB1, Bp, ldsw);
                dma_pair<32768, 32768 + 8192>(vA0, vA1, Ap + 128, ldsw); dma_pair<32768 + 16384, 32768 + 24576>(vA0, vA1, Ap + 128 + 128 * LDA, ldsw);
                dma_pair<RB + 16384, RB + 16384 + 8192>(vB0, vB1, Bp + 64 * WROW, ldsw); dma_pair<RB + 32768, RB + 32768 + 8192>(vB0, vB1, Bp + 128 * WROW, ldsw);
            }
            f32x2 rs[2], of[2];
#pragma unroll
            for (int m = 0; m < 2; ++m) { const f32x2 t = tab[(m0_ ^ m) * 32 + r]; const float o = (mean[m] - t[0]) * t[1]; rs[m] = (f32x2){t[1], t[1]}; of[m] = (f32x2){o, o}; }
            const lds_p tr = lds + 114688;
#pragma unroll
            for (int i = 0; i < 12; ++i)
                if (i < 8 || 2 * (i - 8) < nx) {
                    const int b = i >> 2, m = b & 1;
                    *(LAS u32x2*)(tr + ((m0_ ^ m) * 32 + r) * 720 + (b == 2 ? 8 + (wid >> 1) : wid) * 64 + (i & 3) * 16 + hh * 8) =
                        (u32x2){cvt2(__builtin_elementwise_fma(d[2 * i], rs[m], of[m])), cvt2(__builtin_elementwise_fma(d[2 * i + 1], rs[m], of[m]))};
                }
            WAIT_L(0); BAR();
#pragma unroll
            for (int i = 0; i < 6; ++i) { int c = tid + 512 * i; const bool ok = c < 2752; c = ok ? c : 0; const int zr = c / 43, zc = c - zr * 43;
                st16(rws, ok ? WS_Z2N + (rg * 64 + zr) * SLB + hf * 688 + zc * 16 : 0x80000000u, *(const LAS u32x4*)(tr + zr * 720 + zc * 16)); }
#pragma unroll
            for (int i = 0; i < 2; ++i) { const int c = tid + 512 * i, zr = c / 10;
                st16(rws, hf == 1 && c < 640 ? WS_Z2N + (rg * 64 + zr) * SLB + 1376 + (c - zr * 10) * 16 : 0x80000000u, (u32x4){0u, 0u, 0u, 0u}); }
        }
        if (kk < 30) {
            u32x4 z1[2][2][2];
#pragma unroll
            for (int k = 0; k < 4; ++k) aoff[k] = ((wid >> 1) & 1) * 16384 + ((wid & 1) * 64 + r) * 128 + (((2 * k + hh) ^ ((r >> 1) & 7)) << 4);
            const int bo0 = RB + (8 * hh + qq) * 256 + (((2 * grp) ^ qq) << 6) + blk * 32 + p * 8, bo1 = RB + (8 * hh + qq) * 256 + (((2 * grp + 1) ^ qq) << 6) + blk * 32 + p * 8;
            f32x16 acc[2][2];
#pragma unroll
            for (int c = 0; c < 4; ++c) acc[c >> 1][c & 1] = f32x16{};
#pragma unroll
            for (int c = 0; c < 8; ++c) z1[c >> 2][(c >> 1) & 1][c & 1] = (u32x4){0u, 0u, 0u, 0u};
            Ap += 256; Bp += 192 * WROW;
            unsigned sa = 0, sa1 = 32768, sa2 = RA2;
            unsigned la = ldsw + RA2, la1 = ldsw, la2 = ldsw + 32768, lb = 49152;
            const char* Aq = Ap; unsigned lq = la, lq1 = la1, lq2 = la2, vQ0 = vA0, vQ1 = vA1, dQ = dA;
#define SP_SRC(s_) do { if ((s_) == 6) { Ap = (const char*)wsp + (size_t)pm * 256 * SLA; vA1 = arow * SLA + achk + 64 * SLA; dA = 128 * SLA; } \
                if ((s_) == 5) { Bp = (const char*)ws + WS_Z2N + pn * 256; vB0 = brow * SLB + bchk; vB1 = vB0 + 32 * SLB; dB = 64 * SLB; } \
                if (t == 6) { Aq = (const char*)wsp + (size_t)pm * 256 * SLA; vQ0 = arow * SLA + achk; vQ1 = vQ0 + 64 * SLA; dQ = 128 * SLA; } } while (0)
#define SP_LATE() do { dma_pair<0, 8192>(vQ0, vQ1, Aq, lq); dma_one<16384>(vQ0, Aq + dQ, lq); Aq += 128; const unsigned l_ = lq; lq = lq1; lq1 = lq2; lq2 = l_; } while (0)
#define SP_A1() dma_one<24576>(vA1, Ap + dA, la)
#define SP_B0() dma_one<RB>(vB0, Bp, ldsw + lb)
#define SP_B1() dma_one<RB + 8192>(vB1, Bp, ldsw + lb)
#define SP_ADV() do { Ap += 128; Bp += dB; const unsigned l_ = la; la = la1; la1 = la2; la2 = l_; lb = (lb + 16384) & 49152; } while (0)
#define SP_GELU() do { _Pragma("unroll") for (int c = 0; c < 4; ++c) { f32x16& a = acc[c >> 1][c & 1]; z1[c >> 1][c & 1][0] = gelu8(a, 0); z1[c >> 1][c & 1][1] = gelu8(a, 1); a = f32x16{}; } } while (0)
#define SP_MMA(k) do { _Pragma("unroll") for (int c = 0; c < 4; ++c) acc[c >> 1][c & 1] = MFMA32(Bt[c & 1][k], At[c >> 1][k], acc[c >> 1][c & 1]); } while (0)
#define SP_STEP(PRE, LATE, WAIT0, WAIT1, D0, D1, D2, POST, LASTBAR) do { \
                bf16x8 At[2][4], Bt[2][4]; \
                PRE; \
                const lds_cp pa = lds + sa, pb = lds + (t & 3) * 16384; \
                _Pragma("unroll") for (int k = 0; k < 4; ++k) { Bt[0][k] = ldtr2(pb + bo0 + k * 4096, 1024); At[0][k] = ld128(pa + aoff[k]); At[1][k] = ld128(pa + aoff[k] + 4096); Bt[1][k] = ldtr2(pb + bo1 + k * 4096, 1024); } \
                LATE; \
                if (grp == 1) { WAIT1; } \
                WAIT_L(0); BAR(); SBAR(); \
                __builtin_amdgcn_s_setprio(1); \
                SP_MMA(0); SBAR(); D0; SBAR(); SP_MMA(1); SBAR(); D1; SBAR(); SP_MMA(2); SBAR(); D2; SBAR(); SP_MMA(3); \
                __builtin_amdgcn_s_setprio(0); \
                SBAR(); \
                POST; \
                if (grp == 0) { WAIT0; } \
                if (LASTBAR) BAR(); \
                const unsigned s_ = sa; sa = sa1; sa1 = sa2; sa2 = s_; } while (0)
            asm volatile("s_waitcnt vmcnt(16) lgkmcnt(0)\n\ts_barrier" ::: "memory");
            if (grp == 1) { SP_A1(); SP_B0(); SP_B1(); SP_ADV(); BAR(); }
            int t = 0;
#pragma unroll 1
            for (; t < 8; ++t)
                SP_STEP({ if (wid == 0) { if (t == 2 && lane == 0) { set_flag(ws, flz, tag); set_flag(ws, F_S + bid, tag); } if (t == 3) { poll_flags(ws, F_L + lane, true, tag); ACQUIRE(); } } SP_SRC(t + grp); },
                        SP_LATE(), { if (t == 0) WAIT_V(16); else WAIT_V(8); }, { if (t == 0) WAIT_V(16); else WAIT_V(6); }, SP_A1(), SP_B0(), SP_B1(), { SP_ADV(); if (t == 7 && grp == 1) SP_GELU(); }, true);
            if (grp == 0) SP_GELU();
#pragma unroll 1
            for (; t < 36; ++t) SP_STEP(NOP_, SP_LATE(), WAIT_V(8), WAIT_V(6), SP_A1(), SP_B0(), SP_B1(), SP_ADV(), true);
#pragma unroll 1
            for (; t < 38; ++t)
                SP_STEP({ if (wid == 0) { SP_CHECK(); } }, SP_LATE(), { if (t == 36) WAIT_V(8); else WAIT_V(6); }, { if (t == 36) WAIT_V(6); else WAIT_V(4); },
                        if (t + grp < 38) SP_A1(), if (t + grp < 37) SP_B0(), if (t + grp < 37) SP_B1(), SP_ADV(), true);
            Q1_LOAD();
#pragma unroll 1
            for (; t < 40; ++t) SP_STEP(NOP_, NOP_, WAIT_V(8), WAIT_V(8), NOP_, NOP_, NOP_, NOP_, !(t == 39 && grp == 1));
            G_STAGE(G_SA(0, 0), rat.A, rat.vA0, rat.vA1); G_STAGE(G_SB(0, 0), rat.B, rat.vA0, rat.vA1); G_STAGE(G_SA(1, 0), rat.A + 8192, rat.vA0, rat.vA1); G_STAGE(G_SB(1, 0), rat.B + 8192, rat.vA0, rat.vA1);
            G_STAGE(G_SA(0, 1), rat.A + 65536, rat.vA0, rat.vA1); G_STAGE(G_SB(0, 1), rat.B + 65536, rat.vA0, rat.vA1); G_STAGE(G_SA(1, 1), rat.A + 73728, rat.vA0, rat.vA1); G_STAGE(G_SB(1, 1), rat.B + 73728, rat.vA0, rat.vA1);
            Q2_LOAD();
            SBAR();
#pragma unroll
            for (int m = 0; m < 2; ++m)
#pragma unroll
                for (int c = 0; c < 4; ++c) {
                    const f32x16& a = acc[m][c >> 1]; const u32x4 z = z1[m][c >> 1][c & 1]; const int s8 = 8 * (c & 1);
                    u32x4 o;
#pragma unroll
                    for (int e = 0; e < 4; ++e) o[e] = cvtpk(bflo(z[e]) * (a[s8 + 2 * e] + bias[m]), bfhi(z[e]) * (a[s8 + 2 * e + 1] + bias[m]));
                    st16(rws, fs0 + c < 43 ? WS_AF + ((256 + fs0 + c) * 64 + tb0 + m) * 1024 + lane * 16 : 0x80000000u, o);
                }
            pf = F_U + pm * 8 + pn;
            Q1_WAIT(20);
        } else {
            WAIT_V(0); WAIT_L(0); BAR();
            if (tid == 0) { set_flag(ws, flz, tag); set_flag(ws, F_S + bid, tag); }
            {
                const int aw = x8 * 2 + (kk - 30);
#pragma unroll
                for (int i = 0; i < 4; ++i) touch(tid * 128 + i * 65536, (const char*)wo + aw * 262144, lds0);
                if (tid < 344) touch(tid * 128, (const char*)wout + aw * 44032, lds0);
            }
            if (wid == 0) { poll_flags(ws, fatt + (lane & 31), lane < 32 && (lane & 15) < 12, tag); ACQUIRE(); }
            WAIT_V(0); BAR();
            g_prologue_b(rat, ldsw); g_prologue_rest(rat, ldsw); G_STAGE(G_SA(1, 1), rat.A + rat.dA1 + rat.dA2, rat.vA0, rat.vA1);
            Q1_LOAD();
            pf = -1;
            Q1_WAIT(0);
        }
    }
    BAR();
    {
        const int kh = wid >> 2, rg = wid & 3;
        const bool early = kk < 24 || kk >= 30;
        const rsrc_t rkv = __builtin_amdgcn_make_buffer_rsrc((void*)kvx, (short)0, 4 * 64 * 16384, 0x00020000);
        const unsigned dv0 = rg * 2048 + lane * 16 + kh * 32768;
        const unsigned wrapoff = (unsigned)(h1 * 16 + i1 + 1) * 65536u, wrapto = (unsigned)h2 << 20;
        unsigned sA_ = 0, sB_ = ((unsigned)h1 << 20) + 65536u, lA = lds0 + kh * 65536 + rg * 2048, lB = lA + 16384;
#define A_ADV() do { sA_ = sB_; sB_ += 65536u; if (sB_ == wrapoff) sB_ = wrapto; const unsigned t_ = lA; lA = lB; lB = t_; } while (0)
#define D_KO(h) (bdma1<8192, (h) * 1024>(dv0, rkv, sA_ + 16384, lA))
#define D_VE(h) (bdma1<32768, (h) * 1024>(dv0, rkv, sA_ + 8192, lA))
#define D_KE(h) (bdma1<0, (h) * 1024>(dv0, rkv, sB_, lB))
#define D_VO(h) (bdma1<40960, (h) * 1024>(dv0, rkv, sA_ + 24576, lA))
#define PUBLISH() do { if (pf >= 0) { if (wid == 0 && lane == 0) set_flag(ws, pf, tag); pf = -1; } } while (0)
        const lds_cp qrd = lds + 131072 + rg * 8192 + lane * 16;
        const lds_cp rbase = lds + kh * 65536 + lane * 16;
        int so = 0;
#pragma unroll 1
        for (int blkno = 0; blkno < 2; ++blkno) {
            const int ib = blkno ? i2 : i1, head = 4 * x8 + (blkno ? h2 : h1);
            const int qrow = 128 * ib + 32 * rg + r - 4 * hh - 64 * kh;
            f32x16 o[4], sA, sB, negm;
            bf16x8 fr[8];
            u32x4 pw0, pw1;
            float mref, l0 = 0.f, l1 = 0.f, resc = 1.0f, ma, mb;
            bool dead = false, pend = false;
            lds_cp pc = rbase + so, pn = rbase + (so ^ 16384);
#pragma unroll
            for (int d = 0; d < 8; ++d) fr[d] = ld128(pc + d * 1024);
            sA = f32x16{};
#pragma unroll
            for (int d = 0; d < 8; ++d) sA = MFMA32(fr[d], q[d], sA);
            asm volatile("s_waitcnt lgkmcnt(0)\n\ts_barrier" ::: "memory");
            A_ADV();
            if (blkno == 1) { D_KO(0); D_KO(1); }
            if (blkno == 1 && wid < 5) {
                if (wid < 4) touch(lane * 128, (const char*)wo + (size_t)(x8 >> 1) * 1048576 + kk * 32768 + wid * 8192, lds0);
                else { const unsigned ur = 192 * (x8 >> 1) + 6 * kk + (lane >> 3); touch((ur > FH - 1 ? FH - 1 : ur) * 1024 + (lane & 7) * 128, wout, lds0); }
            }
#pragma unroll
            for (int d = 0; d < 4; ++d) fr[d] = ld128(pc + 8192 + d * 1024);
#pragma unroll
            for (int d = 0; d < 4; ++d) o[d] = f32x16{};
            if (ib == 0) att_mask(sA, qrow);
            {
                float m = MX3(sA[0], sA[1], sA[2]);
#pragma unroll
                for (int e = 3; e < 15; e += 2) m = MX3(m, sA[e], sA[e + 1]);
                m = swap_max(__builtin_fmaxf(m, sA[15]));
                dead = m == -__builtin_inff();
                mref = dead ? 0.f : m;
#pragma unroll
                for (int e = 0; e < 16; ++e) { sA[e] -= mref; negm[e] = -mref; }
                PIN(negm);
#pragma unroll
                for (int e = 0; e < 6; ++e) sA[e] = EX2(sA[e]);
                pw0[0] = cvtpk(sA[0], sA[1]); pw0[1] = cvtpk(sA[2], sA[3]);
            }
#define A_RD(j, ptr) do { fr[j] = ld128(ptr); } while (0)
#define XE(S, i) S[i] = EX2(S[i])
#define A_P1(SC, SN, KP, VP, E0, E1, E2) do { \
            A_RD(4, (KP) + 4096); SBAR(); SN = MFMA32(fr[0], q[0], negm); XE(SC, 6); XE(SC, 7); pw0[2] = cvtpk(SC[4], SC[5]); PIN(SC); PIN(pw0); SBAR(); \
            A_RD(5, (KP) + 5120); SBAR(); SN = MFMA32(fr[1], q[1], SN); XE(SC, 8); XE(SC, 9); pw0[3] = cvtpk(SC[6], SC[7]); PIN(SC); PIN(pw0); E0; SBAR(); \
            A_RD(6, (KP) + 6144); SBAR(); SN = MFMA32(fr[2], q[2], SN); XE(SC, 10); XE(SC, 11); l0 += SC[0]; PIN(SC); PIN(l0); SBAR(); \
            A_RD(7, (KP) + 7168); SBAR(); SN = MFMA32(fr[3], q[3], SN); XE(SC, 12); XE(SC, 13); l1 += SC[1]; PIN(SC); PIN(l1); SBAR(); \
            A_RD(0, (VP));        SBAR(); SN = MFMA32(fr[4], q[4], SN); XE(SC, 14); XE(SC, 15); pw1[0] = cvtpk(SC[8], SC[9]); PIN(SC); PIN(pw1); E1; SBAR(); \
            A_RD(1, (VP) + 2048); SBAR(); SN = MFMA32(fr[5], q[5], SN); l0 += SC[2]; l1 += SC[3]; l0 += SC[4]; l1 += SC[5]; l0 += SC[6]; PIN(l0); PIN(l1); SBAR(); \
            A_RD(2, (VP) + 4096); SBAR(); SN = MFMA32(fr[6], q[6], SN); l1 += SC[7]; l0 += SC[8]; l1 += SC[9]; l0 += SC[10]; pw1[1] = cvtpk(SC[10], SC[11]); PIN(l0); PIN(l1); PIN(pw1); SBAR(); \
            A_RD(3, (VP) + 6144); SBAR(); SN = MFMA32(fr[7], q[7], SN); l1 += SC[11]; l0 += SC[12]; l1 += SC[13]; pw1[2] = cvtpk(SC[12], SC[13]); PIN(l0); PIN(l1); PIN(pw1); E2; SBAR(); } while (0)
#define PWF(x) __builtin_bit_cast(bf16x8, x)
#define A_P2A(SC, SN, VP, MASK, DQ) do { \
            A_RD(4, (VP) + 1024); A_RD(5, (VP) + 3072); SBAR(); o[0] = MFMA32(fr[0], PWF(pw0), o[0]); l0 += SC[14]; l1 += SC[15]; pw1[3] = cvtpk(SC[14], SC[15]); PIN(l0); PIN(l1); PIN(pw1); SBAR(); \
            A_RD(6, (VP) + 5120); A_RD(7, (VP) + 7168); SBAR(); o[1] = MFMA32(fr[1], PWF(pw0), o[1]); \
            if (MASK) att_mask(SN, DQ); \
            ma = MX3(SN[0], SN[1], SN[2]); mb = MX3(SN[3], SN[4], SN[5]); PIN(ma); PIN(mb); SBAR(); \
            o[2] = MFMA32(fr[2], PWF(pw0), o[2]); ma = MX3(ma, SN[6], SN[7]); mb = MX3(mb, SN[8], SN[9]); PIN(ma); PIN(mb); ma = MX3(ma, SN[10], SN[11]); PIN(ma); SBAR(); \
            o[3] = MFMA32(fr[3], PWF(pw0), o[3]); mb = MX3(mb, SN[12], SN[13]); ma = MX3(ma, SN[14], SN[15]); PIN(ma); PIN(mb); SBAR(); } while (0)
#define A_CHECK(SN) do { if (__builtin_expect(((__builtin_amdgcn_fcmpf(ma, ATT_THR, 2) | __builtin_amdgcn_fcmpf(mb, ATT_THR, 2)) != 0), 0)) {            \
                const float dl_ = __builtin_fmaxf(swap_max(__builtin_fmaxf(ma, mb)), 0.f); mref += dl_; resc = EX2(-dl_); l0 *= resc; l1 *= resc; pend = true; \
                _Pragma("unroll") for (int e = 0; e < 16; ++e) { SN[e] -= dl_; negm[e] = -mref; } PIN(negm); } SBAR(); } while (0)
#define A_RESC() do { if (__builtin_expect(pend, 0)) { pend = false;         \
            _Pragma("unroll") for (int d = 0; d < 4; ++d) _Pragma("unroll") for (int e = 0; e < 16; ++e) o[d][e] *= resc; } } while (0)
#define A_P2B(SN, RD0, RD1, RD2, RD3, D0, D1) do { \
            RD0; SBAR(); o[0] = MFMA32(fr[4], PWF(pw1), o[0]); SBAR(); A_CHECK(SN); D0; SBAR(); \
            RD1; SBAR(); o[1] = MFMA32(fr[5], PWF(pw1), o[1]); XE(SN, 0); XE(SN, 1); PIN(SN); SBAR(); \
            RD2; SBAR(); o[2] = MFMA32(fr[6], PWF(pw1), o[2]); XE(SN, 2); XE(SN, 3); pw0[0] = cvtpk(SN[0], SN[1]); PIN(SN); PIN(pw0); D1; SBAR(); \
            RD3; SBAR(); o[3] = MFMA32(fr[7], PWF(pw1), o[3]); XE(SN, 4); XE(SN, 5); pw0[1] = cvtpk(SN[2], SN[3]); PIN(SN); PIN(pw0); SBAR(); \
            A_RESC(); } while (0)
#define A_P2K(SN, KN, D0, D1) A_P2B(SN, A_RD(0, (KN)), A_RD(1, (KN) + 1024), A_RD(2, (KN) + 2048), A_RD(3, (KN) + 3072), D0, D1)
#pragma unroll 1
            for (int n = 0; n < ib; ++n) {
                A_P1(sA, sB, pc + 8192, pc + 32768, D_VE(0), D_VE(1), D_KE(0));
                A_P2A(sA, sB, pc + 32768, false, 0);
                A_P2K(sB, pn, D_KE(1), D_VO(0));
                A_P1(sB, sA, pn, pc + 32768 + 8192, D_VO(1), NOP_, NOP_);
                A_P2A(sB, sA, pc + 32768 + 8192, (n == ib - 1), qrow - 128 * ib);
                const int ev = blkno == 0 && (early || n == 0) ? n : 6;
                if (ev != 0) WAIT_V(0); else if (kk < 24) WAIT_V(22); else WAIT_V(14);
                asm volatile("s_waitcnt lgkmcnt(0)\n\ts_barrier" ::: "memory");
#define A_EV() do { A_ADV(); if (ev != 0) PUBLISH(); \
                if (ev < 6) { \
                    if (ev == 3 && wid == 0) dma_words(lane * 4, ws + (size_t)(fatt + 32) * 8, lds0 + 131072); \
                    if (ev == 4 && wid == 0) { check_parked<0>(i2 >= 8 ? 8 : 12, lds + 131072, ws, fatt + 32, tag, lane); ACQUIRE(); } \
                    if (ev == 5) Q2_LOAD(); } \
                D_KO(0); } while (0)
                A_P2B(sA, A_RD(0, pn + 8192), A_RD(1, pn + 9216), A_RD(2, pn + 10240), A_RD(3, pn + 11264), A_EV(), D_KO(1));
                so ^= 16384;
                const lds_cp t_ = pc; pc = pn; pn = t_;
            }
            lA = __builtin_amdgcn_readfirstlane(lA); lB = __builtin_amdgcn_readfirstlane(lB); sA_ = __builtin_amdgcn_readfirstlane(sA_); sB_ = __builtin_amdgcn_readfirstlane(sB_);
            A_P1(sA, sB, pc + 8192, pc + 32768, D_VE(0); D_VE(1), D_KE(0); D_KE(1), D_VO(0); D_VO(1));
            if (blkno == 0) {
#pragma unroll
                for (int d = 0; d < 8; ++d) q[d] = ld128(qrd + d * 1024);
            }
            A_P2A(sA, sB, pc + 32768, true, qrow - 128 * ib - 32);
            {
                const lds_cp vp = pc + 32768 + 8192;
                A_P2B(sB, A_RD(0, vp), A_RD(1, vp + 2048), A_RD(2, vp + 4096), A_RD(3, vp + 6144), NOP_, NOP_);
#pragma unroll
                for (int j = 0; j < 4; ++j) fr[4 + j] = ld128(vp + j * 2048 + 1024);
#pragma unroll
                for (int e = 6; e < 16; ++e) sB[e] = EX2(sB[e]);
#pragma unroll
                for (int e = 0; e < 16; e += 2) { l0 += sB[e]; l1 += sB[e + 1]; }
#pragma unroll
                for (int e = 0; e < 4; ++e) { if (e >= 2) pw0[e] = cvtpk(sB[2 * e], sB[2 * e + 1]); pw1[e] = cvtpk(sB[8 + 2 * e], sB[9 + 2 * e]); }
                asm volatile("s_waitcnt vmcnt(0) lgkmcnt(0)\n\ts_barrier" ::: "memory");
                PUBLISH();
#pragma unroll
                for (int j = 0; j < 4; ++j) o[j] = MFMA32(fr[j], PWF(pw0), o[j]);
#pragma unroll
                for (int j = 0; j < 4; ++j) o[j] = MFMA32(fr[4 + j], PWF(pw1), o[j]);
            }
            {
                const int sb_own = kh == 0 ? 131072 + rg * 8192 : (rg >> 1) * 65536 + 32768 + so + (rg & 1) * 8192;
                const int sb_par = kh == 1 ? 131072 + rg * 8192 : (rg >> 1) * 65536 + 32768 + so + (rg & 1) * 8192;
                const float mown = dead ? -__builtin_inff() : mref, lown = l0 + l1;
                *(LAS f32x2*)(lds + kh * 65536 + so + 8192 + rg * 512 + lane * 8) = (f32x2){mown, lown};
                const f32x16 s0 = kh ? o[0] : o[2], s1 = kh ? o[1] : o[3], k0 = kh ? o[2] : o[0], k1 = kh ? o[3] : o[1];
#pragma unroll
                for (int qd = 0; qd < 4; ++qd) {
                    *(LAS f32x4*)(lds + sb_own + qd * 1024 + lane * 16) = (f32x4){s0[4 * qd], s0[4 * qd + 1], s0[4 * qd + 2], s0[4 * qd + 3]};
                    *(LAS f32x4*)(lds + sb_own + 4096 + qd * 1024 + lane * 16) = (f32x4){s1[4 * qd], s1[4 * qd + 1], s1[4 * qd + 2], s1[4 * qd + 3]};
                }
                asm volatile("s_waitcnt lgkmcnt(0)\n\ts_barrier" ::: "memory");
                const f32x2 ml = *(const LAS f32x2*)(lds + (kh ^ 1) * 65536 + so + 8192 + rg * 512 + lane * 8);
                f32x16 p0, p1;
#pragma unroll
                for (int qd = 0; qd < 4; ++qd) {
                    const f32x4 a = *(const LAS f32x4*)(lds + sb_par + qd * 1024 + lane * 16), b = *(const LAS f32x4*)(lds + sb_par + 4096 + qd * 1024 + lane * 16);
#pragma unroll
                    for (int e = 0; e < 4; ++e) { p0[4 * qd + e] = a[e]; p1[4 * qd + e] = b[e]; }
                }
                const float mf = __builtin_fmaxf(mown, ml[0]);
                const float fo = EX2(mown - mf), fp = EX2(ml[0] - mf);
                const float inv = 1.0f / swap_add(fo * lown + fp * ml[1]);
                const float ca = fo * inv, cb = fp * inv;
#pragma unroll
                for (int e = 0; e < 16; ++e) { p0[e] = ca * k0[e] + cb * p0[e]; p1[e] = ca * k1[e] + cb * p1[e]; }
                const unsigned dst = WS_AF + ((head * 8 + 4 * kh) * 64 + 4 * ib + rg) * 1024 + lane * 16;
                st16(rws, dst, pack8(p0, 0, 1.0f)); st16(rws, dst + 65536, pack8(p0, 1, 1.0f));
                st16(rws, dst + 2 * 65536, pack8(p1, 0, 1.0f)); st16(rws, dst + 3 * 65536, pack8(p1, 1, 1.0f));
                pf = F_C + ib * 32 + head;
            }
            so ^= 16384;
        }
#undef A_RD
#undef A_P1
#undef A_P2A
#undef A_P2B
#undef A_P2K
#undef A_CHECK
#undef XE
#undef PWF
    }

    {
        int lane;
        asm volatile("v_mbcnt_lo_u32_b32 %0, -1, 0\n\tv_mbcnt_hi_u32_b32 %0, -1, %0" : "=v"(lane));
        const int r = lane & 31, hh = lane >> 5, blk = (lane >> 4) & 1, qq = (lane & 15) >> 2, p = lane & 3;
        const unsigned brow = 4 * wid + (lane >> 4), bchk = ((lane & 15) ^ (((lane >> 4) & 3) << 2)) << 4;
        const int sq = x8 >> 1, tm = (x8 & 1) * 8 + (kk >> 2), tn = kk & 3, rb = wid & 3, kp = wid >> 2;
        OutArgs oa;
        oa.ra = __builtin_amdgcn_make_buffer_rsrc((void*)(ws + WS_AF), (short)0, NKS * 65536, 0x00020000);
        oa.wo = (const char*)wo + (size_t)sq * 1048576 + tn * 256; oa.wu = (const char*)wout + tn * 256;
        oa.wv = brow * 1024 + bchk;
#pragma unroll
        for (int j = 0; j < 6; ++j) { const unsigned ur = 192 * sq + 32 * j + brow; oa.uv[j] = (ur > FH - 1 ? FH - 1 : ur) * 1024 + bchk; }
        oa.va = kp * 131072 + (tm * 4 + rb) * 1024 + lane * 16; oa.sc = sq * 64 * 65536; oa.su = (256 + 12 * sq) * 65536; oa.ldsw = ldsw;
#pragma unroll
        for (int c = 0; c < 4; ++c) oa.rd[c] = lds + (kp * 32 + 4 * hh + qq) * 256 + ((c ^ qq) << 6) + blk * 32 + p * 8;
        WAIT_L(0); BAR();
        out_w<0>(oa); out_w<1>(oa); out_w<2>(oa); out_w<3>(oa);
        WAIT_V(8); BAR();
        if (wid == 0 && lane == 0) set_flag(ws, pf, tag);
        if (wid == 1) {
            if (bid == 0) {
#pragma unroll 1
                for (int i = 0; i < 4; ++i) poll_flags(ws, F_S + i * 64 + lane, true, tag);
                if (lane == 0) set_flag(ws, F_E, tag);
            }
            poll_flags(ws, lane < 8 ? F_C + tm * 32 + 8 * sq + lane : F_U + (tm >> 1) * 8 + ((3 * sq) >> 1) + (lane & 1), lane < 10, tag);
        }
        out_w<4>(oa); out_w<5>(oa); out_w<6>(oa);
        WAIT_V(10); BAR();
        u32x4 af[6][2];
        bf16x8 wf[2][4];
        f32x16 acc[4] = {};
        out_a<0>(oa, af); out_a<1>(oa, af); out_a<2>(oa, af); out_a<3>(oa, af); out_a<4>(oa, af);
#pragma unroll
        for (int c = 0; c < 4; ++c) wf[0][c] = ldtr2(oa.rd[c], 2048);
#define OS(t) out_step<t>(oa, af, wf, acc)
        OS(0); OS(1); OS(2); OS(3); OS(4); OS(5); OS(6); OS(7); OS(8); OS(9); OS(10); OS(11); OS(12); OS(13); OS(14); OS(15); OS(16); OS(17); OS(18);
#undef OS
        const f32x16 s0 = kp ? acc[0] : acc[2], s1 = kp ? acc[1] : acc[3];
        f32x16 k0 = kp ? acc[2] : acc[0], k1 = kp ? acc[3] : acc[1];
#pragma unroll
        for (int g = 0; g < 4; ++g) {
            *(LAS f32x4*)(lds + 16384 + wid * 8192 + g * 1024 + lane * 16) = (f32x4){s0[4 * g], s0[4 * g + 1], s0[4 * g + 2], s0[4 * g + 3]};
            *(LAS f32x4*)(lds + 16384 + wid * 8192 + 4096 + g * 1024 + lane * 16) = (f32x4){s1[4 * g], s1[4 * g + 1], s1[4 * g + 2], s1[4 * g + 3]};
        }
        const size_t orow = (size_t)(tm * 128 + rb * 32 + r) * DM + tn * 128 + kp * 64 + 4 * hh;
        u32x2 xv[8];
        if (rb == sq) {
#pragma unroll
            for (int g = 0; g < 8; ++g) xv[g] = *(const u32x2*)(x + orow + 8 * g);
        }
        WAIT_L(0); BAR();
#pragma unroll
        for (int g = 0; g < 4; ++g) {
            const f32x4 a = *(const LAS f32x4*)(lds + 16384 + (wid ^ 4) * 8192 + g * 1024 + lane * 16), b = *(const LAS f32x4*)(lds + 16384 + (wid ^ 4) * 8192 + 4096 + g * 1024 + lane * 16);
#pragma unroll
            for (int e = 0; e < 4; ++e) { k0[4 * g + e] += a[e]; k1[4 * g + e] += b[e]; }
        }
        const unsigned pb = WS_P + ((tm * 4 + tn) * 4 + rb) * 65536 + kp * 8192 + lane * 16;
        const int fp = F_P + (((tm * 4 + tn) * 4 + rb) * 2 + kp) * 4;
        if (rb != sq) {
#pragma unroll
            for (int g = 0; g < 4; ++g) {
                st16(rws, pb + sq * 16384 + g * 1024, __builtin_bit_cast(u32x4, (f32x4){k0[4 * g], k0[4 * g + 1], k0[4 * g + 2], k0[4 * g + 3]}));
                st16(rws, pb + sq * 16384 + 4096 + g * 1024, __builtin_bit_cast(u32x4, (f32x4){k1[4 * g], k1[4 * g + 1], k1[4 * g + 2], k1[4 * g + 3]}));
            }
            WAIT_V(0);
            if (lane == 0) set_flag(ws, fp + sq, tag);
        }
        if (rb == sq) {
            poll_flags(ws, fp + (lane & 3), (lane & 3) != sq, tag);
            u32x4 pp[3][8];
#pragma unroll
            for (int j = 0; j < 3; ++j)
#pragma unroll
                for (int g = 0; g < 8; ++g) pp[j][g] = ld16(rws, pb + ((sq + 1 + j) & 3) * 16384 + g * 1024);
#pragma unroll
            for (int g = 0; g < 8; ++g) {
                const f32x16& k = g < 4 ? k0 : k1; const int e0 = 4 * (g & 3);
                const f32x4 a = __builtin_bit_cast(f32x4, pp[0][g]), b = __builtin_bit_cast(f32x4, pp[1][g]), c = __builtin_bit_cast(f32x4, pp[2][g]);
                const f32x4 t = ((f32x4){k[e0], k[e0 + 1], k[e0 + 2], k[e0 + 3]} + a) + (b + c);
                *(u32x2*)(out + orow + 8 * g) = (u32x2){cvtpk(bflo(xv[g][0]) + t[0], bfhi(xv[g][0]) + t[1]), cvtpk(bflo(xv[g][1]) + t[2], bfhi(xv[g][1]) + t[3])};
            }
        }
    }
}

extern "C" void kernel_launch(void* const* d_in, const int* in_sizes, int n_in, void* d_out, int out_size, void* d_ws, size_t ws_size, hipStream_t stream) {
    (void)in_sizes; (void)n_in; (void)out_size; (void)ws_size;
    fused_fwd<<<256, 512, 0, stream>>>((const bf16_t*)d_in[0], (const bf16_t*)d_in[1], (const bf16_t*)d_in[2], (const bf16_t*)d_in[3], (const bf16_t*)d_in[4], (const bf16_t*)d_in[5],
                                       (const bf16_t*)d_in[7], (const bf16_t*)d_in[8], (const bf16_t*)d_in[9], (const bf16_t*)d_in[10], (bf16_t*)d_out, (unsigned char*)d_ws);
}
```

```cpp
#include <hip/hip_runtime.h>
#include <hip/hip_bf16.h>
#include <stdint.h>

#define LAS __attribute__((address_space(3)))
#define DI __device__ __forceinline__
typedef unsigned short bf16_t;
typedef unsigned long long u64;
typedef __attribute__((address_space(1))) u64 gu64;
typedef short bf16x8 __attribute__((ext_vector_type(8)));
typedef short s16x4 __attribute__((ext_vector_type(4)));
typedef __bf16 bf16x2_t __attribute__((ext_vector_type(2)));
typedef float f32x16 __attribute__((ext_vector_type(16)));
typedef float f32x4 __attribute__((ext_vector_type(4)));
typedef float f32x2 __attribute__((ext_vector_type(2)));
typedef unsigned u32x4 __attribute__((ext_vector_type(4)));
typedef unsigned u32x2 __attribute__((ext_vector_type(2)));
typedef LAS unsigned char* lds_p;
typedef const LAS unsigned char* lds_cp;
typedef __amdgpu_buffer_rsrc_t rsrc_t;

constexpr int SEQ = 2048, DM = 512, NH = 32, FH = 688, FF = 1376;
constexpr int NKS = 299;
constexpr int F_E = 0;
constexpr int F_S = 512;
constexpr int F_X = 1024;
constexpr int F_L = 2048;
constexpr int F_U = 3072;
constexpr int F_K = 3584;
constexpr int F_C = 4096;
constexpr int F_P = 5120;
constexpr unsigned WS_G   = 65536;
constexpr unsigned WS_XN  = 131072;
constexpr unsigned WS_QF  = WS_XN + SEQ * DM * 2;
constexpr unsigned WS_KVF = WS_QF + NH * 64 * 8192;
constexpr unsigned WS_Z2N = WS_KVF + NH * 64 * 16384;
constexpr unsigned WS_AF  = WS_Z2N + SEQ * 768 * 2;
constexpr unsigned WS_P   = WS_AF + 304 * 64 * 1024;
constexpr unsigned WS_END = WS_P + 256 * 65536;

#define PROF_DECL
#define PROF(i)
#define PROF_END(wsp)
#define MFMA32(a, b, c) __builtin_amdgcn_mfma_f32_32x32x16_bf16((a), (b), (c), 0, 0, 0)
#define SBAR() __builtin_amdgcn_sched_barrier(0)
#define PIN(x) asm volatile("" : "+v"(x))
#define WAIT_V(n) asm volatile("s_waitcnt vmcnt(" #n ")" ::: "memory")
#define WAIT_L(n) asm volatile("s_waitcnt lgkmcnt(" #n ")" ::: "memory")
#define BAR() asm volatile("s_barrier" ::: "memory")
#define SC1 16

DI unsigned cvtpk(float lo, float hi) { f32x2 v = {lo, hi}; bf16x2_t b = __builtin_convertvector(v, bf16x2_t); return __builtin_bit_cast(unsigned, b); }
DI float bflo(unsigned w) { return __uint_as_float(w << 16); }
DI float bfhi(unsigned w) { return __uint_as_float(w & 0xffff0000u); }
DI u32x4 pack8(const f32x16& a, int s, float sc) {
    u32x4 w;
    w[0] = cvtpk(a[8 * s + 0] * sc, a[8 * s + 1] * sc); w[1] = cvtpk(a[8 * s + 2] * sc, a[8 * s + 3] * sc);
    w[2] = cvtpk(a[8 * s + 4] * sc, a[8 * s + 5] * sc); w[3] = cvtpk(a[8 * s + 6] * sc, a[8 * s + 7] * sc);
    return w;
}
DI bf16x8 ld128(lds_cp p) { return *(const LAS bf16x8*)p; }
typedef short v4i16_t __attribute__((ext_vector_type(4)));
DI s16x4 ldtr(lds_cp p) { return __builtin_bit_cast(s16x4, __builtin_amdgcn_ds_read_tr16_b64_v4i16((LAS v4i16_t*)p)); }
DI bf16x8 ldtr2(lds_cp p, int step) { const s16x4 lo = ldtr(p), hi = ldtr(p + step); return __builtin_shufflevector(lo, hi, 0, 1, 2, 3, 4, 5, 6, 7); }
template <int I0, int I1> DI void dma_pair(unsigned v0, unsigned v1, const void* sbase, unsigned l) {
    asm volatile("s_add_u32 m0, %3, %4\n\ts_nop 0\n\tglobal_load_lds_dwordx4 %0, %2 sc1\n\ts_add_u32 m0, %3, %5\n\ts_nop 0\n\tglobal_load_lds_dwordx4 %1, %2 sc1"
                 :: "v"(v0), "v"(v1), "s"(sbase), "s"(l), "i"(I0), "i"(I1) : "memory", "scc");
}
template <int I, int O> DI void bdma1(unsigned voff, rsrc_t rs, unsigned soff, unsigned l) {
    asm volatile("s_add_u32 m0, %3, %4\n\ts_nop 0\n\tbuffer_load_dwordx4 %0, %1, %2 offen offset:%5 sc1 lds" :: "v"(voff), "s"(rs), "s"(soff), "s"(l), "i"(I), "i"(O) : "memory", "scc");
}
DI void dma_words(unsigned voff, const void* sbase, unsigned l) {
    asm volatile("s_mov_b32 m0, %2\n\ts_nop 0\n\tglobal_load_lds_dword %0, %1 sc1" :: "v"(voff), "s"(sbase), "s"(l) : "memory");
}
DI void st16(rsrc_t rs, unsigned off, u32x4 v) { __builtin_amdgcn_raw_buffer_store_b128(v, rs, off, 0, SC1); }
DI u32x4 ld16(rsrc_t rs, unsigned off) { return __builtin_amdgcn_raw_buffer_load_b128(rs, off, 0, SC1); }
DI float swap_add(float v) { auto r = __builtin_amdgcn_permlane32_swap(__float_as_uint(v), __float_as_uint(v), false, false); return __uint_as_float(r[0]) + __uint_as_float(r[1]); }
DI float swap_max(float v) { auto r = __builtin_amdgcn_permlane32_swap(__float_as_uint(v), __float_as_uint(v), false, false); return __builtin_fmaxf(__uint_as_float(r[0]), __uint_as_float(r[1])); }
DI f32x2 unpk(unsigned w) { return (f32x2){bflo(w), bfhi(w)}; }
DI unsigned cvt2(f32x2 v) { return __builtin_bit_cast(unsigned, __builtin_convertvector(v, bf16x2_t)); }
DI float sum_pair(unsigned w, float acc) { return __builtin_amdgcn_fdot2_f32_bf16(__builtin_bit_cast(bf16x2_t, w), __builtin_bit_cast(bf16x2_t, 0x3f803f80u), acc, false); }
DI unsigned gelu2(float a, float b) {
    const f32x2 v = {a, b};
    const f32x2 t = v * __builtin_elementwise_fma(v * v, (f32x2){-2.302208198f * 0.044715f, -2.302208198f * 0.044715f}, (f32x2){-2.302208198f, -2.302208198f});
    const f32x2 d = (f32x2){__builtin_amdgcn_exp2f(t[0]), __builtin_amdgcn_exp2f(t[1])} + (f32x2){1.0f, 1.0f};
    return cvt2(v * (f32x2){__builtin_amdgcn_rcpf(d[0]), __builtin_amdgcn_rcpf(d[1])});
}
DI u32x4 gelu8(const f32x16& a, int s) { return (u32x4){gelu2(a[8 * s], a[8 * s + 1]), gelu2(a[8 * s + 2], a[8 * s + 3]), gelu2(a[8 * s + 4], a[8 * s + 5]), gelu2(a[8 * s + 6], a[8 * s + 7])}; }
DI void set_flag(unsigned char* ws, int idx, u64 tag) { __hip_atomic_store((gu64*)ws + idx, tag, __ATOMIC_RELAXED, __HIP_MEMORY_SCOPE_AGENT); }
DI void poll_flags(unsigned char* ws, int idx, bool active, u64 tag) {
#pragma unroll 1
    for (unsigned spins = 0; spins < (1u << 17); ++spins) {
        const u64 v = __hip_atomic_load((gu64*)ws + idx, __ATOMIC_RELAXED, __HIP_MEMORY_SCOPE_AGENT);
        if (__all(!active || v == tag)) break;
        __builtin_amdgcn_s_sleep(4);
    }
}
#define ACQUIRE() asm volatile("buffer_inv sc1" ::: "memory")

constexpr int LDS_TAG = 163584, LDS_TOUCH = 163328, LDS_PARK = 163072;
DI void touch(unsigned voff, const void* sbase, unsigned lds0) { dma_words(voff, sbase, lds0 + LDS_TOUCH); }
#define TOUCH_A(v, s, l) touch(v, s, l)
#define TOUCH_S(v, s, l) touch(v, s, l)
#define TOUCH_Z(v, s, l) touch(v, s, l)
template <int I> DI void dma_one(unsigned v0, const void* sbase, unsigned l) {
    asm volatile("s_add_u32 m0, %2, %3\n\ts_nop 0\n\tglobal_load_lds_dwordx4 %0, %1 sc1" :: "v"(v0), "s"(sbase), "s"(l), "i"(I) : "memory", "scc");
}
DI void z2_half(const lds_p lds, const unsigned lds0, f32x16 (&acc)[3], const bf16_t* __restrict__ x, const bf16_t* __restrict__ w_in, const unsigned char* fx, const int rg, const int hf,
                const int tid, const int wid, const int lane
#define GP(i)
                ) {
    constexpr int ZS = 704, ZST = 32 * ZS;
    constexpr unsigned WROW = FF * 2;
    const int row = tid >> 3, seg = tid & 7;
    const u32x4* src = (const u32x4*)(x + (size_t)(rg * 64 + row) * DM) + seg;
    u32x4 v[8];
#pragma unroll
    for (int j = 0; j < 8; ++j) asm volatile("global_load_dwordx4 %0, %1, off offset:%2" : "=v"(v[j]) : "v"(src), "i"(j * 128) : "memory");
    unsigned bv[3];
    const int p2 = wid < 6 ? wid + 16 : wid + 8;
#pragma unroll
    for (int i = 0; i < 3; ++i) { const int c = (i == 2 ? p2 : wid + 8 * i) * 64 + lane, zr = c / 44, zc = c - zr * 44; bv[i] = zr * WROW + (zc > 42 ? 42 : zc) * 16; }
    const unsigned lw = lds0 + 65536 + wid * 1024, lw2 = lds0 + 65536 + p2 * 1024;
    const char* wg = (const char*)w_in + FH * 2 + hf * FH;
#define Z_STAGE(slot, g_) do { dma_pair<(slot) * ZST, (slot) * ZST + 8192>(bv[0], bv[1], (g_), lw); dma_one<(slot) * ZST>(bv[2], (g_), lw2); } while (0)
    BAR();
    Z_STAGE(0, wg); Z_STAGE(1, wg + 32 * WROW); Z_STAGE(2, wg + 64 * WROW); Z_STAGE(3, wg + 96 * WROW);
#pragma unroll
    for (int i = 0; i < 2; ++i) { int c = (2 * wid + i) * 64 + lane; c = c > 895 ? 895 : c; const int zr = c / 7, zc = c - zr * 7; TOUCH_Z(((rg & 3) * 128 + zr) * WROW + (zc == 6 ? 684 : zc * 128), wg, lds0); }
    SBAR();
    GP(5);
    {
        asm volatile("s_waitcnt vmcnt(14)" : "+v"(v[0]), "+v"(v[1]), "+v"(v[2]), "+v"(v[3]), "+v"(v[4]), "+v"(v[5]), "+v"(v[6]), "+v"(v[7]));
        GP(6);
        float s = 0.f, s1 = 0.f;
#pragma unroll
        for (int j = 0; j < 8; ++j) { s = sum_pair(v[j][0], s); s1 = sum_pair(v[j][1], s1); s = sum_pair(v[j][2], s); s1 = sum_pair(v[j][3], s1); }
        s += s1;
        s += __shfl_xor(s, 1); s += __shfl_xor(s, 2); s += __shfl_xor(s, 4);
        const float mean = s * (1.0f / DM);
        f32x2 d[32], q2 = {0.f, 0.f}, q3 = {0.f, 0.f};
#pragma unroll
        for (int i = 0; i < 32; i += 2) { d[i] = unpk(v[i >> 2][i & 3]) - (f32x2){mean, mean}; d[i + 1] = unpk(v[i >> 2][(i & 3) + 1]) - (f32x2){mean, mean};
            q2 = __builtin_elementwise_fma(d[i], d[i], q2); q3 = __builtin_elementwise_fma(d[i + 1], d[i + 1], q3); }
        q2 += q3;
        float q = q2[0] + q2[1];
        q += __shfl_xor(q, 1); q += __shfl_xor(q, 2); q += __shfl_xor(q, 4);
        const float rstd = 1.0f / sqrtf(q * (1.0f / DM) + 1e-5f);
#pragma unroll
        for (int j = 0; j < 8; ++j) {
            u32x4 o;
#pragma unroll
            for (int e = 0; e < 4; ++e) o[e] = cvt2(d[4 * j + e] * (f32x2){rstd, rstd});
            *(LAS u32x4*)(lds + j * 8192 + row * 128 + ((seg ^ ((row >> 1) & 7)) << 4)) = o;
        }
    }
    const int r = lane & 31, hh = lane >> 5, blk = (lane >> 4) & 1, qq = (lane & 15) >> 2, p = lane & 3;
    const int asw = (r >> 1) & 7, oth = (wid & 1) ? -4096 : 4096;
    lds_cp ab[4];
#pragma unroll
    for (int k = 0; k < 4; ++k) ab[k] = lds + (wid & 1) * 4096 + r * 128 + (((2 * k + hh) ^ asw) << 4);
    const lds_cp bb = lds + 65536 + (8 * hh + qq) * ZS + wid * 64 + blk * 32 + p * 8;
    const lds_cp bx = bb + (8 + (wid >> 1) - wid) * 64;
    const bool w3 = wid < 6;
#pragma unroll
    for (int i = 0; i < 3; ++i) acc[i] = f32x16{};
    bf16x8 fa[2][2][2], fb[2][2][2];
#define Z_READ(set, slot, k0, aoff_) do { _Pragma("unroll") for (int k_ = 0; k_ < 2; ++k_) { \
        fa[set][k_][0] = ld128(ab[(k0) + k_] + (aoff_)); fa[set][k_][1] = ld128(ab[(k0) + k_] + oth + (aoff_)); \
        fb[set][k_][0] = ldtr2(bb + (slot) * ZST + k_ * 16 * ZS, 4 * ZS); if (w3) fb[set][k_][1] = ldtr2(bx + (slot) * ZST + k_ * 16 * ZS, 4 * ZS); } } while (0)
#define Z_MMA(set, k_) do { acc[0] = MFMA32(fb[set][k_][0], fa[set][k_][0], acc[0]); acc[1] = MFMA32(fb[set][k_][0], fa[set][k_][1], acc[1]); \
        if (w3) acc[2] = MFMA32(fb[set][k_][1], fa[set][k_][0], acc[2]); } while (0)
#define Z_SYNC(n) asm volatile("s_waitcnt vmcnt(" #n ") lgkmcnt(0)\n\ts_barrier" ::: "memory")
    GP(7);
    Z_SYNC(11);
    GP(8);
    Z_READ(0, 0, 0, 0);
    const char* g = wg + 128 * WROW;
#pragma unroll 1
    for (int kt = 0; kt < 3; ++kt) {
        Z_SYNC(6); Z_READ(1, 1, 2, 0);     SBAR(); Z_MMA(0, 0); SBAR(); Z_STAGE(0, g);             SBAR(); Z_MMA(0, 1); SBAR();
        Z_SYNC(6); Z_READ(0, 2, 0, 8192);  SBAR(); Z_MMA(1, 0); SBAR(); Z_STAGE(1, g + 32 * WROW); SBAR(); Z_MMA(1, 1); SBAR();
        Z_SYNC(6); Z_READ(1, 3, 2, 8192);  SBAR(); Z_MMA(0, 0); SBAR(); Z_STAGE(2, g + 64 * WROW); SBAR(); Z_MMA(0, 1); SBAR();
        Z_SYNC(6); Z_READ(0, 0, 0, 16384); SBAR(); Z_MMA(1, 0); SBAR(); Z_STAGE(3, g + 96 * WROW); SBAR(); Z_MMA(1, 1); SBAR();
        g += 128 * WROW;
#pragma unroll
        for (int k = 0; k < 4; ++k) ab[k] += 16384;
    }
    if (wid == 0) dma_words(lane * 4, fx, lds0 + LDS_PARK);
    Z_SYNC(6); Z_READ(1, 1, 2, 0);    SBAR(); Z_MMA(0, 0); Z_MMA(0, 1); SBAR();
    Z_SYNC(3); Z_READ(0, 2, 0, 8192); SBAR(); Z_MMA(1, 0); Z_MMA(1, 1); SBAR();
    Z_SYNC(0); Z_READ(1, 3, 2, 8192); SBAR(); Z_MMA(0, 0); Z_MMA(0, 1); SBAR();
    Z_SYNC(0);                                Z_MMA(1, 0); Z_MMA(1, 1);
#undef Z_STAGE
#undef Z_READ
#undef Z_MMA
#undef Z_SYNC
}

struct Run { const char* A; const char* B; unsigned dA1, dA2, dB1, dB2, vA0, vA1, vB0, vB1; };
constexpr int HTB = 16384;
constexpr unsigned LDA = DM * 2, LDB = 4096 * 2;
#define G_SA(b, h) (((b) * 2 + (h)) * HTB)
#define G_SB(b, h) ((4 + (b) * 2 + (h)) * HTB)
#define G_STAGE(bufoff, gb, v0, v1) dma_pair<(bufoff), (bufoff) + 8192>((v0), (v1), (gb), ldsw)
#define G_LDA(dst, b, h) do { _Pragma("unroll") for (int m_ = 0; m_ < 2; ++m_) _Pragma("unroll") for (int k_ = 0; k_ < 4; ++k_) dst[m_][k_] = ld128(lds + G_SA(b, h) + aoff[k_] + m_ * 4096); } while (0)
#define G_LDB(dst, b, h) do { _Pragma("unroll") for (int k_ = 0; k_ < 4; ++k_) dst[k_] = ldtr2(lds + G_SB(b, h) + boff + k_ * 4096, 1024); } while (0)
#define G_MMA(ai, bj, At, Bt) do { __builtin_amdgcn_s_setprio(1); _Pragma("unroll") for (int k_ = 0; k_ < 4; ++k_) _Pragma("unroll") for (int m_ = 0; m_ < 2; ++m_) \
        acc[ai][bj][m_] = SWAP ? MFMA32(At[m_][k_], Bt[k_], acc[ai][bj][m_]) : MFMA32(Bt[k_], At[m_][k_], acc[ai][bj][m_]); __builtin_amdgcn_s_setprio(0); } while (0)

DI void g_prologue_b(const Run& u, const unsigned ldsw) {
    G_STAGE(G_SB(0, 0), u.B, u.vB0, u.vB1); G_STAGE(G_SB(0, 1), u.B + u.dB1, u.vB0, u.vB1);
}
DI void g_prologue_rest(const Run& u, const unsigned ldsw) {
    G_STAGE(G_SA(0, 0), u.A, u.vA0, u.vA1); G_STAGE(G_SA(0, 1), u.A + u.dA1, u.vA0, u.vA1);
    G_STAGE(G_SB(1, 0), u.B + u.dB2, u.vB0, u.vB1); G_STAGE(G_SA(1, 0), u.A + u.dA2, u.vA0, u.vA1); G_STAGE(G_SB(1, 1), u.B + u.dB2 + u.dB1, u.vB0, u.vB1);
}
template <int LO> DI void check_parked(const int HI, lds_cp parked, unsigned char* ws, const int fidx, const u64 tag, const int lane) {
    const unsigned w = *(const LAS unsigned*)(parked + lane * 4);
    const int sl = (lane >> 1) & 15;
    if (!__all(sl < LO || sl >= HI || w == ((lane & 1) ? (unsigned)(tag >> 32) : (unsigned)tag))) poll_flags(ws, fidx + (lane & 31), lane < 32 && (lane & 15) >= LO && (lane & 15) < HI, tag);
}

template <bool SWAP, int ROUND>
DI void g_kloop(const lds_p lds, const unsigned lds0, f32x16 (&acc)[2][2][2], const Run& cur, const Run& nxt, const int (&aoff)[4], const int boff, const unsigned ldsw,
                unsigned char* ws, const u64 tag, const int fl0, const int fatt, const int lane, const int wid, const unsigned tch) {
    bf16x8 At[2][4], B0[4], B1[4];
#pragma unroll
    for (int i = 0; i < 8; ++i) acc[i >> 2][(i >> 1) & 1][i & 1] = f32x16{};
#pragma unroll 1
    for (int t = 0; t < 8; t += 2) {
        const bool last = (t == 6);
        const char* a1 = cur.A + (t + 1) * 128 + 128 * LDA;
        const char* a2 = last ? nxt.A : cur.A + (t + 2) * 128;
        const char* b2 = last ? nxt.B : cur.B + (size_t)(t + 2) * 64 * LDB;
        const unsigned dA1 = last ? nxt.dA1 : cur.dA1, dA2 = last ? nxt.dA2 : cur.dA2, dB1 = last ? nxt.dB1 : cur.dB1, dB2 = last ? nxt.dB2 : cur.dB2;
        const unsigned vA0 = last ? nxt.vA0 : cur.vA0, vA1 = last ? nxt.vA1 : cur.vA1, vB0 = last ? nxt.vB0 : cur.vB0, vB1 = last ? nxt.vB1 : cur.vB1;
        if (ROUND == 1 && last && wid == 0) dma_words(lane * 4, ws + (size_t)fatt * 8, lds0 + 131072);
        if (t == 2 && wid == 0 && lane == 0) set_flag(ws, fl0, tag);
        if (t == 0) G_LDB(B0, 0, 0);
        SBAR(); G_LDA(At, 0, 0); if (!(ROUND == 1 && t == 0)) G_STAGE(G_SA(1, 1), a1, cur.vA0, cur.vA1);
        if (ROUND == 0 && t == 2) touch(tch, nxt.A, lds0);
        if (ROUND == 1 && t == 4 && wid == 0) { check_parked<0>(8, lds + 131072, ws, fatt, tag, lane); ACQUIRE(); }
        WAIT_L(8); BAR(); WAIT_L(0); SBAR(); G_MMA(0, 0, At, B0); SBAR(); BAR();
        G_LDB(B1, 0, 1);
        G_STAGE(G_SB(0, 0), b2, vB0, vB1);
        BAR(); WAIT_L(0); SBAR(); G_MMA(0, 1, At, B1); SBAR(); BAR();
        G_LDA(At, 0, 1); G_STAGE(G_SA(0, 0), a2, vA0, vA1);
        if (ROUND == 1 && t == 0) WAIT_V(26); else WAIT_V(10);
        BAR(); WAIT_L(0); SBAR(); G_MMA(1, 0, At, B0); SBAR(); BAR();
        G_LDB(B0, 1, 0); G_STAGE(G_SB(0, 1), b2 + dB1, vB0, vB1);
        if (ROUND == 1 && t == 0) WAIT_V(22); else if ((ROUND == 1 && t == 4 && wid == 0) || (ROUND == 0 && t == 2)) WAIT_V(7); else WAIT_V(6);
        BAR(); SBAR(); G_MMA(1, 1, At, B1); SBAR(); BAR();
        if (ROUND == 1 && wid == 0) {
            if (t == 2) dma_words(lane * 4, ws + (size_t)fatt * 8, lds0 + 131072);
            if (last) check_parked<8>(12, lds + 131072, ws, fatt, tag, lane);
        }
        G_LDA(At, 1, 0); G_STAGE(G_SA(0, 1), a2 + dA1, vA0, vA1);
        BAR(); WAIT_L(0); SBAR(); G_MMA(0, 0, At, B0); SBAR(); BAR();
        G_LDB(B1, 1, 1); G_STAGE(G_SB(1, 0), b2 + dB2, vB0, vB1);
        BAR(); WAIT_L(0); SBAR(); G_MMA(0, 1, At, B1); SBAR(); BAR();
        G_LDA(At, 1, 1); G_STAGE(G_SA(1, 0), a2 + dA2, vA0, vA1);
        if (!last) WAIT_V(10);
        BAR(); WAIT_L(0); SBAR(); G_MMA(1, 0, At, B0); SBAR(); BAR();
        if (!last) G_LDB(B0, 0, 0);
        G_STAGE(G_SB(1, 1), b2 + dB2 + dB1, vB0, vB1);
        if (!(ROUND == 1 && last)) WAIT_V(6);
        BAR(); SBAR(); G_MMA(1, 1, At, B1); SBAR();
        if (!(last && wid >= 4)) BAR();
    }
}

DI void g_epilogue(const f32x16 (&acc)[2][2][2], const rsrc_t rws, const int type, const int pn, const int pm, const int wr, const int wc, const int lane) {
    constexpr float QS = 0.08838834764831845f * 1.4426950408889634f;
    const float sc = type == 2 ? QS : 1.0f;
    const unsigned base = (type == 2 ? WS_QF : WS_KVF + (type == 1 ? 8192 : 0)) + wc * 2048 + lane * 16;
    const unsigned hstride = type == 2 ? 8192 : 16384;
#pragma unroll
    for (int ai = 0; ai < 2; ++ai)
#pragma unroll
        for (int bj = 0; bj < 2; ++bj)
#pragma unroll
            for (int m = 0; m < 2; ++m) {
                const unsigned dst = base + (unsigned)((2 * pn + bj) * 64 + pm * 8 + ai * 4 + wr * 2 + m) * hstride;
                st16(rws, dst, pack8(acc[ai][bj][m], 0, sc)); st16(rws, dst + 1024, pack8(acc[ai][bj][m], 1, sc));
            }
}

#define NOP_ (void)0
#define SP_CHECK() if (t == 36) { poll_flags(ws, fatt + lane, (lane & 15) < 12, tag); ACQUIRE(); }
#define EX2(v) __builtin_amdgcn_exp2f(v)
#define MX3(a, b, c) __builtin_fmaxf(__builtin_fmaxf((a), (b)), (c))
constexpr float ATT_THR = 32.0f;
DI void att_mask(f32x16& s, int dq) {
#pragma unroll
    for (int e = 0; e < 16; ++e) if ((e & 3) + 8 * (e >> 2) > dq) s[e] = -__builtin_inff();
}

struct OutArgs { rsrc_t ra; const char* wo; const char* wu; lds_cp rd[4]; unsigned wv, uv[6], va, sc, su, ldsw; };
template <int T> DI void out_w(const OutArgs& o) {
    constexpr int S = (T % 9) * 16384;
    if constexpr (T < 16) dma_pair<S, S + 8192>(o.wv, o.wv + 32768, o.wo + T * 65536, o.ldsw);
    else if constexpr (T < 19) dma_pair<S, S + 8192>(o.uv[2 * (T - 16)], o.uv[2 * (T - 16) + 1], o.wu, o.ldsw);
}
template <int T> DI void out_a(const OutArgs& o, u32x4 (&af)[6][2]) {
    if constexpr (T < 19) {
        const unsigned so = T < 16 ? o.sc + T * 262144 : o.su + (T - 16) * 262144;
        asm volatile("buffer_load_dwordx4 %0, %1, %2, %3 offen sc1" : "=v"(af[T % 6][0]) : "v"(o.va), "s"(o.ra), "s"(so) : "memory");
        asm volatile("buffer_load_dwordx4 %0, %1, %2, %3 offen sc1" : "=v"(af[T % 6][1]) : "v"(o.va), "s"(o.ra), "s"(so + 65536) : "memory");
    }
}
constexpr int out_cnt(int i) { return (i + 8 < 19 ? 2 : 0) + (i + 5 < 19 ? 2 : 0); }
constexpr int out_allow(int t) { int n = t < 4 ? 2 * (4 - t) : 0; for (int i = t < 4 ? 0 : t - 4; i < t; ++i) n += out_cnt(i); return n; }
template <int T> DI void out_step(const OutArgs& o, u32x4 (&af)[6][2], bf16x8 (&wf)[2][4], f32x16 (&acc)[4]) {
    asm volatile("s_waitcnt vmcnt(%2)\n\ts_barrier" : "+v"(af[T % 6][0]), "+v"(af[T % 6][1]) : "n"(out_allow(T)) : "memory");
    const bf16x8 a0 = __builtin_bit_cast(bf16x8, af[T % 6][0]), a1 = __builtin_bit_cast(bf16x8, af[T % 6][1]);
#pragma unroll
    for (int c = 0; c < 4; ++c) wf[1][c] = ldtr2(o.rd[c] + (T % 9) * 16384 + 4096, 2048);
    SBAR();
    acc[0] = MFMA32(wf[0][0], a0, acc[0]); acc[1] = MFMA32(wf[0][1], a0, acc[1]); SBAR();
    out_w<T + 8>(o); SBAR();
    acc[2] = MFMA32(wf[0][2], a0, acc[2]); acc[3] = MFMA32(wf[0][3], a0, acc[3]); SBAR();
    if constexpr (T < 18) {
#pragma unroll
        for (int c = 0; c < 4; ++c) wf[0][c] = ldtr2(o.rd[c] + ((T + 1) % 9) * 16384, 2048);
    }
    SBAR();
    acc[0] = MFMA32(wf[1][0], a1, acc[0]); acc[1] = MFMA32(wf[1][1], a1, acc[1]); SBAR();
    out_a<T + 5>(o, af); SBAR();
    acc[2] = MFMA32(wf[1][2], a1, acc[2]); acc[3] = MFMA32(wf[1][3], a1, acc[3]); SBAR();
}

#define IT(h1, i1, h2, i2) ((h1) | (i1) << 2 | (h2) << 6 | (i2) << 8)
__device__ constexpr unsigned ITEMS[32] = {
    IT(0, 15, 0, 4), IT(1, 15, 1, 4), IT(2, 15, 2, 4), IT(3, 15, 3, 4), IT(0, 13, 0, 5), IT(1, 13, 1, 5), IT(2, 13, 2, 5), IT(3, 13, 3, 5),
    IT(0, 12, 0, 6), IT(1, 12, 1, 6), IT(2, 12, 2, 6), IT(3, 12, 3, 6), IT(0, 11, 0, 7), IT(1, 11, 1, 7), IT(2, 11, 2, 7), IT(3, 11, 3, 7),
    IT(0, 14, 0, 3), IT(1, 14, 1, 3), IT(2, 14, 2, 3), IT(3, 14, 3, 3), IT(0, 9, 0, 8), IT(1, 9, 1, 8), IT(2, 9, 2, 8), IT(3, 9, 3, 8),
    IT(0, 2, 0, 0), IT(1, 2, 1, 0), IT(2, 2, 2, 0), IT(3, 2, 3, 0), IT(0, 1, 1, 1), IT(2, 1, 3, 1), IT(0, 10, 1, 10), IT(2, 10, 3, 10)
};

__global__ __launch_bounds__(512, 2) void fused_fwd(const bf16_t* __restrict__ x, const bf16_t* __restrict__ ckv, const bf16_t* __restrict__ wq, const bf16_t* __restrict__ wk,
                                                    const bf16_t* __restrict__ wv, const bf16_t* __restrict__ wo, const bf16_t* __restrict__ w_in, const bf16_t* __restrict__ wsp,
                                                    const bf16_t* __restrict__ bsp, const bf16_t* __restrict__ wout, bf16_t* __restrict__ out, unsigned char* ws) {
    __shared__ __attribute__((aligned(16))) unsigned char smem[163840];
    const lds_p lds = (lds_p)smem;
    const unsigned lds0 = (unsigned)(uintptr_t)smem;
    const int tid = threadIdx.x, wid = __builtin_amdgcn_readfirstlane(tid >> 6), lane = tid & 63;
    const int bid = blockIdx.x, x8 = bid & 7, kk = bid >> 3;
    const int r = lane & 31, hh = lane >> 5, blk = (lane >> 4) & 1, qq = (lane & 15) >> 2, p = lane & 3;
    const rsrc_t rws = __builtin_amdgcn_make_buffer_rsrc((void*)ws, (short)0, WS_END, 0x00020000);
    const unsigned ldsw = lds0 + (unsigned)wid * 1024u;
    PROF_DECL;
    dma_words(lane * 4, ws, lds0 + LDS_TAG);
    u64 tag;
#define MAKE_TAG() do { const u32x2 e_ = *(const LAS u32x2*)(lds + LDS_TAG); \
        const u64 ep_ = ((u64)(unsigned)__builtin_amdgcn_readfirstlane(e_[1]) << 32) | (unsigned)__builtin_amdgcn_readfirstlane(e_[0]); \
        u64 z_ = ep_ + 0x9E3779B97F4A7C15ull; z_ = (z_ ^ (z_ >> 30)) * 0xBF58476D1CE4E5B9ull; z_ = (z_ ^ (z_ >> 27)) * 0x94D049BB133111EBull; z_ ^= z_ >> 31; tag = z_ == ep_ ? ~z_ : z_; } while (0)
    const unsigned item = ITEMS[kk];
    const int h1 = item & 3, i1 = (item >> 2) & 15, h2 = (item >> 6) & 3, i2 = (item >> 8) & 15;
    const unsigned char* kvx = ws + WS_KVF + (size_t)x8 * 4 * 64 * 16384;
    const unsigned char* kvh = kvx + (size_t)h1 * 64 * 16384;
    const char* q1src = (const char*)ws + WS_QF + (size_t)((4 * x8 + h1) * 64 + 4 * i1 + (wid & 3)) * 8192;
    bf16x8 q[8];
#define Q1_LOAD() do { _Pragma("unroll") for (int d = 0; d < 8; ++d) asm volatile("global_load_dwordx4 %0, %1, %2 offset:%3 sc1" : "=v"(q[d]) : "v"(lane * 16 + (d >> 2) * 4096), "s"(q1src), "i"((d & 3) * 1024) : "memory"); } while (0)
#define Q1_WAIT(n) asm volatile("s_waitcnt vmcnt(" #n ")" : "+v"(q[0]), "+v"(q[1]), "+v"(q[2]), "+v"(q[3]), "+v"(q[4]), "+v"(q[5]), "+v"(q[6]), "+v"(q[7]) :: "memory")
    const int fatt = F_K + x8 * 64;
#define Q2_LOAD() do { const char* q2src = (const char*)ws + WS_QF + (size_t)((4 * x8 + h2) * 64 + 4 * i2) * 8192 + wid * 4096; \
        const unsigned lq_ = __builtin_amdgcn_readfirstlane(lds0 + wid * 4096); \
        dma_pair<131072, 131072 + 1024>(lane * 16, lane * 16 + 1024, q2src, lq_); dma_pair<131072 + 2048, 131072 + 3072>(lane * 16 + 2048, lane * 16 + 3072, q2src, lq_); } while (0)
    Run rat;
    rat.A = (const char*)kvh; rat.B = (const char*)kvh + 32768; rat.dA1 = rat.dB1 = 65536; rat.dA2 = rat.dB2 = 8192;
    rat.vA0 = rat.vB0 = wid * 1024 + lane * 16; rat.vA1 = rat.vB1 = rat.vA0 + 16384;
    int pf;
    int aoff[4];
    const unsigned arow = 8 * wid + (lane >> 3), achk = ((lane & 7) ^ (((wid & 1) << 2) + (lane >> 4))) << 4;
    const unsigned brow = 4 * wid + (lane >> 4), bchk = ((lane & 15) ^ (((lane >> 4) & 3) << 2)) << 4;

    if (kk < 24) {
        const int type = kk >> 3, pn = 2 * x8 + ((kk >> 2) & 1), pm0 = (kk & 3) + (type == 2 ? 4 : 0), pm1 = pm0 ^ 4, wr = wid >> 2, wc = wid & 3;
        Run u0, u1;
        const char* Ab = type == 2 ? (const char*)ws + WS_XN : (const char*)ckv;
        u0.B = (const char*)(type == 0 ? wk : type == 1 ? wv : wq) + pn * 512; u0.A = Ab + (size_t)pm0 * 256 * LDA;
        u0.dA1 = 128 * LDA; u0.dA2 = 128; u0.dB1 = 256; u0.dB2 = 64 * LDB;
        u0.vA0 = arow * LDA + achk; u0.vA1 = u0.vA0 + 64 * LDA; u0.vB0 = brow * LDB + bchk; u0.vB1 = u0.vB0 + 32 * LDB;
        u1 = u0; u1.A = Ab + (size_t)pm1 * 256 * LDA;
        const unsigned tch = (type == 2 ? ((kk >> 2) & 1) * 2 : type * 2 + ((kk >> 2) & 1)) * 65536 + tid * 128, bl = ((kk & 3) * 128 + wid * 16 + (lane >> 2)) * LDB + (lane & 3) * 128;
        if (type < 2) {
            g_prologue_b(u0, ldsw); g_prologue_rest(u0, ldsw);
            PROF(1);
            WAIT_V(6);
            MAKE_TAG();
        } else {
            const int j = x8 * 8 + (kk - 16), row = 32 * j + 4 * wid + (lane >> 4);
            const u32x4* src = (const u32x4*)(x + (size_t)row * DM) + (lane & 15);
            u32x4 v[4];
#pragma unroll
            for (int i = 0; i < 4; ++i) v[i] = src[16 * i];
            SBAR();
            g_prologue_b(u0, ldsw); touch(bl, u0.B, lds0);
            SBAR();
            float s = 0.f;
#pragma unroll
            for (int i = 0; i < 4; ++i)
#pragma unroll
                for (int e = 0; e < 4; ++e) s += bflo(v[i][e]) + bfhi(v[i][e]);
            s += __shfl_xor(s, 1); s += __shfl_xor(s, 2); s += __shfl_xor(s, 4); s += __shfl_xor(s, 8);
            const float mean = s * (1.0f / DM);
            float q = 0.f;
#pragma unroll
            for (int i = 0; i < 4; ++i)
#pragma unroll
                for (int e = 0; e < 4; ++e) { const float a = bflo(v[i][e]) - mean, b = bfhi(v[i][e]) - mean; q += a * a + b * b; }
            q += __shfl_xor(q, 1); q += __shfl_xor(q, 2); q += __shfl_xor(q, 4); q += __shfl_xor(q, 8);
            const float rstd = 1.0f / sqrtf(q * (1.0f / DM) + 1e-5f);
#pragma unroll
            for (int i = 0; i < 4; ++i) {
                u32x4 o;
#pragma unroll
                for (int e = 0; e < 4; ++e) o[e] = cvtpk((bflo(v[i][e]) - mean) * rstd, (bfhi(v[i][e]) - mean) * rstd);
                st16(rws, WS_XN + row * 1024 + ((lane & 15) + 16 * i) * 16, o);
            }
            WAIT_V(0);
            MAKE_TAG();
            BAR();
            if (tid == 0) set_flag(ws, F_X + j, tag);
            if (wid == 0) { poll_flags(ws, F_X + ((lane & 8) ? pm1 : pm0) * 8 + (lane & 7), lane < 16, tag); ACQUIRE(); WAIT_V(0); }
            BAR();
            g_prologue_rest(u0, ldsw);
            PROF(1);
            WAIT_V(6);
        }
#pragma unroll
        for (int k = 0; k < 4; ++k) aoff[k] = (wr * 64 + r) * 128 + (((2 * k + hh) ^ ((r >> 1) & 7)) << 4);
        const int boff = (8 * hh + qq) * 256 + ((wc ^ qq) << 6) + blk * 32 + p * 8;
        BAR();
        if (wr == 1) BAR();
        PROF(2);
        f32x16 acc[2][2][2];
        const int fk = fatt + (pn & 1) * 16 + type * 4 + (kk & 3);
        if (type == 1) g_kloop<true, 0>(lds, lds0, acc, u0, u1, aoff, boff, ldsw, ws, tag, F_S + bid, fatt, lane, wid, tch);
        else           g_kloop<false, 0>(lds, lds0, acc, u0, u1, aoff, boff, ldsw, ws, tag, F_S + bid, fatt, lane, wid, tch);
        PROF(3);
        G_STAGE(G_SA(1, 1), u1.A + 128 + 128 * LDA, u1.vA0, u1.vA1);
        SBAR();
        g_epilogue(acc, rws, type, pn, pm0, wr, wc, lane);
        PROF(11);
        if (wr == 1) BAR();
        if (type == 1) g_kloop<true, 1>(lds, lds0, acc, u1, rat, aoff, boff, ldsw, ws, tag, fk, fatt, lane, wid, tch);
        else           g_kloop<false, 1>(lds, lds0, acc, u1, rat, aoff, boff, ldsw, ws, tag, fk, fatt, lane, wid, tch);
        PROF(4);
        Q1_LOAD();
        G_STAGE(G_SA(1, 1), rat.A + rat.dA1 + rat.dA2, rat.vA0, rat.vA1);
        SBAR();
        g_epilogue(acc, rws, type, pn, pm1, wr, wc, lane);
        PROF(12);
        pf = fk + 32;
        Q1_WAIT(18);
    } else {
        const int pm = x8, pn = kk - 24, grp = wid >> 2;
        constexpr unsigned SLA = SEQ * 2, SLB = 768 * 2, WROW = FF * 2;
        constexpr int RB = 65536, RA2 = 131072;
        const char* Ap = (const char*)ws + WS_XN + (size_t)pm * 256 * LDA;
        const char* Bp = (const char*)w_in + pn * 256;
        unsigned vA0 = arow * LDA + achk, vA1 = vA0 + 64 * LDA, vB0 = brow * WROW + bchk, vB1 = vB0 + 32 * WROW, dA = 128 * LDA, dB = 64 * WROW;
        const int tb0 = 8 * pm + 2 * (wid & 3), fs0 = 8 * pn + 4 * grp;
        float bias[2];
        const int flz = F_L + (4 * x8 + (pn >> 1)) * 2 + (kk & 1);
        {
            f32x16 za[3];
            const int rg = 4 * x8 + (pn >> 1), hf = kk & 1, m0_ = wid & 1;
#pragma unroll
            for (int m = 0; m < 2; ++m) bias[m] = __uint_as_float((unsigned)bsp[(tb0 + m) * 32 + r] << 16);
            z2_half(lds, lds0, za, x, w_in, ws + (F_X + 8 * pm) * 8, rg, hf, tid, wid, lane
                );
            PIN(bias[0]); PIN(bias[1]);
            PROF(3);
            MAKE_TAG();
            if (wid == 0 && kk < 30) {
                const unsigned w = *(const LAS unsigned*)(lds + LDS_PARK + lane * 4);
                if (!__all(lane >= 16 || w == ((lane & 1) ? (unsigned)(tag >> 32) : (unsigned)tag))) poll_flags(ws, F_X + 8 * pm + (lane & 7), true, tag);
                ACQUIRE();
            }
            {
                const char* slab = (const char*)wsp + (size_t)x8 * 1048576 + pn * 131072;
                TOUCH_S(tid * 128, slab, lds0); TOUCH_S(tid * 128 + 65536, slab, lds0);
            }
            u32x4 zp[3][2];
#pragma unroll
            for (int i = 0; i < 3; ++i) { zp[i][0] = gelu8(za[i], 0); zp[i][1] = gelu8(za[i], 1); }
            const int nx = wid < 4 ? 8 : wid < 6 ? 6 : 0;
            LAS float* red = (LAS float*)lds;
            float s[2] = {0.f, 0.f}, mean[2];
#pragma unroll
            for (int i = 0; i < 24; ++i) if (i < 16 || i - 16 < nx) s[(i >> 3) & 1] = sum_pair(zp[i >> 3][(i >> 2) & 1][i & 3], s[(i >> 3) & 1]);
            red[wid * 128 + m0_ * 64 + lane] = s[0]; red[wid * 128 + (m0_ ^ 1) * 64 + lane] = s[1];
            WAIT_L(0); BAR();
#pragma unroll
            for (int m = 0; m < 2; ++m) { float t = 0.f;
#pragma unroll
                for (int w = 0; w < 8; ++w) t += red[w * 128 + (m0_ ^ m) * 64 + lane];
                mean[m] = swap_add(t) * (2.0f / FH); }
            f32x2 d[24], q2[2] = {{0.f, 0.f}, {0.f, 0.f}};
#pragma unroll
            for (int i = 0; i < 24; ++i) { const int m = (i >> 3) & 1; d[i] = unpk(zp[i >> 3][(i >> 2) & 1][i & 3]) - (f32x2){mean[m], mean[m]};
                if (i < 16 || i - 16 < nx) q2[m] = __builtin_elementwise_fma(d[i], d[i], q2[m]); }
            red[1024 + wid * 128 + m0_ * 64 + lane] = q2[0][0] + q2[0][1]; red[1024 + wid * 128 + (m0_ ^ 1) * 64 + lane] = q2[1][0] + q2[1][1];
            GP(9);
            WAIT_V(2); WAIT_L(0); BAR();
            LAS f32x2* tab = (LAS f32x2*)(lds + 160768);
            if (wid == 0) {
#pragma unroll
                for (int m = 0; m < 2; ++m) { float t = 0.f;
#pragma unroll
                    for (int w = 0; w < 8; ++w) t += red[1024 + w * 128 + m * 64 + lane];
                    s[m] = swap_add(t); }
                const float mo = hh ? mean[1] : mean[0], qo = hh ? s[1] : s[0];
                const unsigned t32 = (unsigned)tag;
                st16(rws, WS_G + (rg * 2 + hf) * 1024 + lane * 16, (u32x4){__float_as_uint(mo), t32, __float_as_uint(qo), t32});
                u32x4 o;
#pragma unroll 1
                for (unsigned spins = 0; spins < (1u << 17); ++spins) {
                    o = ld16(rws, WS_G + (rg * 2 + (hf ^ 1)) * 1024 + lane * 16);
                    if (__all(o[1] == t32 && o[3] == t32)) break;
                    __builtin_amdgcn_s_sleep(2);
                }
                const float mp = __uint_as_float(o[0]), dl = mo - mp;
                tab[lane] = (f32x2){0.5f * (mo + mp), 1.0f / sqrtf((qo + __uint_as_float(o[2]) + dl * dl * (FH * 0.25f)) * (1.0f / FH) + 1e-5f)};
            }
            WAIT_L(0); BAR();
            GP(10);
            if (kk < 30) {
                dma_pair<0, 8192>(vA0, vA1, Ap, ldsw); dma_pair<16384, 24576>(vA0, vA1, Ap + 128 * LDA, ldsw); dma_pair<RB, RB + 8192>(vB0, vB1, Bp, ldsw);
                dma_pair<32768, 32768 + 8192>(vA0, vA1, Ap + 128, ldsw); dma_pair<32768 + 16384, 32768 + 24576>(vA0, vA1, Ap + 128 + 128 * LDA, ldsw);
                dma_pair<RB + 16384, RB + 16384 + 8192>(vB0, vB1, Bp + 64 * WROW, ldsw); dma_pair<RB + 32768, RB + 32768 + 8192>(vB0, vB1, Bp + 128 * WROW, ldsw);
            }
            f32x2 rs[2], of[2];
#pragma unroll
            for (int m = 0; m < 2; ++m) { const f32x2 t = tab[(m0_ ^ m) * 32 + r]; const float o = (mean[m] - t[0]) * t[1]; rs[m] = (f32x2){t[1], t[1]}; of[m] = (f32x2){o, o}; }
            const lds_p tr = lds + 114688;
#pragma unroll
            for (int i = 0; i < 12; ++i)
                if (i < 8 || 2 * (i - 8) < nx) {
                    const int b = i >> 2, m = b & 1;
                    *(LAS u32x2*)(tr + ((m0_ ^ m) * 32 + r) * 720 + (b == 2 ? 8 + (wid >> 1) : wid) * 64 + (i & 3) * 16 + hh * 8) =
                        (u32x2){cvt2(__builtin_elementwise_fma(d[2 * i], rs[m], of[m])), cvt2(__builtin_elementwise_fma(d[2 * i + 1], rs[m], of[m]))};
                }
            WAIT_L(0); BAR();
#pragma unroll
            for (int i = 0; i < 6; ++i) { int c = tid + 512 * i; const bool ok = c < 2752; c = ok ? c : 0; const int zr = c / 43, zc = c - zr * 43;
                st16(rws, ok ? WS_Z2N + (rg * 64 + zr) * SLB + hf * 688 + zc * 16 : 0x80000000u, *(const LAS u32x4*)(tr + zr * 720 + zc * 16)); }
#pragma unroll
            for (int i = 0; i < 2; ++i) { const int c = tid + 512 * i, zr = c / 10;
                st16(rws, hf == 1 && c < 640 ? WS_Z2N + (rg * 64 + zr) * SLB + 1376 + (c - zr * 10) * 16 : 0x80000000u, (u32x4){0u, 0u, 0u, 0u}); }
        }
        PROF(1);
        if (kk < 30) {
            u32x4 z1[2][2][2];
#pragma unroll
            for (int k = 0; k < 4; ++k) aoff[k] = ((wid >> 1) & 1) * 16384 + ((wid & 1) * 64 + r) * 128 + (((2 * k + hh) ^ ((r >> 1) & 7)) << 4);
            const int bo0 = RB + (8 * hh + qq) * 256 + (((2 * grp) ^ qq) << 6) + blk * 32 + p * 8, bo1 = RB + (8 * hh + qq) * 256 + (((2 * grp + 1) ^ qq) << 6) + blk * 32 + p * 8;
            f32x16 acc[2][2];
#pragma unroll
            for (int c = 0; c < 4; ++c) acc[c >> 1][c & 1] = f32x16{};
#pragma unroll
            for (int c = 0; c < 8; ++c) z1[c >> 2][(c >> 1) & 1][c & 1] = (u32x4){0u, 0u, 0u, 0u};
            Ap += 256; Bp += 192 * WROW;
            unsigned sa = 0, sa1 = 32768, sa2 = RA2;
            unsigned la = ldsw + RA2, la1 = ldsw, la2 = ldsw + 32768, lb = 49152;
            const char* Aq = Ap; unsigned lq = la, lq1 = la1, lq2 = la2, vQ0 = vA0, vQ1 = vA1, dQ = dA;
#define SP_SRC(s_) do { if ((s_) == 6) { Ap = (const char*)wsp + (size_t)pm * 256 * SLA; vA1 = arow * SLA + achk + 64 * SLA; dA = 128 * SLA; } \
                if ((s_) == 5) { Bp = (const char*)ws + WS_Z2N + pn * 256; vB0 = brow * SLB + bchk; vB1 = vB0 + 32 * SLB; dB = 64 * SLB; } \
                if (t == 6) { Aq = (const char*)wsp + (size_t)pm * 256 * SLA; vQ0 = arow * SLA + achk; vQ1 = vQ0 + 64 * SLA; dQ = 128 * SLA; } } while (0)
#define SP_LATE() do { dma_pair<0, 8192>(vQ0, vQ1, Aq, lq); dma_one<16384>(vQ0, Aq + dQ, lq); Aq += 128; const unsigned l_ = lq; lq = lq1; lq1 = lq2; lq2 = l_; } while (0)
#define SP_A1() dma_one<24576>(vA1, Ap + dA, la)
#define SP_B0() dma_one<RB>(vB0, Bp, ldsw + lb)
#define SP_B1() dma_one<RB + 8192>(vB1, Bp, ldsw + lb)
#define SP_ADV() do { Ap += 128; Bp += dB; const unsigned l_ = la; la = la1; la1 = la2; la2 = l_; lb = (lb + 16384) & 49152; } while (0)
#define SP_GELU() do { _Pragma("unroll") for (int c = 0; c < 4; ++c) { f32x16& a = acc[c >> 1][c & 1]; z1[c >> 1][c & 1][0] = gelu8(a, 0); z1[c >> 1][c & 1][1] = gelu8(a, 1); a = f32x16{}; } } while (0)
#define SP_MMA(k) do { _Pragma("unroll") for (int c = 0; c < 4; ++c) acc[c >> 1][c & 1] = MFMA32(Bt[c & 1][k], At[c >> 1][k], acc[c >> 1][c & 1]); } while (0)
#define SP_STEP(PRE, LATE, WAIT0, WAIT1, D0, D1, D2, POST, LASTBAR) do { \
                bf16x8 At[2][4], Bt[2][4]; \
                PRE; \
                const lds_cp pa = lds + sa, pb = lds + (t & 3) * 16384; \
                _Pragma("unroll") for (int k = 0; k < 4; ++k) { Bt[0][k] = ldtr2(pb + bo0 + k * 4096, 1024); At[0][k] = ld128(pa + aoff[k]); At[1][k] = ld128(pa + aoff[k] + 4096); Bt[1][k] = ldtr2(pb + bo1 + k * 4096, 1024); } \
                LATE; \
                if (grp == 1) { WAIT1; } \
                WAIT_L(0); BAR(); SBAR(); \
                __builtin_amdgcn_s_setprio(1); \
                SP_MMA(0); SBAR(); D0; SBAR(); SP_MMA(1); SBAR(); D1; SBAR(); SP_MMA(2); SBAR(); D2; SBAR(); SP_MMA(3); \
                __builtin_amdgcn_s_setprio(0); \
                SBAR(); \
                POST; \
                if (grp == 0) { WAIT0; } \
                if (LASTBAR) BAR(); \
                const unsigned s_ = sa; sa = sa1; sa1 = sa2; sa2 = s_; } while (0)
            asm volatile("s_waitcnt vmcnt(16) lgkmcnt(0)\n\ts_barrier" ::: "memory");
            if (grp == 1) { SP_A1(); SP_B0(); SP_B1(); SP_ADV(); BAR(); }
            PROF(2);
            int t = 0;
#pragma unroll 1
            for (; t < 8; ++t)
                SP_STEP({ if (wid == 0) { if (t == 2 && lane == 0) { set_flag(ws, flz, tag); set_flag(ws, F_S + bid, tag); } if (t == 3) { poll_flags(ws, F_L + lane, true, tag); ACQUIRE(); } } SP_SRC(t + grp); },
                        SP_LATE(), { if (t == 0) WAIT_V(16); else WAIT_V(8); }, { if (t == 0) WAIT_V(16); else WAIT_V(6); }, SP_A1(), SP_B0(), SP_B1(), { SP_ADV(); if (t == 7 && grp == 1) SP_GELU(); }, true);
            PROF(11);
            if (grp == 0) SP_GELU();
            PROF(12);
#pragma unroll 1
            for (; t < 36; ++t) SP_STEP(NOP_, SP_LATE(), WAIT_V(8), WAIT_V(6), SP_A1(), SP_B0(), SP_B1(), SP_ADV(), true);
#pragma unroll 1
            for (; t < 40; ++t)
                SP_STEP({ if (wid == 0) { SP_CHECK(); } }, if (t < 38) SP_LATE(),
                        { if (t == 36) WAIT_V(8); else if (t == 37) WAIT_V(6); else WAIT_V(0); }, { if (t == 36) WAIT_V(6); else if (t == 37) WAIT_V(4); else WAIT_V(0); },
                        if (t + grp < 38) SP_A1(), if (t + grp < 37) SP_B0(), if (t + grp < 37) SP_B1(), SP_ADV(), !(t == 39 && grp == 1));
            PROF(4);
            Q1_LOAD();
            G_STAGE(G_SA(0, 0), rat.A, rat.vA0, rat.vA1); G_STAGE(G_SB(0, 0), rat.B, rat.vA0, rat.vA1); G_STAGE(G_SA(1, 0), rat.A + 8192, rat.vA0, rat.vA1); G_STAGE(G_SB(1, 0), rat.B + 8192, rat.vA0, rat.vA1);
            G_STAGE(G_SA(0, 1), rat.A + 65536, rat.vA0, rat.vA1); G_STAGE(G_SB(0, 1), rat.B + 65536, rat.vA0, rat.vA1); G_STAGE(G_SA(1, 1), rat.A + 73728, rat.vA0, rat.vA1); G_STAGE(G_SB(1, 1), rat.B + 73728, rat.vA0, rat.vA1);
            Q2_LOAD();
            SBAR();
#pragma unroll
            for (int m = 0; m < 2; ++m)
#pragma unroll
                for (int c = 0; c < 4; ++c) {
                    const f32x16& a = acc[m][c >> 1]; const u32x4 z = z1[m][c >> 1][c & 1]; const int s8 = 8 * (c & 1);
                    u32x4 o;
#pragma unroll
                    for (int e = 0; e < 4; ++e) o[e] = cvtpk(bflo(z[e]) * (a[s8 + 2 * e] + bias[m]), bfhi(z[e]) * (a[s8 + 2 * e + 1] + bias[m]));
                    st16(rws, fs0 + c < 43 ? WS_AF + ((256 + fs0 + c) * 64 + tb0 + m) * 1024 + lane * 16 : 0x80000000u, o);
                }
            pf = F_U + pm * 8 + pn;
            Q1_WAIT(20);
        } else {
            WAIT_V(0); WAIT_L(0); BAR();
            if (tid == 0) { set_flag(ws, flz, tag); set_flag(ws, F_S + bid, tag); }
            {
                const int aw = x8 * 2 + (kk - 30);
#pragma unroll
                for (int i = 0; i < 4; ++i) touch(tid * 128 + i * 65536, (const char*)wo + aw * 262144, lds0);
                if (tid < 344) touch(tid * 128, (const char*)wout + aw * 44032, lds0);
            }
            if (wid == 0) { poll_flags(ws, fatt + (lane & 31), lane < 32 && (lane & 15) < 12, tag); ACQUIRE(); }
            WAIT_V(0); BAR();
            PROF(4);
            g_prologue_b(rat, ldsw); g_prologue_rest(rat, ldsw); G_STAGE(G_SA(1, 1), rat.A + rat.dA1 + rat.dA2, rat.vA0, rat.vA1);
            Q1_LOAD();
            pf = -1;
            Q1_WAIT(0);
        }
    }
    BAR();
    PROF(5);
    {
        const int kh = wid >> 2, rg = wid & 3;
        const bool early = kk < 24 || kk >= 30;
        const rsrc_t rkv = __builtin_amdgcn_make_buffer_rsrc((void*)kvx, (short)0, 4 * 64 * 16384, 0x00020000);
        const unsigned dv0 = rg * 2048 + lane * 16 + kh * 32768;
        const unsigned wrapoff = (unsigned)(h1 * 16 + i1 + 1) * 65536u, wrapto = (unsigned)h2 << 20;
        unsigned sA_ = 0, sB_ = ((unsigned)h1 << 20) + 65536u, lA = lds0 + kh * 65536 + rg * 2048, lB = lA + 16384;
#define A_ADV() do { sA_ = sB_; sB_ += 65536u; if (sB_ == wrapoff) sB_ = wrapto; const unsigned t_ = lA; lA = lB; lB = t_; } while (0)
#define D_KO(h) (bdma1<8192, (h) * 1024>(dv0, rkv, sA_ + 16384, lA))
#define D_VE(h) (bdma1<32768, (h) * 1024>(dv0, rkv, sA_ + 8192, lA))
#define D_KE(h) (bdma1<0, (h) * 1024>(dv0, rkv, sB_, lB))
#define D_VO(h) (bdma1<40960, (h) * 1024>(dv0, rkv, sA_ + 24576, lA))
#define PUBLISH() do { if (pf >= 0) { if (wid == 0 && lane == 0) set_flag(ws, pf, tag); pf = -1; } } while (0)
        const lds_cp qrd = lds + 131072 + rg * 8192 + lane * 16;
        const lds_cp rbase = lds + kh * 65536 + lane * 16;
        int so = 0;
#pragma unroll 1
        for (int blkno = 0; blkno < 2; ++blkno) {
            const int ib = blkno ? i2 : i1, head = 4 * x8 + (blkno ? h2 : h1);
            const int qrow = 128 * ib + 32 * rg + r - 4 * hh - 64 * kh;
            f32x16 o[4], sA, sB, negm;
            bf16x8 fr[8];
            u32x4 pw0, pw1;
            float mref, l0 = 0.f, l1 = 0.f, resc = 1.0f, ma, mb;
            bool dead = false, pend = false;
            lds_cp pc = rbase + so, pn = rbase + (so ^ 16384);
#pragma unroll
            for (int d = 0; d < 8; ++d) fr[d] = ld128(pc + d * 1024);
            sA = f32x16{};
#pragma unroll
            for (int d = 0; d < 8; ++d) sA = MFMA32(fr[d], q[d], sA);
            asm volatile("s_waitcnt lgkmcnt(0)\n\ts_barrier" ::: "memory");
            A_ADV();
            if (blkno == 1) { D_KO(0); D_KO(1); }
            if (blkno == 1 && wid < 5) {
                if (wid < 4) touch(lane * 128, (const char*)wo + (size_t)(x8 >> 1) * 1048576 + kk * 32768 + wid * 8192, lds0);
                else { const unsigned ur = 192 * (x8 >> 1) + 6 * kk + (lane >> 3); touch((ur > FH - 1 ? FH - 1 : ur) * 1024 + (lane & 7) * 128, wout, lds0); }
            }
#pragma unroll
            for (int d = 0; d < 4; ++d) fr[d] = ld128(pc + 8192 + d * 1024);
#pragma unroll
            for (int d = 0; d < 4; ++d) o[d] = f32x16{};
            if (ib == 0) att_mask(sA, qrow);
            {
                float m = MX3(sA[0], sA[1], sA[2]);
#pragma unroll
                for (int e = 3; e < 15; e += 2) m = MX3(m, sA[e], sA[e + 1]);
                m = swap_max(__builtin_fmaxf(m, sA[15]));
                dead = m == -__builtin_inff();
                mref = dead ? 0.f : m;
#pragma unroll
                for (int e = 0; e < 16; ++e) { sA[e] -= mref; negm[e] = -mref; }
                PIN(negm);
#pragma unroll
                for (int e = 0; e < 6; ++e) sA[e] = EX2(sA[e]);
                pw0[0] = cvtpk(sA[0], sA[1]); pw0[1] = cvtpk(sA[2], sA[3]);
            }
#define A_RD(j, ptr) do { fr[j] = ld128(ptr); } while (0)
#define ANYCHK(x) (x)
#define XE(S, i) S[i] = EX2(S[i])
#define A_P1(SC, SN, KP, VP, E0, E1, E2) do { \
            A_RD(4, (KP) + 4096); SBAR(); SN = MFMA32(fr[0], q[0], negm); XE(SC, 6); XE(SC, 7); pw0[2] = cvtpk(SC[4], SC[5]); PIN(SC); PIN(pw0); SBAR(); \
            A_RD(5, (KP) + 5120); SBAR(); SN = MFMA32(fr[1], q[1], SN); XE(SC, 8); XE(SC, 9); pw0[3] = cvtpk(SC[6], SC[7]); PIN(SC); PIN(pw0); E0; SBAR(); \
            A_RD(6, (KP) + 6144); SBAR(); SN = MFMA32(fr[2], q[2], SN); XE(SC, 10); XE(SC, 11); l0 += SC[0]; PIN(SC); PIN(l0); SBAR(); \
            A_RD(7, (KP) + 7168); SBAR(); SN = MFMA32(fr[3], q[3], SN); XE(SC, 12); XE(SC, 13); l1 += SC[1]; PIN(SC); PIN(l1); SBAR(); \
            A_RD(0, (VP));        SBAR(); SN = MFMA32(fr[4], q[4], SN); XE(SC, 14); XE(SC, 15); pw1[0] = cvtpk(SC[8], SC[9]); PIN(SC); PIN(pw1); E1; SBAR(); \
            A_RD(1, (VP) + 2048); SBAR(); SN = MFMA32(fr[5], q[5], SN); l0 += SC[2]; l1 += SC[3]; l0 += SC[4]; l1 += SC[5]; l0 += SC[6]; PIN(l0); PIN(l1); SBAR(); \
            A_RD(2, (VP) + 4096); SBAR(); SN = MFMA32(fr[6], q[6], SN); l1 += SC[7]; l0 += SC[8]; l1 += SC[9]; l0 += SC[10]; pw1[1] = cvtpk(SC[10], SC[11]); PIN(l0); PIN(l1); PIN(pw1); SBAR(); \
            A_RD(3, (VP) + 6144); SBAR(); SN = MFMA32(fr[7], q[7], SN); l1 += SC[11]; l0 += SC[12]; l1 += SC[13]; pw1[2] = cvtpk(SC[12], SC[13]); PIN(l0); PIN(l1); PIN(pw1); E2; SBAR(); } while (0)
#define PWF(x) __builtin_bit_cast(bf16x8, x)
#define A_P2A(SC, SN, VP, MASK, DQ) do { \
            A_RD(4, (VP) + 1024); A_RD(5, (VP) + 3072); SBAR(); o[0] = MFMA32(fr[0], PWF(pw0), o[0]); l0 += SC[14]; l1 += SC[15]; pw1[3] = cvtpk(SC[14], SC[15]); PIN(l0); PIN(l1); PIN(pw1); SBAR(); \
            A_RD(6, (VP) + 5120); A_RD(7, (VP) + 7168); SBAR(); o[1] = MFMA32(fr[1], PWF(pw0), o[1]); \
            if (MASK) att_mask(SN, DQ); \
            ma = MX3(SN[0], SN[1], SN[2]); mb = MX3(SN[3], SN[4], SN[5]); PIN(ma); PIN(mb); SBAR(); \
            o[2] = MFMA32(fr[2], PWF(pw0), o[2]); ma = MX3(ma, SN[6], SN[7]); mb = MX3(mb, SN[8], SN[9]); PIN(ma); PIN(mb); ma = MX3(ma, SN[10], SN[11]); PIN(ma); SBAR(); \
            o[3] = MFMA32(fr[3], PWF(pw0), o[3]); mb = MX3(mb, SN[12], SN[13]); ma = MX3(ma, SN[14], SN[15]); PIN(ma); PIN(mb); SBAR(); } while (0)
#define A_CHECK(SN) do { if (__builtin_expect(ANYCHK((__builtin_amdgcn_fcmpf(ma, ATT_THR, 2) | __builtin_amdgcn_fcmpf(mb, ATT_THR, 2)) != 0), 0)) {            \
                const float dl_ = __builtin_fmaxf(swap_max(__builtin_fmaxf(ma, mb)), 0.f); mref += dl_; resc = EX2(-dl_); l0 *= resc; l1 *= resc; pend = true; \
                _Pragma("unroll") for (int e = 0; e < 16; ++e) { SN[e] -= dl_; negm[e] = -mref; } PIN(negm); } SBAR(); } while (0)
#define A_RESC() do { if (__builtin_expect(pend, 0)) { pend = false;         \
            _Pragma("unroll") for (int d = 0; d < 4; ++d) _Pragma("unroll") for (int e = 0; e < 16; ++e) o[d][e] *= resc; } } while (0)
#define A_P2B(SN, RD0, RD1, RD2, RD3, D0, D1) do { \
            RD0; SBAR(); o[0] = MFMA32(fr[4], PWF(pw1), o[0]); SBAR(); A_CHECK(SN); D0; SBAR(); \
            RD1; SBAR(); o[1] = MFMA32(fr[5], PWF(pw1), o[1]); XE(SN, 0); XE(SN, 1); PIN(SN); SBAR(); \
            RD2; SBAR(); o[2] = MFMA32(fr[6], PWF(pw1), o[2]); XE(SN, 2); XE(SN, 3); pw0[0] = cvtpk(SN[0], SN[1]); PIN(SN); PIN(pw0); D1; SBAR(); \
            RD3; SBAR(); o[3] = MFMA32(fr[7], PWF(pw1), o[3]); XE(SN, 4); XE(SN, 5); pw0[1] = cvtpk(SN[2], SN[3]); PIN(SN); PIN(pw0); SBAR(); \
            A_RESC(); } while (0)
#define A_P2K(SN, KN, D0, D1) A_P2B(SN, A_RD(0, (KN)), A_RD(1, (KN) + 1024), A_RD(2, (KN) + 2048), A_RD(3, (KN) + 3072), D0, D1)
#define UNP(...) __VA_ARGS__
#define PROFO(i) PROF(i)
#pragma unroll 1
            for (int n = 0; n < ib; ++n) {
                A_P1(sA, sB, pc + 8192, pc + 32768, D_VE(0), D_VE(1), D_KE(0));
                A_P2A(sA, sB, pc + 32768, false, 0);
                A_P2K(sB, pn, D_KE(1), D_VO(0));
                A_P1(sB, sA, pn, pc + 32768 + 8192, D_VO(1), NOP_, NOP_);
                A_P2A(sB, sA, pc + 32768 + 8192, (n == ib - 1), qrow - 128 * ib);
                const int ev = blkno == 0 && (early || n == 0) ? n : 6;
                if (ev != 0) WAIT_V(0); else if (kk < 24) WAIT_V(22); else WAIT_V(14);
                asm volatile("s_waitcnt lgkmcnt(0)\n\ts_barrier" ::: "memory");
#define A_EV() do { A_ADV(); if (ev != 0) PUBLISH(); \
                if (ev < 6) { \
                    if (ev == 3 && wid == 0) dma_words(lane * 4, ws + (size_t)(fatt + 32) * 8, lds0 + 131072); \
                    if (ev == 4 && wid == 0) { check_parked<0>(i2 >= 8 ? 8 : 12, lds + 131072, ws, fatt + 32, tag, lane); ACQUIRE(); } \
                    if (ev == 5) Q2_LOAD(); } \
                D_KO(0); } while (0)
                A_P2B(sA, A_RD(0, pn + 8192), A_RD(1, pn + 9216), A_RD(2, pn + 10240), A_RD(3, pn + 11264), A_EV(), D_KO(1));
                so ^= 16384;
                const lds_cp t_ = pc; pc = pn; pn = t_;
            }
            if (blkno == 0) PROF(6);
            lA = __builtin_amdgcn_readfirstlane(lA); lB = __builtin_amdgcn_readfirstlane(lB); sA_ = __builtin_amdgcn_readfirstlane(sA_); sB_ = __builtin_amdgcn_readfirstlane(sB_);
            A_P1(sA, sB, pc + 8192, pc + 32768, D_VE(0); D_VE(1), D_KE(0); D_KE(1), D_VO(0); D_VO(1));
            if (blkno == 0) {
#pragma unroll
                for (int d = 0; d < 8; ++d) q[d] = ld128(qrd + d * 1024);
            }
            A_P2A(sA, sB, pc + 32768, true, qrow - 128 * ib - 32);
            {
                const lds_cp vp = pc + 32768 + 8192;
                A_P2B(sB, A_RD(0, vp), A_RD(1, vp + 2048), A_RD(2, vp + 4096), A_RD(3, vp + 6144), NOP_, NOP_);
#pragma unroll
                for (int j = 0; j < 4; ++j) fr[4 + j] = ld128(vp + j * 2048 + 1024);
#pragma unroll
                for (int e = 6; e < 16; ++e) sB[e] = EX2(sB[e]);
#pragma unroll
                for (int e = 0; e < 16; e += 2) { l0 += sB[e]; l1 += sB[e + 1]; }
#pragma unroll
                for (int e = 0; e < 4; ++e) { if (e >= 2) pw0[e] = cvtpk(sB[2 * e], sB[2 * e + 1]); pw1[e] = cvtpk(sB[8 + 2 * e], sB[9 + 2 * e]); }
                asm volatile("s_waitcnt vmcnt(0) lgkmcnt(0)\n\ts_barrier" ::: "memory");
                PUBLISH();
#pragma unroll
                for (int j = 0; j < 4; ++j) o[j] = MFMA32(fr[j], PWF(pw0), o[j]);
#pragma unroll
                for (int j = 0; j < 4; ++j) o[j] = MFMA32(fr[4 + j], PWF(pw1), o[j]);
            }
            {
                const int sb_own = kh == 0 ? 131072 + rg * 8192 : (rg >> 1) * 65536 + 32768 + so + (rg & 1) * 8192;
                const int sb_par = kh == 1 ? 131072 + rg * 8192 : (rg >> 1) * 65536 + 32768 + so + (rg & 1) * 8192;
                const float mown = dead ? -__builtin_inff() : mref, lown = l0 + l1;
                *(LAS f32x2*)(lds + kh * 65536 + so + 8192 + rg * 512 + lane * 8) = (f32x2){mown, lown};
                const f32x16 s0 = kh ? o[0] : o[2], s1 = kh ? o[1] : o[3], k0 = kh ? o[2] : o[0], k1 = kh ? o[3] : o[1];
#pragma unroll
                for (int qd = 0; qd < 4; ++qd) {
                    *(LAS f32x4*)(lds + sb_own + qd * 1024 + lane * 16) = (f32x4){s0[4 * qd], s0[4 * qd + 1], s0[4 * qd + 2], s0[4 * qd + 3]};
                    *(LAS f32x4*)(lds + sb_own + 4096 + qd * 1024 + lane * 16) = (f32x4){s1[4 * qd], s1[4 * qd + 1], s1[4 * qd + 2], s1[4 * qd + 3]};
                }
                asm volatile("s_waitcnt lgkmcnt(0)\n\ts_barrier" ::: "memory");
                const f32x2 ml = *(const LAS f32x2*)(lds + (kh ^ 1) * 65536 + so + 8192 + rg * 512 + lane * 8);
                f32x16 p0, p1;
#pragma unroll
                for (int qd = 0; qd < 4; ++qd) {
                    const f32x4 a = *(const LAS f32x4*)(lds + sb_par + qd * 1024 + lane * 16), b = *(const LAS f32x4*)(lds + sb_par + 4096 + qd * 1024 + lane * 16);
#pragma unroll
                    for (int e = 0; e < 4; ++e) { p0[4 * qd + e] = a[e]; p1[4 * qd + e] = b[e]; }
                }
                const float mf = __builtin_fmaxf(mown, ml[0]);
                const float fo = EX2(mown - mf), fp = EX2(ml[0] - mf);
                const float inv = 1.0f / swap_add(fo * lown + fp * ml[1]);
                const float ca = fo * inv, cb = fp * inv;
#pragma unroll
                for (int e = 0; e < 16; ++e) { p0[e] = ca * k0[e] + cb * p0[e]; p1[e] = ca * k1[e] + cb * p1[e]; }
                const unsigned dst = WS_AF + ((head * 8 + 4 * kh) * 64 + 4 * ib + rg) * 1024 + lane * 16;
                st16(rws, dst, pack8(p0, 0, 1.0f)); st16(rws, dst + 65536, pack8(p0, 1, 1.0f));
                st16(rws, dst + 2 * 65536, pack8(p1, 0, 1.0f)); st16(rws, dst + 3 * 65536, pack8(p1, 1, 1.0f));
                pf = F_C + ib * 32 + head;
            }
            so ^= 16384;
            if (blkno == 0) PROF(7);
        }
        PROF(8);
#undef A_RD
#undef A_P1
#undef A_P2A
#undef A_P2B
#undef A_P2K
#undef A_CHECK
#undef XE
#undef PWF
    }

    {
        int lane;
        asm volatile("v_mbcnt_lo_u32_b32 %0, -1, 0\n\tv_mbcnt_hi_u32_b32 %0, -1, %0" : "=v"(lane));
        const int r = lane & 31, hh = lane >> 5, blk = (lane >> 4) & 1, qq = (lane & 15) >> 2, p = lane & 3;
        const unsigned brow = 4 * wid + (lane >> 4), bchk = ((lane & 15) ^ (((lane >> 4) & 3) << 2)) << 4;
        const int sq = x8 >> 1, tm = (x8 & 1) * 8 + (kk >> 2), tn = kk & 3, rb = wid & 3, kp = wid >> 2;
        OutArgs oa;
        oa.ra = __builtin_amdgcn_make_buffer_rsrc((void*)(ws + WS_AF), (short)0, NKS * 65536, 0x00020000);
        oa.wo = (const char*)wo + (size_t)sq * 1048576 + tn * 256; oa.wu = (const char*)wout + tn * 256;
        oa.wv = brow * 1024 + bchk;
#pragma unroll
        for (int j = 0; j < 6; ++j) { const unsigned ur = 192 * sq + 32 * j + brow; oa.uv[j] = (ur > FH - 1 ? FH - 1 : ur) * 1024 + bchk; }
        oa.va = kp * 131072 + (tm * 4 + rb) * 1024 + lane * 16; oa.sc = sq * 64 * 65536; oa.su = (256 + 12 * sq) * 65536; oa.ldsw = ldsw;
#pragma unroll
        for (int c = 0; c < 4; ++c) oa.rd[c] = lds + (kp * 32 + 4 * hh + qq) * 256 + ((c ^ qq) << 6) + blk * 32 + p * 8;
        WAIT_L(0); BAR();
        out_w<0>(oa); out_w<1>(oa); out_w<2>(oa); out_w<3>(oa);
        WAIT_V(8); BAR();
        if (wid == 0 && lane == 0) set_flag(ws, pf, tag);
        if (wid == 1) {
            if (bid == 0) {
#pragma unroll 1
                for (int i = 0; i < 4; ++i) poll_flags(ws, F_S + i * 64 + lane, true, tag);
                if (lane == 0) set_flag(ws, F_E, tag);
            }
            poll_flags(ws, lane < 8 ? F_C + tm * 32 + 8 * sq + lane : F_U + (tm >> 1) * 8 + ((3 * sq) >> 1) + (lane & 1), lane < 10, tag);
        }
        out_w<4>(oa); out_w<5>(oa); out_w<6>(oa); out_w<7>(oa);
        WAIT_V(12); BAR();
        PROFO(9);
        u32x4 af[6][2];
        bf16x8 wf[2][4];
        f32x16 acc[4] = {};
        out_a<0>(oa, af); out_a<1>(oa, af); out_a<2>(oa, af); out_a<3>(oa, af); out_a<4>(oa, af);
#pragma unroll
        for (int c = 0; c < 4; ++c) wf[0][c] = ldtr2(oa.rd[c], 2048);
#define OS(t) out_step<t>(oa, af, wf, acc)
        OS(0); OS(1); OS(2); OS(3); OS(4); OS(5); OS(6); OS(7); OS(8); OS(9); OS(10); OS(11); OS(12); OS(13); OS(14); OS(15); OS(16); OS(17); OS(18);
#undef OS
        PROFO(10);
        const f32x16 s0 = kp ? acc[0] : acc[2], s1 = kp ? acc[1] : acc[3];
        f32x16 k0 = kp ? acc[2] : acc[0], k1 = kp ? acc[3] : acc[1];
#pragma unroll
        for (int g = 0; g < 4; ++g) {
            *(LAS f32x4*)(lds + 16384 + wid * 8192 + g * 1024 + lane * 16) = (f32x4){s0[4 * g], s0[4 * g + 1], s0[4 * g + 2], s0[4 * g + 3]};
            *(LAS f32x4*)(lds + 16384 + wid * 8192 + 4096 + g * 1024 + lane * 16) = (f32x4){s1[4 * g], s1[4 * g + 1], s1[4 * g + 2], s1[4 * g + 3]};
        }
        const size_t orow = (size_t)(tm * 128 + rb * 32 + r) * DM + tn * 128 + kp * 64 + 4 * hh;
        u32x2 xv[8];
        if (rb == sq) {
#pragma unroll
            for (int g = 0; g < 8; ++g) xv[g] = *(const u32x2*)(x + orow + 8 * g);
        }
        WAIT_L(0); BAR();
#pragma unroll
        for (int g = 0; g < 4; ++g) {
            const f32x4 a = *(const LAS f32x4*)(lds + 16384 + (wid ^ 4) * 8192 + g * 1024 + lane * 16), b = *(const LAS f32x4*)(lds + 16384 + (wid ^ 4) * 8192 + 4096 + g * 1024 + lane * 16);
#pragma unroll
            for (int e = 0; e < 4; ++e) { k0[4 * g + e] += a[e]; k1[4 * g + e] += b[e]; }
        }
        const unsigned pb = WS_P + ((tm * 4 + tn) * 4 + rb) * 65536 + kp * 8192 + lane * 16;
        const int fp = F_P + (tm * 4 + tn) * 4;
        if (rb != sq) {
#pragma unroll
            for (int g = 0; g < 4; ++g) {
                st16(rws, pb + sq * 16384 + g * 1024, __builtin_bit_cast(u32x4, (f32x4){k0[4 * g], k0[4 * g + 1], k0[4 * g + 2], k0[4 * g + 3]}));
                st16(rws, pb + sq * 16384 + 4096 + g * 1024, __builtin_bit_cast(u32x4, (f32x4){k1[4 * g], k1[4 * g + 1], k1[4 * g + 2], k1[4 * g + 3]}));
            }
            WAIT_V(0);
        }
        BAR();
        if (wid == 0 && lane == 0) set_flag(ws, fp + sq, tag);
        PROFO(11);
        if (rb == sq) {
            poll_flags(ws, fp + (lane & 3), (lane & 3) != sq, tag);
            PROFO(12);
            u32x4 pp[3][8];
#pragma unroll
            for (int j = 0; j < 3; ++j)
#pragma unroll
                for (int g = 0; g < 8; ++g) pp[j][g] = ld16(rws, pb + ((sq + 1 + j) & 3) * 16384 + g * 1024);
#pragma unroll
            for (int g = 0; g < 8; ++g) {
                const f32x16& k = g < 4 ? k0 : k1; const int e0 = 4 * (g & 3);
                const f32x4 a = __builtin_bit_cast(f32x4, pp[0][g]), b = __builtin_bit_cast(f32x4, pp[1][g]), c = __builtin_bit_cast(f32x4, pp[2][g]);
                const f32x4 t = ((f32x4){k[e0], k[e0 + 1], k[e0 + 2], k[e0 + 3]} + a) + (b + c);
                *(u32x2*)(out + orow + 8 * g) = (u32x2){cvtpk(bflo(xv[g][0]) + t[0], bfhi(xv[g][0]) + t[1]), cvtpk(bflo(xv[g][1]) + t[2], bfhi(xv[g][1]) + t[3])};
            }
        }
    }
    PROF_END(ws);
}

extern "C" void kernel_launch(void* const* d_in, const int* in_sizes, int n_in, void* d_out, int out_size, void* d_ws, size_t ws_size, hipStream_t stream) {
    (void)in_sizes; (void)n_in; (void)out_size; (void)ws_size;
    fused_fwd<<<256, 512, 0, stream>>>((const bf16_t*)d_in[0], (const bf16_t*)d_in[1], (const bf16_t*)d_in[2], (const bf16_t*)d_in[3], (const bf16_t*)d_in[4], (const bf16_t*)d_in[5],
                                       (const bf16_t*)d_in[7], (const bf16_t*)d_in[8], (const bf16_t*)d_in[9], (const bf16_t*)d_in[10], (bf16_t*)d_out, (unsigned char*)d_ws);
}
```

```cpp
#include <hip/hip_runtime.h>
#include <hip/hip_bf16.h>
#include <stdint.h>

#define LAS __attribute__((address_space(3)))
#define DI __device__ __forceinline__
typedef unsigned short bf16_t;
typedef unsigned long long u64;
typedef __attribute__((address_space(1))) u64 gu64;
typedef short bf16x8 __attribute__((ext_vector_type(8)));
typedef short s16x4 __attribute__((ext_vector_type(4)));
typedef __bf16 bf16x2_t __attribute__((ext_vector_type(2)));
typedef float f32x16 __attribute__((ext_vector_type(16)));
typedef float f32x4 __attribute__((ext_vector_type(4)));
typedef float f32x2 __attribute__((ext_vector_type(2)));
typedef unsigned u32x4 __attribute__((ext_vector_type(4)));
typedef unsigned u32x2 __attribute__((ext_vector_type(2)));
typedef LAS unsigned char* lds_p;
typedef const LAS unsigned char* lds_cp;
typedef __amdgpu_buffer_rsrc_t rsrc_t;

constexpr int SEQ = 2048, DM = 512, NH = 32, FH = 688, FF = 1376;
constexpr int NKS = 299;
constexpr int F_E = 0;
constexpr int F_S = 512;
constexpr int F_X = 1024;
constexpr int F_L = 2048;
constexpr int F_U = 3072;
constexpr int F_K = 3584;
constexpr int F_C = 4096;
constexpr int F_P = 5120;
constexpr unsigned WS_G   = 65536;
constexpr unsigned WS_XN  = 131072;
constexpr unsigned WS_QF  = WS_XN + SEQ * DM * 2;
constexpr unsigned WS_KVF = WS_QF + NH * 64 * 8192;
constexpr unsigned WS_Z2N = WS_KVF + NH * 64 * 16384;
constexpr unsigned WS_AF  = WS_Z2N + SEQ * 768 * 2;
constexpr unsigned WS_P   = WS_AF + 304 * 64 * 1024;
constexpr unsigned WS_END = WS_P + 256 * 65536;

#define PROF_DECL
#define PROF(i)
#define PROF_END(wsp)
#define MFMA32(a, b, c) __builtin_amdgcn_mfma_f32_32x32x16_bf16((a), (b), (c), 0, 0, 0)
#define SBAR() __builtin_amdgcn_sched_barrier(0)
#define PIN(x) asm volatile("" : "+v"(x))
#define WAIT_V(n) asm volatile("s_waitcnt vmcnt(" #n ")" ::: "memory")
#define WAIT_L(n) asm volatile("s_waitcnt lgkmcnt(" #n ")" ::: "memory")
#define BAR() asm volatile("s_barrier" ::: "memory")
#define SC1 16

DI unsigned cvtpk(float lo, float hi) { f32x2 v = {lo, hi}; bf16x2_t b = __builtin_convertvector(v, bf16x2_t); return __builtin_bit_cast(unsigned, b); }
DI float bflo(unsigned w) { return __uint_as_float(w << 16); }
DI float bfhi(unsigned w) { return __uint_as_float(w & 0xffff0000u); }
DI u32x4 pack8(const f32x16& a, int s, float sc) {
    u32x4 w;
    w[0] = cvtpk(a[8 * s + 0] * sc, a[8 * s + 1] * sc); w[1] = cvtpk(a[8 * s + 2] * sc, a[8 * s + 3] * sc);
    w[2] = cvtpk(a[8 * s + 4] * sc, a[8 * s + 5] * sc); w[3] = cvtpk(a[8 * s + 6] * sc, a[8 * s + 7] * sc);
    return w;
}
DI bf16x8 ld128(lds_cp p) { return *(const LAS bf16x8*)p; }
typedef short v4i16_t __attribute__((ext_vector_type(4)));
DI s16x4 ldtr(lds_cp p) { return __builtin_bit_cast(s16x4, __builtin_amdgcn_ds_read_tr16_b64_v4i16((LAS v4i16_t*)p)); }
DI bf16x8 ldtr2(lds_cp p, int step) { const s16x4 lo = ldtr(p), hi = ldtr(p + step); return __builtin_shufflevector(lo, hi, 0, 1, 2, 3, 4, 5, 6, 7); }
template <int I0, int I1> DI void dma_pair(unsigned v0, unsigned v1, const void* sbase, unsigned l) {
    asm volatile("s_add_u32 m0, %3, %4\n\ts_nop 0\n\tglobal_load_lds_dwordx4 %0, %2\n\ts_add_u32 m0, %3, %5\n\ts_nop 0\n\tglobal_load_lds_dwordx4 %1, %2"
                 :: "v"(v0), "v"(v1), "s"(sbase), "s"(l), "i"(I0), "i"(I1) : "memory", "scc");
}
template <int I, int O> DI void bdma1(unsigned voff, rsrc_t rs, unsigned soff, unsigned l) {
    asm volatile("s_add_u32 m0, %3, %4\n\ts_nop 0\n\tbuffer_load_dwordx4 %0, %1, %2 offen offset:%5 lds" :: "v"(voff), "s"(rs), "s"(soff), "s"(l), "i"(I), "i"(O) : "memory", "scc");
}
DI void dma_words(unsigned voff, const void* sbase, unsigned l) {
    asm volatile("s_mov_b32 m0, %2\n\ts_nop 0\n\tglobal_load_lds_dword %0, %1 sc1" :: "v"(voff), "s"(sbase), "s"(l) : "memory");
}
DI void st16(rsrc_t rs, unsigned off, u32x4 v) { __builtin_amdgcn_raw_buffer_store_b128(v, rs, off, 0, SC1); }
DI u32x4 ld16(rsrc_t rs, unsigned off) { return __builtin_amdgcn_raw_buffer_load_b128(rs, off, 0, SC1); }
DI float swap_add(float v) { auto r = __builtin_amdgcn_permlane32_swap(__float_as_uint(v), __float_as_uint(v), false, false); return __uint_as_float(r[0]) + __uint_as_float(r[1]); }
DI float swap_max(float v) { auto r = __builtin_amdgcn_permlane32_swap(__float_as_uint(v), __float_as_uint(v), false, false); return __builtin_fmaxf(__uint_as_float(r[0]), __uint_as_float(r[1])); }
DI f32x2 unpk(unsigned w) { return (f32x2){bflo(w), bfhi(w)}; }
DI unsigned cvt2(f32x2 v) { return __builtin_bit_cast(unsigned, __builtin_convertvector(v, bf16x2_t)); }
DI float sum_pair(unsigned w, float acc) { return __builtin_amdgcn_fdot2_f32_bf16(__builtin_bit_cast(bf16x2_t, w), __builtin_bit_cast(bf16x2_t, 0x3f803f80u), acc, false); }
DI unsigned gelu2(float a, float b) {
    const f32x2 v = {a, b};
    const f32x2 t = v * __builtin_elementwise_fma(v * v, (f32x2){-2.302208198f * 0.044715f, -2.302208198f * 0.044715f}, (f32x2){-2.302208198f, -2.302208198f});
    const f32x2 d = (f32x2){__builtin_amdgcn_exp2f(t[0]), __builtin_amdgcn_exp2f(t[1])} + (f32x2){1.0f, 1.0f};
    return cvt2(v * (f32x2){__builtin_amdgcn_rcpf(d[0]), __builtin_amdgcn_rcpf(d[1])});
}
DI u32x4 gelu8(const f32x16& a, int s) { return (u32x4){gelu2(a[8 * s], a[8 * s + 1]), gelu2(a[8 * s + 2], a[8 * s + 3]), gelu2(a[8 * s + 4], a[8 * s + 5]), gelu2(a[8 * s + 6], a[8 * s + 7])}; }
DI void set_flag(unsigned char* ws, int idx, u64 tag) { __hip_atomic_store((gu64*)ws + idx, tag, __ATOMIC_RELAXED, __HIP_MEMORY_SCOPE_AGENT); }
DI void poll_flags(unsigned char* ws, int idx, bool active, u64 tag) {
#pragma unroll 1
    for (unsigned spins = 0; spins < (1u << 17); ++spins) {
        const u64 v = __hip_atomic_load((gu64*)ws + idx, __ATOMIC_RELAXED, __HIP_MEMORY_SCOPE_AGENT);
        if (__all(!active || v == tag)) break;
        __builtin_amdgcn_s_sleep(4);
    }
}
#define ACQUIRE() asm volatile("buffer_inv sc1" ::: "memory")

constexpr int LDS_TAG = 163584, LDS_TOUCH = 163328, LDS_PARK = 163072;
DI void touch(unsigned voff, const void* sbase, unsigned lds0) { dma_words(voff, sbase, lds0 + LDS_TOUCH); }
#define TOUCH_A(v, s, l) touch(v, s, l)
#define TOUCH_S(v, s, l) touch(v, s, l)
#define TOUCH_Z(v, s, l) touch(v, s, l)
template <int I> DI void dma_one(unsigned v0, const void* sbase, unsigned l) {
    asm volatile("s_add_u32 m0, %2, %3\n\ts_nop 0\n\tglobal_load_lds_dwordx4 %0, %1" :: "v"(v0), "s"(sbase), "s"(l), "i"(I) : "memory", "scc");
}
DI void z2_half(const lds_p lds, const unsigned lds0, f32x16 (&acc)[3], const bf16_t* __restrict__ x, const bf16_t* __restrict__ w_in, const unsigned char* fx, const int rg, const int hf,
                const int tid, const int wid, const int lane
#define GP(i)
                ) {
    constexpr int ZS = 704, ZST = 32 * ZS;
    constexpr unsigned WROW = FF * 2;
    const int row = tid >> 3, seg = tid & 7;
    const u32x4* src = (const u32x4*)(x + (size_t)(rg * 64 + row) * DM) + seg;
    u32x4 v[8];
#pragma unroll
    for (int j = 0; j < 8; ++j) asm volatile("global_load_dwordx4 %0, %1, off offset:%2" : "=v"(v[j]) : "v"(src), "i"(j * 128) : "memory");
    unsigned bv[3];
    const int p2 = wid < 6 ? wid + 16 : wid + 8;
#pragma unroll
    for (int i = 0; i < 3; ++i) { const int c = (i == 2 ? p2 : wid + 8 * i) * 64 + lane, zr = c / 44, zc = c - zr * 44; bv[i] = zr * WROW + (zc > 42 ? 42 : zc) * 16; }
    const unsigned lw = lds0 + 65536 + wid * 1024, lw2 = lds0 + 65536 + p2 * 1024;
    const char* wg = (const char*)w_in + FH * 2 + hf * FH;
#define Z_STAGE(slot, g_) do { dma_pair<(slot) * ZST, (slot) * ZST + 8192>(bv[0], bv[1], (g_), lw); dma_one<(slot) * ZST>(bv[2], (g_), lw2); } while (0)
    BAR();
    Z_STAGE(0, wg); Z_STAGE(1, wg + 32 * WROW); Z_STAGE(2, wg + 64 * WROW); Z_STAGE(3, wg + 96 * WROW);
#pragma unroll
    for (int i = 0; i < 2; ++i) { int c = (2 * wid + i) * 64 + lane; c = c > 895 ? 895 : c; const int zr = c / 7, zc = c - zr * 7; TOUCH_Z(((rg & 3) * 128 + zr) * WROW + (zc == 6 ? 684 : zc * 128), wg, lds0); }
    SBAR();
    GP(5);
    {
        asm volatile("s_waitcnt vmcnt(14)" : "+v"(v[0]), "+v"(v[1]), "+v"(v[2]), "+v"(v[3]), "+v"(v[4]), "+v"(v[5]), "+v"(v[6]), "+v"(v[7]));
        GP(6);
        float s = 0.f, s1 = 0.f;
#pragma unroll
        for (int j = 0; j < 8; ++j) { s = sum_pair(v[j][0], s); s1 = sum_pair(v[j][1], s1); s = sum_pair(v[j][2], s); s1 = sum_pair(v[j][3], s1); }
        s += s1;
        s += __shfl_xor(s, 1); s += __shfl_xor(s, 2); s += __shfl_xor(s, 4);
        const float mean = s * (1.0f / DM);
        f32x2 d[32], q2 = {0.f, 0.f}, q3 = {0.f, 0.f};
#pragma unroll
        for (int i = 0; i < 32; i += 2) { d[i] = unpk(v[i >> 2][i & 3]) - (f32x2){mean, mean}; d[i + 1] = unpk(v[i >> 2][(i & 3) + 1]) - (f32x2){mean, mean};
            q2 = __builtin_elementwise_fma(d[i], d[i], q2); q3 = __builtin_elementwise_fma(d[i + 1], d[i + 1], q3); }
        q2 += q3;
        float q = q2[0] + q2[1];
        q += __shfl_xor(q, 1); q += __shfl_xor(q, 2); q += __shfl_xor(q, 4);
        const float rstd = 1.0f / sqrtf(q * (1.0f / DM) + 1e-5f);
#pragma unroll
        for (int j = 0; j < 8; ++j) {
            u32x4 o;
#pragma unroll
            for (int e = 0; e < 4; ++e) o[e] = cvt2(d[4 * j + e] * (f32x2){rstd, rstd});
            *(LAS u32x4*)(lds + j * 8192 + row * 128 + ((seg ^ ((row >> 1) & 7)) << 4)) = o;
        }
    }
    const int r = lane & 31, hh = lane >> 5, blk = (lane >> 4) & 1, qq = (lane & 15) >> 2, p = lane & 3;
    const int asw = (r >> 1) & 7, oth = (wid & 1) ? -4096 : 4096;
    lds_cp ab[4];
#pragma unroll
    for (int k = 0; k < 4; ++k) ab[k] = lds + (wid & 1) * 4096 + r * 128 + (((2 * k + hh) ^ asw) << 4);
    const lds_cp bb = lds + 65536 + (8 * hh + qq) * ZS + wid * 64 + blk * 32 + p * 8;
    const lds_cp bx = bb + (8 + (wid >> 1) - wid) * 64;
    const bool w3 = wid < 6;
#pragma unroll
    for (int i = 0; i < 3; ++i) acc[i] = f32x16{};
    bf16x8 fa[2][2][2], fb[2][2][2];
#define Z_READ(set, slot, k0, aoff_) do { _Pragma("unroll") for (int k_ = 0; k_ < 2; ++k_) { \
        fa[set][k_][0] = ld128(ab[(k0) + k_] + (aoff_)); fa[set][k_][1] = ld128(ab[(k0) + k_] + oth + (aoff_)); \
        fb[set][k_][0] = ldtr2(bb + (slot) * ZST + k_ * 16 * ZS, 4 * ZS); if (w3) fb[set][k_][1] = ldtr2(bx + (slot) * ZST + k_ * 16 * ZS, 4 * ZS); } } while (0)
#define Z_MMA(set, k_) do { acc[0] = MFMA32(fb[set][k_][0], fa[set][k_][0], acc[0]); acc[1] = MFMA32(fb[set][k_][0], fa[set][k_][1], acc[1]); \
        if (w3) acc[2] = MFMA32(fb[set][k_][1], fa[set][k_][0], acc[2]); } while (0)
#define Z_SYNC(n) asm volatile("s_waitcnt vmcnt(" #n ") lgkmcnt(0)\n\ts_barrier" ::: "memory")
    GP(7);
    Z_SYNC(11);
    GP(8);
    Z_READ(0, 0, 0, 0);
    const char* g = wg + 128 * WROW;
#pragma unroll 1
    for (int kt = 0; kt < 3; ++kt) {
        Z_SYNC(6); Z_READ(1, 1, 2, 0);     SBAR(); Z_MMA(0, 0); SBAR(); Z_STAGE(0, g);             SBAR(); Z_MMA(0, 1); SBAR();
        Z_SYNC(6); Z_READ(0, 2, 0, 8192);  SBAR(); Z_MMA(1, 0); SBAR(); Z_STAGE(1, g + 32 * WROW); SBAR(); Z_MMA(1, 1); SBAR();
        Z_SYNC(6); Z_READ(1, 3, 2, 8192);  SBAR(); Z_MMA(0, 0); SBAR(); Z_STAGE(2, g + 64 * WROW); SBAR(); Z_MMA(0, 1); SBAR();
        Z_SYNC(6); Z_READ(0, 0, 0, 16384); SBAR(); Z_MMA(1, 0); SBAR(); Z_STAGE(3, g + 96 * WROW); SBAR(); Z_MMA(1, 1); SBAR();
        g += 128 * WROW;
#pragma unroll
        for (int k = 0; k < 4; ++k) ab[k] += 16384;
    }
    if (wid == 0) dma_words(lane * 4, fx, lds0 + LDS_PARK);
    Z_SYNC(6); Z_READ(1, 1, 2, 0);    SBAR(); Z_MMA(0, 0); Z_MMA(0, 1); SBAR();
    Z_SYNC(3); Z_READ(0, 2, 0, 8192); SBAR(); Z_MMA(1, 0); Z_MMA(1, 1); SBAR();
    Z_SYNC(0); Z_READ(1, 3, 2, 8192); SBAR(); Z_MMA(0, 0); Z_MMA(0, 1); SBAR();
    Z_SYNC(0);                                Z_MMA(1, 0); Z_MMA(1, 1);
#undef Z_STAGE
#undef Z_READ
#undef Z_MMA
#undef Z_SYNC
}

struct Run { const char* A; const char* B; unsigned dA1, dA2, dB1, dB2, vA0, vA1, vB0, vB1; };
constexpr int HTB = 16384;
constexpr unsigned LDA = DM * 2, LDB = 4096 * 2;
#define G_SA(b, h) (((b) * 2 + (h)) * HTB)
#define G_SB(b, h) ((4 + (b) * 2 + (h)) * HTB)
#define G_STAGE(bufoff, gb, v0, v1) dma_pair<(bufoff), (bufoff) + 8192>((v0), (v1), (gb), ldsw)
#define G_LDA(dst, b, h) do { _Pragma("unroll") for (int m_ = 0; m_ < 2; ++m_) _Pragma("unroll") for (int k_ = 0; k_ < 4; ++k_) dst[m_][k_] = ld128(lds + G_SA(b, h) + aoff[k_] + m_ * 4096); } while (0)
#define G_LDB(dst, b, h) do { _Pragma("unroll") for (int k_ = 0; k_ < 4; ++k_) dst[k_] = ldtr2(lds + G_SB(b, h) + boff + k_ * 4096, 1024); } while (0)
#define G_MMA(ai, bj, At, Bt) do { __builtin_amdgcn_s_setprio(1); _Pragma("unroll") for (int k_ = 0; k_ < 4; ++k_) _Pragma("unroll") for (int m_ = 0; m_ < 2; ++m_) \
        acc[ai][bj][m_] = SWAP ? MFMA32(At[m_][k_], Bt[k_], acc[ai][bj][m_]) : MFMA32(Bt[k_], At[m_][k_], acc[ai][bj][m_]); __builtin_amdgcn_s_setprio(0); } while (0)

DI void g_prologue_b(const Run& u, const unsigned ldsw) {
    G_STAGE(G_SB(0, 0), u.B, u.vB0, u.vB1); G_STAGE(G_SB(0, 1), u.B + u.dB1, u.vB0, u.vB1);
}
DI void g_prologue_rest(const Run& u, const unsigned ldsw) {
    G_STAGE(G_SA(0, 0), u.A, u.vA0, u.vA1); G_STAGE(G_SA(0, 1), u.A + u.dA1, u.vA0, u.vA1);
    G_STAGE(G_SB(1, 0), u.B + u.dB2, u.vB0, u.vB1); G_STAGE(G_SA(1, 0), u.A + u.dA2, u.vA0, u.vA1); G_STAGE(G_SB(1, 1), u.B + u.dB2 + u.dB1, u.vB0, u.vB1);
}
template <int LO> DI void check_parked(const int HI, lds_cp parked, unsigned char* ws, const int fidx, const u64 tag, const int lane) {
    const unsigned w = *(const LAS unsigned*)(parked + lane * 4);
    const int sl = (lane >> 1) & 15;
    if (!__all(sl < LO || sl >= HI || w == ((lane & 1) ? (unsigned)(tag >> 32) : (unsigned)tag))) poll_flags(ws, fidx + (lane & 31), lane < 32 && (lane & 15) >= LO && (lane & 15) < HI, tag);
}

template <bool SWAP, int ROUND>
DI void g_kloop(const lds_p lds, const unsigned lds0, f32x16 (&acc)[2][2][2], const Run& cur, const Run& nxt, const int (&aoff)[4], const int boff, const unsigned ldsw,
                unsigned char* ws, const u64 tag, const int fl0, const int fatt, const int lane, const int wid, const unsigned tch) {
    bf16x8 At[2][4], B0[4], B1[4];
#pragma unroll
    for (int i = 0; i < 8; ++i) acc[i >> 2][(i >> 1) & 1][i & 1] = f32x16{};
#pragma unroll 1
    for (int t = 0; t < 8; t += 2) {
        const bool last = (t == 6);
        const char* a1 = cur.A + (t + 1) * 128 + 128 * LDA;
        const char* a2 = last ? nxt.A : cur.A + (t + 2) * 128;
        const char* b2 = last ? nxt.B : cur.B + (size_t)(t + 2) * 64 * LDB;
        const unsigned dA1 = last ? nxt.dA1 : cur.dA1, dA2 = last ? nxt.dA2 : cur.dA2, dB1 = last ? nxt.dB1 : cur.dB1, dB2 = last ? nxt.dB2 : cur.dB2;
        const unsigned vA0 = last ? nxt.vA0 : cur.vA0, vA1 = last ? nxt.vA1 : cur.vA1, vB0 = last ? nxt.vB0 : cur.vB0, vB1 = last ? nxt.vB1 : cur.vB1;
        if (ROUND == 1 && last && wid == 0) dma_words(lane * 4, ws + (size_t)fatt * 8, lds0 + 131072);
        if (t == 2 && wid == 0 && lane == 0) set_flag(ws, fl0, tag);
        if (t == 0) G_LDB(B0, 0, 0);
        SBAR(); G_LDA(At, 0, 0); if (!(ROUND == 1 && t == 0)) G_STAGE(G_SA(1, 1), a1, cur.vA0, cur.vA1);
        if (ROUND == 0 && t == 2) touch(tch, nxt.A, lds0);
        if (ROUND == 1 && t == 4 && wid == 0) { check_parked<0>(8, lds + 131072, ws, fatt, tag, lane); ACQUIRE(); }
        WAIT_L(8); BAR(); WAIT_L(0); SBAR(); G_MMA(0, 0, At, B0); SBAR(); BAR();
        G_LDB(B1, 0, 1);
        G_STAGE(G_SB(0, 0), b2, vB0, vB1);
        BAR(); WAIT_L(0); SBAR(); G_MMA(0, 1, At, B1); SBAR(); BAR();
        G_LDA(At, 0, 1); G_STAGE(G_SA(0, 0), a2, vA0, vA1);
        if (ROUND == 1 && t == 0) WAIT_V(26); else WAIT_V(10);
        BAR(); WAIT_L(0); SBAR(); G_MMA(1, 0, At, B0); SBAR(); BAR();
        G_LDB(B0, 1, 0); G_STAGE(G_SB(0, 1), b2 + dB1, vB0, vB1);
        if (ROUND == 1 && t == 0) WAIT_V(22); else if ((ROUND == 1 && t == 4 && wid == 0) || (ROUND == 0 && t == 2)) WAIT_V(7); else WAIT_V(6);
        BAR(); SBAR(); G_MMA(1, 1, At, B1); SBAR(); BAR();
        if (ROUND == 1 && wid == 0) {
            if (t == 2) dma_words(lane * 4, ws + (size_t)fatt * 8, lds0 + 131072);
            if (last) check_parked<8>(12, lds + 131072, ws, fatt, tag, lane);
        }
        G_LDA(At, 1, 0); G_STAGE(G_SA(0, 1), a2 + dA1, vA0, vA1);
        BAR(); WAIT_L(0); SBAR(); G_MMA(0, 0, At, B0); SBAR(); BAR();
        G_LDB(B1, 1, 1); G_STAGE(G_SB(1, 0), b2 + dB2, vB0, vB1);
        BAR(); WAIT_L(0); SBAR(); G_MMA(0, 1, At, B1); SBAR(); BAR();
        G_LDA(At, 1, 1); G_STAGE(G_SA(1, 0), a2 + dA2, vA0, vA1);
        if (!last) WAIT_V(10);
        BAR(); WAIT_L(0); SBAR(); G_MMA(1, 0, At, B0); SBAR(); BAR();
        if (!last) G_LDB(B0, 0, 0);
        G_STAGE(G_SB(1, 1), b2 + dB2 + dB1, vB0, vB1);
        if (!(ROUND == 1 && last)) WAIT_V(6);
        BAR(); SBAR(); G_MMA(1, 1, At, B1); SBAR();
        if (!(last && wid >= 4)) BAR();
    }
}

DI void g_epilogue(const f32x16 (&acc)[2][2][2], const rsrc_t rws, const int type, const int pn, const int pm, const int wr, const int wc, const int lane) {
    constexpr float QS = 0.08838834764831845f * 1.4426950408889634f;
    const float sc = type == 2 ? QS : 1.0f;
    const unsigned base = (type == 2 ? WS_QF : WS_KVF + (type == 1 ? 8192 : 0)) + wc * 2048 + lane * 16;
    const unsigned hstride = type == 2 ? 8192 : 16384;
#pragma unroll
    for (int ai = 0; ai < 2; ++ai)
#pragma unroll
        for (int bj = 0; bj < 2; ++bj)
#pragma unroll
            for (int m = 0; m < 2; ++m) {
                const unsigned dst = base + (unsigned)((2 * pn + bj) * 64 + pm * 8 + ai * 4 + wr * 2 + m) * hstride;
                st16(rws, dst, pack8(acc[ai][bj][m], 0, sc)); st16(rws, dst + 1024, pack8(acc[ai][bj][m], 1, sc));
            }
}

#define NOP_ (void)0
#define SP_CHECK() if (t == 36) { poll_flags(ws, fatt + lane, (lane & 15) < 12, tag); ACQUIRE(); }
#define EX2(v) __builtin_amdgcn_exp2f(v)
#define MX3(a, b, c) __builtin_fmaxf(__builtin_fmaxf((a), (b)), (c))
constexpr float ATT_THR = 32.0f;
DI void att_mask(f32x16& s, int dq) {
#pragma unroll
    for (int e = 0; e < 16; ++e) if ((e & 3) + 8 * (e >> 2) > dq) s[e] = -__builtin_inff();
}

struct OutArgs { rsrc_t ra; const char* wo; const char* wu; lds_cp rd[4]; unsigned wv, uv[6], va, sc, su, ldsw; };
template <int T> DI void out_w(const OutArgs& o) {
    constexpr int S = (T % 9) * 16384;
    if constexpr (T < 16) dma_pair<S, S + 8192>(o.wv, o.wv + 32768, o.wo + T * 65536, o.ldsw);
    else if constexpr (T < 19) dma_pair<S, S + 8192>(o.uv[2 * (T - 16)], o.uv[2 * (T - 16) + 1], o.wu, o.ldsw);
}
template <int T> DI void out_a(const OutArgs& o, u32x4 (&af)[6][2]) {
    if constexpr (T < 19) {
        const unsigned so = T < 16 ? o.sc + T * 262144 : o.su + (T - 16) * 262144;
        asm volatile("buffer_load_dwordx4 %0, %1, %2, %3 offen sc1" : "=v"(af[T % 6][0]) : "v"(o.va), "s"(o.ra), "s"(so) : "memory");
        asm volatile("buffer_load_dwordx4 %0, %1, %2, %3 offen sc1" : "=v"(af[T % 6][1]) : "v"(o.va), "s"(o.ra), "s"(so + 65536) : "memory");
    }
}
constexpr int out_cnt(int i) { return (i + 7 < 19 ? 2 : 0) + (i + 5 < 19 ? 2 : 0); }
constexpr int out_allow(int t) { int n = t < 4 ? 2 * (4 - t) : 0; for (int i = t < 4 ? 0 : t - 4; i < t; ++i) n += out_cnt(i); return n; }
template <int T> DI void out_step(const OutArgs& o, u32x4 (&af)[6][2], bf16x8 (&wf)[2][4], f32x16 (&acc)[4]) {
    asm volatile("s_waitcnt vmcnt(%2)" : "+v"(af[T % 6][0]), "+v"(af[T % 6][1]) : "n"(out_allow(T)) : "memory");
    if constexpr (T % 2 == 0) BAR();
    const bf16x8 a0 = __builtin_bit_cast(bf16x8, af[T % 6][0]), a1 = __builtin_bit_cast(bf16x8, af[T % 6][1]);
#pragma unroll
    for (int c = 0; c < 4; ++c) wf[1][c] = ldtr2(o.rd[c] + (T % 9) * 16384 + 4096, 2048);
    SBAR();
    acc[0] = MFMA32(wf[0][0], a0, acc[0]); acc[1] = MFMA32(wf[0][1], a0, acc[1]); SBAR();
    out_w<T + 7>(o); SBAR();
    acc[2] = MFMA32(wf[0][2], a0, acc[2]); acc[3] = MFMA32(wf[0][3], a0, acc[3]); SBAR();
    if constexpr (T < 18) {
#pragma unroll
        for (int c = 0; c < 4; ++c) wf[0][c] = ldtr2(o.rd[c] + ((T + 1) % 9) * 16384, 2048);
    }
    SBAR();
    acc[0] = MFMA32(wf[1][0], a1, acc[0]); acc[1] = MFMA32(wf[1][1], a1, acc[1]); SBAR();
    out_a<T + 5>(o, af); SBAR();
    acc[2] = MFMA32(wf[1][2], a1, acc[2]); acc[3] = MFMA32(wf[1][3], a1, acc[3]); SBAR();
}

#define IT(h1, i1, h2, i2) ((h1) | (i1) << 2 | (h2) << 6 | (i2) << 8)
__device__ constexpr unsigned ITEMS[32] = {
    IT(0, 15, 0, 4), IT(1, 15, 1, 4), IT(2, 15, 2, 4), IT(3, 15, 3, 4), IT(0, 13, 0, 5), IT(1, 13, 1, 5), IT(2, 13, 2, 5), IT(3, 13, 3, 5),
    IT(0, 12, 0, 6), IT(1, 12, 1, 6), IT(2, 12, 2, 6), IT(3, 12, 3, 6), IT(0, 11, 0, 7), IT(1, 11, 1, 7), IT(2, 11, 2, 7), IT(3, 11, 3, 7),
    IT(0, 14, 0, 3), IT(1, 14, 1, 3), IT(2, 14, 2, 3), IT(3, 14, 3, 3), IT(0, 9, 0, 8), IT(1, 9, 1, 8), IT(2, 9, 2, 8), IT(3, 9, 3, 8),
    IT(0, 2, 0, 0), IT(1, 2, 1, 0), IT(2, 2, 2, 0), IT(3, 2, 3, 0), IT(0, 1, 1, 1), IT(2, 1, 3, 1), IT(0, 10, 1, 10), IT(2, 10, 3, 10)
};

__global__ __launch_bounds__(512, 2) void fused_fwd(const bf16_t* __restrict__ x, const bf16_t* __restrict__ ckv, const bf16_t* __restrict__ wq, const bf16_t* __restrict__ wk,
                                                    const bf16_t* __restrict__ wv, const bf16_t* __restrict__ wo, const bf16_t* __restrict__ w_in, const bf16_t* __restrict__ wsp,
                                                    const bf16_t* __restrict__ bsp, const bf16_t* __restrict__ wout, bf16_t* __restrict__ out, unsigned char* ws) {
    __shared__ __attribute__((aligned(16))) unsigned char smem[163840];
    const lds_p lds = (lds_p)smem;
    const unsigned lds0 = (unsigned)(uintptr_t)smem;
    const int tid = threadIdx.x, wid = __builtin_amdgcn_readfirstlane(tid >> 6), lane = tid & 63;
    const int bid = blockIdx.x, x8 = bid & 7, kk = bid >> 3;
    const int r = lane & 31, hh = lane >> 5, blk = (lane >> 4) & 1, qq = (lane & 15) >> 2, p = lane & 3;
    const rsrc_t rws = __builtin_amdgcn_make_buffer_rsrc((void*)ws, (short)0, WS_END, 0x00020000);
    const unsigned ldsw = lds0 + (unsigned)wid * 1024u;
    PROF_DECL;
    dma_words(lane * 4, ws, lds0 + LDS_TAG);
    u64 tag;
#define MAKE_TAG() do { const u32x2 e_ = *(const LAS u32x2*)(lds + LDS_TAG); \
        const u64 ep_ = ((u64)(unsigned)__builtin_amdgcn_readfirstlane(e_[1]) << 32) | (unsigned)__builtin_amdgcn_readfirstlane(e_[0]); \
        u64 z_ = ep_ + 0x9E3779B97F4A7C15ull; z_ = (z_ ^ (z_ >> 30)) * 0xBF58476D1CE4E5B9ull; z_ = (z_ ^ (z_ >> 27)) * 0x94D049BB133111EBull; z_ ^= z_ >> 31; tag = z_ == ep_ ? ~z_ : z_; } while (0)
    const unsigned item = ITEMS[kk];
    const int h1 = item & 3, i1 = (item >> 2) & 15, h2 = (item >> 6) & 3, i2 = (item >> 8) & 15;
    const unsigned char* kvx = ws + WS_KVF + (size_t)x8 * 4 * 64 * 16384;
    const unsigned char* kvh = kvx + (size_t)h1 * 64 * 16384;
    const char* q1src = (const char*)ws + WS_QF + (size_t)((4 * x8 + h1) * 64 + 4 * i1 + (wid & 3)) * 8192;
    bf16x8 q[8];
#define Q1_LOAD() do { _Pragma("unroll") for (int d = 0; d < 8; ++d) asm volatile("global_load_dwordx4 %0, %1, %2 offset:%3 sc1" : "=v"(q[d]) : "v"(lane * 16 + (d >> 2) * 4096), "s"(q1src), "i"((d & 3) * 1024) : "memory"); } while (0)
#define Q1_WAIT(n) asm volatile("s_waitcnt vmcnt(" #n ")" : "+v"(q[0]), "+v"(q[1]), "+v"(q[2]), "+v"(q[3]), "+v"(q[4]), "+v"(q[5]), "+v"(q[6]), "+v"(q[7]) :: "memory")
    const int fatt = F_K + x8 * 64;
#define Q2_LOAD() do { const char* q2src = (const char*)ws + WS_QF + (size_t)((4 * x8 + h2) * 64 + 4 * i2) * 8192 + wid * 4096; \
        const unsigned lq_ = __builtin_amdgcn_readfirstlane(lds0 + wid * 4096); \
        dma_pair<131072, 131072 + 1024>(lane * 16, lane * 16 + 1024, q2src, lq_); dma_pair<131072 + 2048, 131072 + 3072>(lane * 16 + 2048, lane * 16 + 3072, q2src, lq_); } while (0)
    Run rat;
    rat.A = (const char*)kvh; rat.B = (const char*)kvh + 32768; rat.dA1 = rat.dB1 = 65536; rat.dA2 = rat.dB2 = 8192;
    rat.vA0 = rat.vB0 = wid * 1024 + lane * 16; rat.vA1 = rat.vB1 = rat.vA0 + 16384;
    int pf;
    int aoff[4];
    const unsigned arow = 8 * wid + (lane >> 3), achk = ((lane & 7) ^ (((wid & 1) << 2) + (lane >> 4))) << 4;
    const unsigned brow = 4 * wid + (lane >> 4), bchk = ((lane & 15) ^ (((lane >> 4) & 3) << 2)) << 4;

    if (kk < 24) {
        const int type = kk >> 3, pn = 2 * x8 + ((kk >> 2) & 1), pm0 = (kk & 3) + (type == 2 ? 4 : 0), pm1 = pm0 ^ 4, wr = wid >> 2, wc = wid & 3;
        Run u0, u1;
        const char* Ab = type == 2 ? (const char*)ws + WS_XN : (const char*)ckv;
        u0.B = (const char*)(type == 0 ? wk : type == 1 ? wv : wq) + pn * 512; u0.A = Ab + (size_t)pm0 * 256 * LDA;
        u0.dA1 = 128 * LDA; u0.dA2 = 128; u0.dB1 = 256; u0.dB2 = 64 * LDB;
        u0.vA0 = arow * LDA + achk; u0.vA1 = u0.vA0 + 64 * LDA; u0.vB0 = brow * LDB + bchk; u0.vB1 = u0.vB0 + 32 * LDB;
        u1 = u0; u1.A = Ab + (size_t)pm1 * 256 * LDA;
        const unsigned tch = (type == 2 ? ((kk >> 2) & 1) * 2 : type * 2 + ((kk >> 2) & 1)) * 65536 + tid * 128, bl = ((kk & 3) * 128 + wid * 16 + (lane >> 2)) * LDB + (lane & 3) * 128;
        if (type < 2) {
            g_prologue_b(u0, ldsw); g_prologue_rest(u0, ldsw);
            PROF(1);
            WAIT_V(6);
            MAKE_TAG();
        } else {
            const int j = x8 * 8 + (kk - 16), row = 32 * j + 4 * wid + (lane >> 4);
            const u32x4* src = (const u32x4*)(x + (size_t)row * DM) + (lane & 15);
            u32x4 v[4];
#pragma unroll
            for (int i = 0; i < 4; ++i) v[i] = src[16 * i];
            SBAR();
            g_prologue_b(u0, ldsw); touch(bl, u0.B, lds0);
            SBAR();
            float s = 0.f;
#pragma unroll
            for (int i = 0; i < 4; ++i)
#pragma unroll
                for (int e = 0; e < 4; ++e) s += bflo(v[i][e]) + bfhi(v[i][e]);
            s += __shfl_xor(s, 1); s += __shfl_xor(s, 2); s += __shfl_xor(s, 4); s += __shfl_xor(s, 8);
            const float mean = s * (1.0f / DM);
            float q = 0.f;
#pragma unroll
            for (int i = 0; i < 4; ++i)
#pragma unroll
                for (int e = 0; e < 4; ++e) { const float a = bflo(v[i][e]) - mean, b = bfhi(v[i][e]) - mean; q += a * a + b * b; }
            q += __shfl_xor(q, 1); q += __shfl_xor(q, 2); q += __shfl_xor(q, 4); q += __shfl_xor(q, 8);
            const float rstd = 1.0f / sqrtf(q * (1.0f / DM) + 1e-5f);
#pragma unroll
            for (int i = 0; i < 4; ++i) {
                u32x4 o;
#pragma unroll
                for (int e = 0; e < 4; ++e) o[e] = cvtpk((bflo(v[i][e]) - mean) * rstd, (bfhi(v[i][e]) - mean) * rstd);
                st16(rws, WS_XN + row * 1024 + ((lane & 15) + 16 * i) * 16, o);
            }
            WAIT_V(0);
            MAKE_TAG();
            BAR();
            if (tid == 0) set_flag(ws, F_X + j, tag);
            if (wid == 0) { poll_flags(ws, F_X + ((lane & 8) ? pm1 : pm0) * 8 + (lane & 7), lane < 16, tag); ACQUIRE(); WAIT_V(0); }
            BAR();
            g_prologue_rest(u0, ldsw);
            PROF(1);
            WAIT_V(6);
        }
#pragma unroll
        for (int k = 0; k < 4; ++k) aoff[k] = (wr * 64 + r) * 128 + (((2 * k + hh) ^ ((r >> 1) & 7)) << 4);
        const int boff = (8 * hh + qq) * 256 + ((wc ^ qq) << 6) + blk * 32 + p * 8;
        BAR();
        if (wr == 1) BAR();
        PROF(2);
        f32x16 acc[2][2][2];
        const int fk = fatt + (pn & 1) * 16 + type * 4 + (kk & 3);
        if (type == 1) g_kloop<true, 0>(lds, lds0, acc, u0, u1, aoff, boff, ldsw, ws, tag, F_S + bid, fatt, lane, wid, tch);
        else           g_kloop<false, 0>(lds, lds0, acc, u0, u1, aoff, boff, ldsw, ws, tag, F_S + bid, fatt, lane, wid, tch);
        PROF(3);
        G_STAGE(G_SA(1, 1), u1.A + 128 + 128 * LDA, u1.vA0, u1.vA1);
        SBAR();
        g_epilogue(acc, rws, type, pn, pm0, wr, wc, lane);
        PROF(11);
        if (wr == 1) BAR();
        if (type == 1) g_kloop<true, 1>(lds, lds0, acc, u1, rat, aoff, boff, ldsw, ws, tag, fk, fatt, lane, wid, tch);
        else           g_kloop<false, 1>(lds, lds0, acc, u1, rat, aoff, boff, ldsw, ws, tag, fk, fatt, lane, wid, tch);
        PROF(4);
        Q1_LOAD();
        G_STAGE(G_SA(1, 1), rat.A + rat.dA1 + rat.dA2, rat.vA0, rat.vA1);
        SBAR();
        g_epilogue(acc, rws, type, pn, pm1, wr, wc, lane);
        PROF(12);
        pf = fk + 32;
        Q1_WAIT(18);
    } else {
        const int pm = x8, pn = kk - 24, grp = wid >> 2;
        constexpr unsigned SLA = SEQ * 2, SLB = 768 * 2, WROW = FF * 2;
        constexpr int RB = 65536, RA2 = 131072;
        const char* Ap = (const char*)ws + WS_XN + (size_t)pm * 256 * LDA;
        const char* Bp = (const char*)w_in + pn * 256;
        unsigned vA0 = arow * LDA + achk, vA1 = vA0 + 64 * LDA, vB0 = brow * WROW + bchk, vB1 = vB0 + 32 * WROW, dA = 128 * LDA, dB = 64 * WROW;
        const int tb0 = 8 * pm + 2 * (wid & 3), fs0 = 8 * pn + 4 * grp;
        float bias[2];
        const int flz = F_L + (4 * x8 + (pn >> 1)) * 2 + (kk & 1);
        {
            f32x16 za[3];
            const int rg = 4 * x8 + (pn >> 1), hf = kk & 1, m0_ = wid & 1;
#pragma unroll
            for (int m = 0; m < 2; ++m) bias[m] = __uint_as_float((unsigned)bsp[(tb0 + m) * 32 + r] << 16);
            z2_half(lds, lds0, za, x, w_in, ws + (F_X + 8 * pm) * 8, rg, hf, tid, wid, lane
                );
            PIN(bias[0]); PIN(bias[1]);
            PROF(3);
            MAKE_TAG();
            if (wid == 0 && kk < 30) {
                const unsigned w = *(const LAS unsigned*)(lds + LDS_PARK + lane * 4);
                if (!__all(lane >= 16 || w == ((lane & 1) ? (unsigned)(tag >> 32) : (unsigned)tag))) poll_flags(ws, F_X + 8 * pm + (lane & 7), true, tag);
                ACQUIRE();
            }
            {
                const char* slab = (const char*)wsp + (size_t)x8 * 1048576 + pn * 131072;
                TOUCH_S(tid * 128, slab, lds0); TOUCH_S(tid * 128 + 65536, slab, lds0);
            }
            u32x4 zp[3][2];
#pragma unroll
            for (int i = 0; i < 3; ++i) { zp[i][0] = gelu8(za[i], 0); zp[i][1] = gelu8(za[i], 1); }
            const int nx = wid < 4 ? 8 : wid < 6 ? 6 : 0;
            LAS float* red = (LAS float*)lds;
            float s[2] = {0.f, 0.f}, mean[2];
#pragma unroll
            for (int i = 0; i < 24; ++i) if (i < 16 || i - 16 < nx) s[(i >> 3) & 1] = sum_pair(zp[i >> 3][(i >> 2) & 1][i & 3], s[(i >> 3) & 1]);
            red[wid * 128 + m0_ * 64 + lane] = s[0]; red[wid * 128 + (m0_ ^ 1) * 64 + lane] = s[1];
            WAIT_L(0); BAR();
#pragma unroll
            for (int m = 0; m < 2; ++m) { float t = 0.f;
#pragma unroll
                for (int w = 0; w < 8; ++w) t += red[w * 128 + (m0_ ^ m) * 64 + lane];
                mean[m] = swap_add(t) * (2.0f / FH); }
            f32x2 d[24], q2[2] = {{0.f, 0.f}, {0.f, 0.f}};
#pragma unroll
            for (int i = 0; i < 24; ++i) { const int m = (i >> 3) & 1; d[i] = unpk(zp[i >> 3][(i >> 2) & 1][i & 3]) - (f32x2){mean[m], mean[m]};
                if (i < 16 || i - 16 < nx) q2[m] = __builtin_elementwise_fma(d[i], d[i], q2[m]); }
            red[1024 + wid * 128 + m0_ * 64 + lane] = q2[0][0] + q2[0][1]; red[1024 + wid * 128 + (m0_ ^ 1) * 64 + lane] = q2[1][0] + q2[1][1];
            GP(9);
            WAIT_V(2); WAIT_L(0); BAR();
            LAS f32x2* tab = (LAS f32x2*)(lds + 160768);
            if (wid == 0) {
#pragma unroll
                for (int m = 0; m < 2; ++m) { float t = 0.f;
#pragma unroll
                    for (int w = 0; w < 8; ++w) t += red[1024 + w * 128 + m * 64 + lane];
                    s[m] = swap_add(t); }
                const float mo = hh ? mean[1] : mean[0], qo = hh ? s[1] : s[0];
                const unsigned t32 = (unsigned)tag;
                st16(rws, WS_G + (rg * 2 + hf) * 1024 + lane * 16, (u32x4){__float_as_uint(mo), t32, __float_as_uint(qo), t32});
                u32x4 o;
#pragma unroll 1
                for (unsigned spins = 0; spins < (1u << 17); ++spins) {
                    o = ld16(rws, WS_G + (rg * 2 + (hf ^ 1)) * 1024 + lane * 16);
                    if (__all(o[1] == t32 && o[3] == t32)) break;
                    __builtin_amdgcn_s_sleep(2);
                }
                const float mp = __uint_as_float(o[0]), dl = mo - mp;
                tab[lane] = (f32x2){0.5f * (mo + mp), 1.0f / sqrtf((qo + __uint_as_float(o[2]) + dl * dl * (FH * 0.25f)) * (1.0f / FH) + 1e-5f)};
            }
            WAIT_L(0); BAR();
            GP(10);
            if (kk < 30) {
                dma_pair<0, 8192>(vA0, vA1, Ap, ldsw); dma_pair<16384, 24576>(vA0, vA1, Ap + 128 * LDA, ldsw); dma_pair<RB, RB + 8192>(vB0, vB1, Bp, ldsw);
                dma_pair<32768, 32768 + 8192>(vA0, vA1, Ap + 128, ldsw); dma_pair<32768 + 16384, 32768 + 24576>(vA0, vA1, Ap + 128 + 128 * LDA, ldsw);
                dma_pair<RB + 16384, RB + 16384 + 8192>(vB0, vB1, Bp + 64 * WROW, ldsw); dma_pair<RB + 32768, RB + 32768 + 8192>(vB0, vB1, Bp + 128 * WROW, ldsw);
            }
            f32x2 rs[2], of[2];
#pragma unroll
            for (int m = 0; m < 2; ++m) { const f32x2 t = tab[(m0_ ^ m) * 32 + r]; const float o = (mean[m] - t[0]) * t[1]; rs[m] = (f32x2){t[1], t[1]}; of[m] = (f32x2){o, o}; }
            const lds_p tr = lds + 114688;
#pragma unroll
            for (int i = 0; i < 12; ++i)
                if (i < 8 || 2 * (i - 8) < nx) {
                    const int b = i >> 2, m = b & 1;
                    *(LAS u32x2*)(tr + ((m0_ ^ m) * 32 + r) * 720 + (b == 2 ? 8 + (wid >> 1) : wid) * 64 + (i & 3) * 16 + hh * 8) =
                        (u32x2){cvt2(__builtin_elementwise_fma(d[2 * i], rs[m], of[m])), cvt2(__builtin_elementwise_fma(d[2 * i + 1], rs[m], of[m]))};
                }
            WAIT_L(0); BAR();
#pragma unroll
            for (int i = 0; i < 6; ++i) { int c = tid + 512 * i; const bool ok = c < 2752; c = ok ? c : 0; const int zr = c / 43, zc = c - zr * 43;
                st16(rws, ok ? WS_Z2N + (rg * 64 + zr) * SLB + hf * 688 + zc * 16 : 0x80000000u, *(const LAS u32x4*)(tr + zr * 720 + zc * 16)); }
#pragma unroll
            for (int i = 0; i < 2; ++i) { const int c = tid + 512 * i, zr = c / 10;
                st16(rws, hf == 1 && c < 640 ? WS_Z2N + (rg * 64 + zr) * SLB + 1376 + (c - zr * 10) * 16 : 0x80000000u, (u32x4){0u, 0u, 0u, 0u}); }
        }
        PROF(1);
        if (kk < 30) {
            u32x4 z1[2][2][2];
            const bool busy = fs0 < 43;
#pragma unroll
            for (int k = 0; k < 4; ++k) aoff[k] = ((wid >> 1) & 1) * 16384 + ((wid & 1) * 64 + r) * 128 + (((2 * k + hh) ^ ((r >> 1) & 7)) << 4);
            const int bo0 = RB + (8 * hh + qq) * 256 + (((2 * grp) ^ qq) << 6) + blk * 32 + p * 8, bo1 = RB + (8 * hh + qq) * 256 + (((2 * grp + 1) ^ qq) << 6) + blk * 32 + p * 8;
            f32x16 acc[2][2];
#pragma unroll
            for (int c = 0; c < 4; ++c) acc[c >> 1][c & 1] = f32x16{};
#pragma unroll
            for (int c = 0; c < 8; ++c) z1[c >> 2][(c >> 1) & 1][c & 1] = (u32x4){0u, 0u, 0u, 0u};
            Ap += 256; Bp += 192 * WROW;
            unsigned sa = 0, sa1 = 32768, sa2 = RA2;
            unsigned la = ldsw + RA2, la1 = ldsw, la2 = ldsw + 32768, lb = 49152;
            const char* Aq = Ap; unsigned lq = la, lq1 = la1, lq2 = la2, vQ0 = vA0, vQ1 = vA1, dQ = dA;
#define SP_SRC(s_) do { if ((s_) == 6) { Ap = (const char*)wsp + (size_t)pm * 256 * SLA; vA1 = arow * SLA + achk + 64 * SLA; dA = 128 * SLA; } \
                if ((s_) == 5) { Bp = (const char*)ws + WS_Z2N + pn * 256; vB0 = brow * SLB + bchk; vB1 = vB0 + 32 * SLB; dB = 64 * SLB; } \
                if (t == 6) { Aq = (const char*)wsp + (size_t)pm * 256 * SLA; vQ0 = arow * SLA + achk; vQ1 = vQ0 + 64 * SLA; dQ = 128 * SLA; } } while (0)
#define SP_LATE() do { dma_pair<0, 8192>(vQ0, vQ1, Aq, lq); dma_one<16384>(vQ0, Aq + dQ, lq); Aq += 128; const unsigned l_ = lq; lq = lq1; lq1 = lq2; lq2 = l_; } while (0)
#define SP_A1() dma_one<24576>(vA1, Ap + dA, la)
#define SP_B0() dma_one<RB>(vB0, Bp, ldsw + lb)
#define SP_B1() dma_one<RB + 8192>(vB1, Bp, ldsw + lb)
#define SP_ADV() do { Ap += 128; Bp += dB; const unsigned l_ = la; la = la1; la1 = la2; la2 = l_; lb = (lb + 16384) & 49152; } while (0)
#define SP_GELU() do { _Pragma("unroll") for (int c = 0; c < 4; ++c) { f32x16& a = acc[c >> 1][c & 1]; z1[c >> 1][c & 1][0] = gelu8(a, 0); z1[c >> 1][c & 1][1] = gelu8(a, 1); a = f32x16{}; } } while (0)
#define SP_MMA(k) do { if (busy) { _Pragma("unroll") for (int c = 0; c < 4; ++c) acc[c >> 1][c & 1] = MFMA32(Bt[c & 1][k], At[c >> 1][k], acc[c >> 1][c & 1]); } } while (0)
#define SP_STEP(PRE, LATE, WAIT0, WAIT1, D0, D1, D2, POST, LASTBAR) do { \
                bf16x8 At[2][4], Bt[2][4]; \
                PRE; \
                const lds_cp pa = lds + sa, pb = lds + (t & 3) * 16384; \
                if (busy) { _Pragma("unroll") for (int k = 0; k < 4; ++k) { Bt[0][k] = ldtr2(pb + bo0 + k * 4096, 1024); At[0][k] = ld128(pa + aoff[k]); At[1][k] = ld128(pa + aoff[k] + 4096); Bt[1][k] = ldtr2(pb + bo1 + k * 4096, 1024); } } \
                LATE; \
                if (grp == 1) { WAIT1; } \
                WAIT_L(0); BAR(); SBAR(); \
                __builtin_amdgcn_s_setprio(1); \
                SP_MMA(0); SBAR(); D0; SBAR(); SP_MMA(1); SBAR(); D1; SBAR(); SP_MMA(2); SBAR(); D2; SBAR(); SP_MMA(3); \
                __builtin_amdgcn_s_setprio(0); \
                SBAR(); \
                POST; \
                if (grp == 0) { WAIT0; } \
                if (LASTBAR) BAR(); \
                const unsigned s_ = sa; sa = sa1; sa1 = sa2; sa2 = s_; } while (0)
            asm volatile("s_waitcnt vmcnt(16) lgkmcnt(0)\n\ts_barrier" ::: "memory");
            if (grp == 1) { SP_A1(); SP_B0(); SP_B1(); SP_ADV(); BAR(); }
            PROF(2);
            int t = 0;
#pragma unroll 1
            for (; t < 8; ++t)
                SP_STEP({ if (wid == 0) { if (t == 2 && lane == 0) { set_flag(ws, flz, tag); set_flag(ws, F_S + bid, tag); } if (t == 3) { poll_flags(ws, F_L + lane, true, tag); ACQUIRE(); } } SP_SRC(t + grp); },
                        SP_LATE(), { if (t == 0) WAIT_V(16); else WAIT_V(8); }, { if (t == 0) WAIT_V(16); else WAIT_V(6); }, SP_A1(), SP_B0(), SP_B1(), { SP_ADV(); if (t == 7 && grp == 1) SP_GELU(); }, true);
            PROF(11);
            if (grp == 0) SP_GELU();
            PROF(12);
#pragma unroll 1
            for (; t < 36; ++t) SP_STEP(NOP_, SP_LATE(), WAIT_V(8), WAIT_V(6), SP_A1(), SP_B0(), SP_B1(), SP_ADV(), true);
#pragma unroll 1
            for (; t < 38; ++t)
                SP_STEP({ if (wid == 0) { SP_CHECK(); } }, SP_LATE(), { if (t == 36) WAIT_V(8); else WAIT_V(6); }, { if (t == 36) WAIT_V(6); else WAIT_V(4); },
                        if (t + grp < 38) SP_A1(), if (t + grp < 37) SP_B0(), if (t + grp < 37) SP_B1(), SP_ADV(), true);
            Q1_LOAD();
#pragma unroll 1
            for (; t < 40; ++t) SP_STEP(NOP_, NOP_, WAIT_V(8), WAIT_V(8), NOP_, NOP_, NOP_, NOP_, !(t == 39 && grp == 1));
            PROF(4);
            G_STAGE(G_SA(0, 0), rat.A, rat.vA0, rat.vA1); G_STAGE(G_SB(0, 0), rat.B, rat.vA0, rat.vA1); G_STAGE(G_SA(1, 0), rat.A + 8192, rat.vA0, rat.vA1); G_STAGE(G_SB(1, 0), rat.B + 8192, rat.vA0, rat.vA1);
            G_STAGE(G_SA(0, 1), rat.A + 65536, rat.vA0, rat.vA1); G_STAGE(G_SB(0, 1), rat.B + 65536, rat.vA0, rat.vA1); G_STAGE(G_SA(1, 1), rat.A + 73728, rat.vA0, rat.vA1); G_STAGE(G_SB(1, 1), rat.B + 73728, rat.vA0, rat.vA1);
            Q2_LOAD();
            SBAR();
#pragma unroll
            for (int m = 0; m < 2; ++m)
#pragma unroll
                for (int c = 0; c < 4; ++c) {
                    const f32x16& a = acc[m][c >> 1]; const u32x4 z = z1[m][c >> 1][c & 1]; const int s8 = 8 * (c & 1);
                    u32x4 o;
#pragma unroll
                    for (int e = 0; e < 4; ++e) o[e] = cvtpk(bflo(z[e]) * (a[s8 + 2 * e] + bias[m]), bfhi(z[e]) * (a[s8 + 2 * e + 1] + bias[m]));
                    st16(rws, fs0 + c < 43 ? WS_AF + ((256 + fs0 + c) * 64 + tb0 + m) * 1024 + lane * 16 : 0x80000000u, o);
                }
            pf = F_U + pm * 8 + pn;
            Q1_WAIT(20);
        } else {
            WAIT_V(0); WAIT_L(0); BAR();
            if (tid == 0) { set_flag(ws, flz, tag); set_flag(ws, F_S + bid, tag); }
            {
                const int aw = x8 * 2 + (kk - 30);
#pragma unroll
                for (int i = 0; i < 4; ++i) touch(tid * 128 + i * 65536, (const char*)wo + aw * 262144, lds0);
                if (tid < 344) touch(tid * 128, (const char*)wout + aw * 44032, lds0);
            }
            if (wid == 0) { poll_flags(ws, fatt + (lane & 31), lane < 32 && (lane & 15) < 12, tag); ACQUIRE(); }
            WAIT_V(0); BAR();
            PROF(4);
            g_prologue_b(rat, ldsw); g_prologue_rest(rat, ldsw); G_STAGE(G_SA(1, 1), rat.A + rat.dA1 + rat.dA2, rat.vA0, rat.vA1);
            Q1_LOAD();
            pf = -1;
            Q1_WAIT(0);
        }
    }
    BAR();
    PROF(5);
    {
        const int kh = wid >> 2, rg = wid & 3;
        const bool early = kk < 24 || kk >= 30;
        const rsrc_t rkv = __builtin_amdgcn_make_buffer_rsrc((void*)kvx, (short)0, 4 * 64 * 16384, 0x00020000);
        const unsigned dv0 = rg * 2048 + lane * 16 + kh * 32768;
        unsigned wrapoff = (unsigned)(h1 * 16 + i1 + 1) * 65536u, wrapto = (unsigned)h2 << 20;
        unsigned sA_ = 0, sB_ = ((unsigned)h1 << 20) + 65536u, lA = lds0 + kh * 65536 + rg * 2048, lB = lA + 16384;
#define A_ADV() do { sA_ = sB_; sB_ += 65536u; if (sB_ == wrapoff) { sB_ = wrapto; wrapoff = (unsigned)(h2 * 16 + i2 + 1) * 65536u; wrapto = 0x40000000u; } const unsigned t_ = lA; lA = lB; lB = t_; } while (0)
#define D_KO(h) (bdma1<8192, (h) * 1024>(dv0, rkv, sA_ + 16384, lA))
#define D_VE(h) (bdma1<32768, (h) * 1024>(dv0, rkv, sA_ + 8192, lA))
#define D_KE(h) (bdma1<0, (h) * 1024>(dv0, rkv, sB_, lB))
#define D_VO(h) (bdma1<40960, (h) * 1024>(dv0, rkv, sA_ + 24576, lA))
#define PUBLISH() do { if (pf >= 0) { if (wid == 0 && lane == 0) set_flag(ws, pf, tag); pf = -1; } } while (0)
        const lds_cp qrd = lds + 131072 + rg * 8192 + lane * 16;
        const lds_cp rbase = lds + kh * 65536 + lane * 16;
        int so = 0;
#pragma unroll 1
        for (int blkno = 0; blkno < 2; ++blkno) {
            const int ib = blkno ? i2 : i1, head = 4 * x8 + (blkno ? h2 : h1);
            const int qrow = 128 * ib + 32 * rg + r - 4 * hh - 64 * kh;
            f32x16 o[4], sA, sB, negm;
            bf16x8 fr[8];
            u32x4 pw0, pw1;
            float mref, l0 = 0.f, l1 = 0.f, resc = 1.0f, ma, mb;
            bool dead = false, pend = false;
            lds_cp pc = rbase + so, pn = rbase + (so ^ 16384);
#pragma unroll
            for (int d = 0; d < 8; ++d) fr[d] = ld128(pc + d * 1024);
            sA = f32x16{};
#pragma unroll
            for (int d = 0; d < 8; ++d) sA = MFMA32(fr[d], q[d], sA);
            asm volatile("s_waitcnt lgkmcnt(0)\n\ts_barrier" ::: "memory");
            A_ADV();
            if (blkno == 1) { D_KO(0); D_KO(1); }
            if (blkno == 1 && wid < 5) {
                if (wid < 4) touch(lane * 128, (const char*)wo + (size_t)(x8 >> 1) * 1048576 + kk * 32768 + wid * 8192, lds0);
                else { const unsigned ur = 192 * (x8 >> 1) + 6 * kk + (lane >> 3); touch((ur > FH - 1 ? FH - 1 : ur) * 1024 + (lane & 7) * 128, wout, lds0); }
            }
#pragma unroll
            for (int d = 0; d < 4; ++d) fr[d] = ld128(pc + 8192 + d * 1024);
#pragma unroll
            for (int d = 0; d < 4; ++d) o[d] = f32x16{};
            if (ib == 0) att_mask(sA, qrow);
            {
                float m = MX3(sA[0], sA[1], sA[2]);
#pragma unroll
                for (int e = 3; e < 15; e += 2) m = MX3(m, sA[e], sA[e + 1]);
                m = swap_max(__builtin_fmaxf(m, sA[15]));
                dead = m == -__builtin_inff();
                mref = dead ? 0.f : m;
#pragma unroll
                for (int e = 0; e < 16; ++e) { sA[e] -= mref; negm[e] = -mref; }
                PIN(negm);
#pragma unroll
                for (int e = 0; e < 6; ++e) sA[e] = EX2(sA[e]);
                pw0[0] = cvtpk(sA[0], sA[1]); pw0[1] = cvtpk(sA[2], sA[3]);
            }
#define A_RD(j, ptr) do { fr[j] = ld128(ptr); } while (0)
#define ANYCHK(x) (x)
#define XE(S, i) S[i] = EX2(S[i])
#define A_P1(SC, SN, KP, VP, E0, E1, E2) do { \
            A_RD(4, (KP) + 4096); SBAR(); SN = MFMA32(fr[0], q[0], negm); XE(SC, 6); XE(SC, 7); pw0[2] = cvtpk(SC[4], SC[5]); PIN(SC); PIN(pw0); SBAR(); \
            A_RD(5, (KP) + 5120); SBAR(); SN = MFMA32(fr[1], q[1], SN); XE(SC, 8); XE(SC, 9); pw0[3] = cvtpk(SC[6], SC[7]); PIN(SC); PIN(pw0); E0; SBAR(); \
            A_RD(6, (KP) + 6144); SBAR(); SN = MFMA32(fr[2], q[2], SN); XE(SC, 10); XE(SC, 11); l0 += SC[0]; PIN(SC); PIN(l0); SBAR(); \
            A_RD(7, (KP) + 7168); SBAR(); SN = MFMA32(fr[3], q[3], SN); XE(SC, 12); XE(SC, 13); l1 += SC[1]; PIN(SC); PIN(l1); SBAR(); \
            A_RD(0, (VP));        SBAR(); SN = MFMA32(fr[4], q[4], SN); XE(SC, 14); XE(SC, 15); pw1[0] = cvtpk(SC[8], SC[9]); PIN(SC); PIN(pw1); E1; SBAR(); \
            A_RD(1, (VP) + 2048); SBAR(); SN = MFMA32(fr[5], q[5], SN); l0 += SC[2]; l1 += SC[3]; l0 += SC[4]; l1 += SC[5]; l0 += SC[6]; PIN(l0); PIN(l1); SBAR(); \
            A_RD(2, (VP) + 4096); SBAR(); SN = MFMA32(fr[6], q[6], SN); l1 += SC[7]; l0 += SC[8]; l1 += SC[9]; l0 += SC[10]; pw1[1] = cvtpk(SC[10], SC[11]); PIN(l0); PIN(l1); PIN(pw1); SBAR(); \
            A_RD(3, (VP) + 6144); SBAR(); SN = MFMA32(fr[7], q[7], SN); l1 += SC[11]; l0 += SC[12]; l1 += SC[13]; pw1[2] = cvtpk(SC[12], SC[13]); PIN(l0); PIN(l1); PIN(pw1); E2; SBAR(); } while (0)
#define PWF(x) __builtin_bit_cast(bf16x8, x)
#define A_P2A(SC, SN, VP, MASK, DQ) do { \
            A_RD(4, (VP) + 1024); A_RD(5, (VP) + 3072); SBAR(); o[0] = MFMA32(fr[0], PWF(pw0), o[0]); l0 += SC[14]; l1 += SC[15]; pw1[3] = cvtpk(SC[14], SC[15]); PIN(l0); PIN(l1); PIN(pw1); SBAR(); \
            A_RD(6, (VP) + 5120); A_RD(7, (VP) + 7168); SBAR(); o[1] = MFMA32(fr[1], PWF(pw0), o[1]); \
            if (MASK) att_mask(SN, DQ); \
            ma = MX3(SN[0], SN[1], SN[2]); mb = MX3(SN[3], SN[4], SN[5]); PIN(ma); PIN(mb); SBAR(); \
            o[2] = MFMA32(fr[2], PWF(pw0), o[2]); ma = MX3(ma, SN[6], SN[7]); mb = MX3(mb, SN[8], SN[9]); PIN(ma); PIN(mb); ma = MX3(ma, SN[10], SN[11]); PIN(ma); SBAR(); \
            o[3] = MFMA32(fr[3], PWF(pw0), o[3]); mb = MX3(mb, SN[12], SN[13]); ma = MX3(ma, SN[14], SN[15]); PIN(ma); PIN(mb); SBAR(); } while (0)
#define A_CHECK(SN) do { if (__builtin_expect(ANYCHK((__builtin_amdgcn_fcmpf(ma, ATT_THR, 2) | __builtin_amdgcn_fcmpf(mb, ATT_THR, 2)) != 0), 0)) {            \
                const float dl_ = __builtin_fmaxf(swap_max(__builtin_fmaxf(ma, mb)), 0.f); mref += dl_; resc = EX2(-dl_); l0 *= resc; l1 *= resc; pend = true; \
                _Pragma("unroll") for (int e = 0; e < 16; ++e) { SN[e] -= dl_; negm[e] = -mref; } PIN(negm); } SBAR(); } while (0)
#define A_RESC() do { if (__builtin_expect(pend, 0)) { pend = false;         \
            _Pragma("unroll") for (int d = 0; d < 4; ++d) _Pragma("unroll") for (int e = 0; e < 16; ++e) o[d][e] *= resc; } } while (0)
#define A_P2B(SN, RD0, RD1, RD2, RD3, D0, D1) do { \
            RD0; SBAR(); o[0] = MFMA32(fr[4], PWF(pw1), o[0]); SBAR(); A_CHECK(SN); D0; SBAR(); \
            RD1; SBAR(); o[1] = MFMA32(fr[5], PWF(pw1), o[1]); XE(SN, 0); XE(SN, 1); PIN(SN); SBAR(); \
            RD2; SBAR(); o[2] = MFMA32(fr[6], PWF(pw1), o[2]); XE(SN, 2); XE(SN, 3); pw0[0] = cvtpk(SN[0], SN[1]); PIN(SN); PIN(pw0); D1; SBAR(); \
            RD3; SBAR(); o[3] = MFMA32(fr[7], PWF(pw1), o[3]); XE(SN, 4); XE(SN, 5); pw0[1] = cvtpk(SN[2], SN[3]); PIN(SN); PIN(pw0); SBAR(); \
            A_RESC(); } while (0)
#define A_P2K(SN, KN, D0, D1) A_P2B(SN, A_RD(0, (KN)), A_RD(1, (KN) + 1024), A_RD(2, (KN) + 2048), A_RD(3, (KN) + 3072), D0, D1)
#define UNP(...) __VA_ARGS__
#define PROFO(i) PROF(i)
#pragma unroll 1
            for (int n = 0; n < ib; ++n) {
                A_P1(sA, sB, pc + 8192, pc + 32768, D_VE(0), D_VE(1), D_KE(0));
                A_P2A(sA, sB, pc + 32768, false, 0);
                A_P2K(sB, pn, D_KE(1), D_VO(0));
                A_P1(sB, sA, pn, pc + 32768 + 8192, D_VO(1), NOP_, NOP_);
                A_P2A(sB, sA, pc + 32768 + 8192, (n == ib - 1), qrow - 128 * ib);
                const int ev = blkno == 0 && (early || n == 0) ? n : 6;
                if (ev != 0) WAIT_V(0); else if (kk < 24) WAIT_V(22); else WAIT_V(14);
                asm volatile("s_waitcnt lgkmcnt(0)\n\ts_barrier" ::: "memory");
#define A_EV() do { A_ADV(); if (ev != 0) PUBLISH(); \
                if (ev < 6) { \
                    if (ev == 3 && wid == 0) dma_words(lane * 4, ws + (size_t)(fatt + 32) * 8, lds0 + 131072); \
                    if (ev == 4 && wid == 0) { check_parked<0>(i2 >= 8 ? 8 : 12, lds + 131072, ws, fatt + 32, tag, lane); ACQUIRE(); } \
                    if (ev == 5) Q2_LOAD(); } \
                D_KO(0); } while (0)
                A_P2B(sA, A_RD(0, pn + 8192), A_RD(1, pn + 9216), A_RD(2, pn + 10240), A_RD(3, pn + 11264), A_EV(), D_KO(1));
                so ^= 16384;
                const lds_cp t_ = pc; pc = pn; pn = t_;
            }
            if (blkno == 0) PROF(6);
            lA = __builtin_amdgcn_readfirstlane(lA); lB = __builtin_amdgcn_readfirstlane(lB); sA_ = __builtin_amdgcn_readfirstlane(sA_); sB_ = __builtin_amdgcn_readfirstlane(sB_);
#define A_QNEXT() do { if (blkno == 0) { _Pragma("unroll") for (int d = 0; d < 8; ++d) q[d] = ld128(qrd + d * 1024); } } while (0)
#define A_LASTBAR() do { asm volatile("s_waitcnt vmcnt(0) lgkmcnt(0)\n\ts_barrier" ::: "memory"); PUBLISH(); } while (0)
            const bool some = kh == 0 || rg >= 2, live = 2 * kh + 1 <= rg;
            const lds_cp vp = pc + 32768 + 8192;
            if (some) A_P1(sA, sB, pc + 8192, pc + 32768, if (blkno == 0) { D_VE(0); D_VE(1); }, if (blkno == 0) { D_KE(0); D_KE(1); }, if (blkno == 0) { D_VO(0); D_VO(1); });
            else if (blkno == 0) { D_VE(0); D_VE(1); D_KE(0); D_KE(1); D_VO(0); D_VO(1); }
            A_QNEXT();
            if (some) {
                A_P2A(sA, sB, pc + 32768, true, qrow - 128 * ib - 32);
                A_P2B(sB, A_RD(0, vp), A_RD(1, vp + 2048), A_RD(2, vp + 4096), A_RD(3, vp + 6144), NOP_, NOP_);
            }
            if (live) {
#pragma unroll
                for (int j = 0; j < 4; ++j) fr[4 + j] = ld128(vp + j * 2048 + 1024);
#pragma unroll
                for (int e = 6; e < 16; ++e) sB[e] = EX2(sB[e]);
#pragma unroll
                for (int e = 0; e < 16; e += 2) { l0 += sB[e]; l1 += sB[e + 1]; }
#pragma unroll
                for (int e = 0; e < 4; ++e) { if (e >= 2) pw0[e] = cvtpk(sB[2 * e], sB[2 * e + 1]); pw1[e] = cvtpk(sB[8 + 2 * e], sB[9 + 2 * e]); }
            }
            A_LASTBAR();
            if (live) {
#pragma unroll
                for (int j = 0; j < 4; ++j) o[j] = MFMA32(fr[j], PWF(pw0), o[j]);
#pragma unroll
                for (int j = 0; j < 4; ++j) o[j] = MFMA32(fr[4 + j], PWF(pw1), o[j]);
            }
            {
                const int sb_own = kh == 0 ? 131072 + rg * 8192 : (rg >> 1) * 65536 + 32768 + so + (rg & 1) * 8192;
                const int sb_par = kh == 1 ? 131072 + rg * 8192 : (rg >> 1) * 65536 + 32768 + so + (rg & 1) * 8192;
                const float mown = dead ? -__builtin_inff() : mref, lown = l0 + l1;
                *(LAS f32x2*)(lds + kh * 65536 + so + 8192 + rg * 512 + lane * 8) = (f32x2){mown, lown};
                const f32x16 s0 = kh ? o[0] : o[2], s1 = kh ? o[1] : o[3], k0 = kh ? o[2] : o[0], k1 = kh ? o[3] : o[1];
#pragma unroll
                for (int qd = 0; qd < 4; ++qd) {
                    *(LAS f32x4*)(lds + sb_own + qd * 1024 + lane * 16) = (f32x4){s0[4 * qd], s0[4 * qd + 1], s0[4 * qd + 2], s0[4 * qd + 3]};
                    *(LAS f32x4*)(lds + sb_own + 4096 + qd * 1024 + lane * 16) = (f32x4){s1[4 * qd], s1[4 * qd + 1], s1[4 * qd + 2], s1[4 * qd + 3]};
                }
                asm volatile("s_waitcnt lgkmcnt(0)\n\ts_barrier" ::: "memory");
                const f32x2 ml = *(const LAS f32x2*)(lds + (kh ^ 1) * 65536 + so + 8192 + rg * 512 + lane * 8);
                f32x16 p0, p1;
#pragma unroll
                for (int qd = 0; qd < 4; ++qd) {
                    const f32x4 a = *(const LAS f32x4*)(lds + sb_par + qd * 1024 + lane * 16), b = *(const LAS f32x4*)(lds + sb_par + 4096 + qd * 1024 + lane * 16);
#pragma unroll
                    for (int e = 0; e < 4; ++e) { p0[4 * qd + e] = a[e]; p1[4 * qd + e] = b[e]; }
                }
                const float mf = __builtin_fmaxf(mown, ml[0]);
                const float fo = EX2(mown - mf), fp = EX2(ml[0] - mf);
                const float inv = 1.0f / swap_add(fo * lown + fp * ml[1]);
                const float ca = fo * inv, cb = fp * inv;
#pragma unroll
                for (int e = 0; e < 16; ++e) { p0[e] = ca * k0[e] + cb * p0[e]; p1[e] = ca * k1[e] + cb * p1[e]; }
                const unsigned dst = WS_AF + ((head * 8 + 4 * kh) * 64 + 4 * ib + rg) * 1024 + lane * 16;
                st16(rws, dst, pack8(p0, 0, 1.0f)); st16(rws, dst + 65536, pack8(p0, 1, 1.0f));
                st16(rws, dst + 2 * 65536, pack8(p1, 0, 1.0f)); st16(rws, dst + 3 * 65536, pack8(p1, 1, 1.0f));
                pf = F_C + ib * 32 + head;
            }
            so ^= 16384;
            if (blkno == 0) PROF(7);
        }
        PROF(8);
#undef A_RD
#undef A_P1
#undef A_P2A
#undef A_P2B
#undef A_P2K
#undef A_CHECK
#undef XE
#undef PWF
    }

    {
        int lane;
        asm volatile("v_mbcnt_lo_u32_b32 %0, -1, 0\n\tv_mbcnt_hi_u32_b32 %0, -1, %0" : "=v"(lane));
        const int r = lane & 31, hh = lane >> 5, blk = (lane >> 4) & 1, qq = (lane & 15) >> 2, p = lane & 3;
        const unsigned brow = 4 * wid + (lane >> 4), bchk = ((lane & 15) ^ (((lane >> 4) & 3) << 2)) << 4;
        const int sq = x8 >> 1, tm = (x8 & 1) * 8 + (kk >> 2), tn = kk & 3, rb = wid & 3, kp = wid >> 2;
        OutArgs oa;
        oa.ra = __builtin_amdgcn_make_buffer_rsrc((void*)(ws + WS_AF), (short)0, NKS * 65536, 0x00020000);
        oa.wo = (const char*)wo + (size_t)sq * 1048576 + tn * 256; oa.wu = (const char*)wout + tn * 256;
        oa.wv = brow * 1024 + bchk;
#pragma unroll
        for (int j = 0; j < 6; ++j) { const unsigned ur = 192 * sq + 32 * j + brow; oa.uv[j] = (ur > FH - 1 ? FH - 1 : ur) * 1024 + bchk; }
        oa.va = kp * 131072 + (tm * 4 + rb) * 1024 + lane * 16; oa.sc = sq * 64 * 65536; oa.su = (256 + 12 * sq) * 65536; oa.ldsw = ldsw;
#pragma unroll
        for (int c = 0; c < 4; ++c) oa.rd[c] = lds + (kp * 32 + 4 * hh + qq) * 256 + ((c ^ qq) << 6) + blk * 32 + p * 8;
        WAIT_L(0); BAR();
        out_w<0>(oa); out_w<1>(oa); out_w<2>(oa); out_w<3>(oa);
        WAIT_V(8); BAR();
        if (wid == 0 && lane == 0) set_flag(ws, pf, tag);
        if (wid == 1) {
            if (bid == 0) {
#pragma unroll 1
                for (int i = 0; i < 4; ++i) poll_flags(ws, F_S + i * 64 + lane, true, tag);
                if (lane == 0) set_flag(ws, F_E, tag);
            }
            poll_flags(ws, lane < 8 ? F_C + tm * 32 + 8 * sq + lane : F_U + (tm >> 1) * 8 + ((3 * sq) >> 1) + (lane & 1), lane < 10, tag);
        }
        out_w<4>(oa); out_w<5>(oa); out_w<6>(oa);
        WAIT_V(10); BAR();
        PROFO(9);
        u32x4 af[6][2];
        bf16x8 wf[2][4];
        f32x16 acc[4] = {};
        out_a<0>(oa, af); out_a<1>(oa, af); out_a<2>(oa, af); out_a<3>(oa, af); out_a<4>(oa, af);
#pragma unroll
        for (int c = 0; c < 4; ++c) wf[0][c] = ldtr2(oa.rd[c], 2048);
#define OS(t) out_step<t>(oa, af, wf, acc)
        OS(0); OS(1); OS(2); OS(3); OS(4); OS(5); OS(6); OS(7); OS(8); OS(9); OS(10); OS(11); OS(12); OS(13); OS(14); OS(15); OS(16); OS(17); OS(18);
#undef OS
        PROFO(10);
        const f32x16 s0 = kp ? acc[0] : acc[2], s1 = kp ? acc[1] : acc[3];
        f32x16 k0 = kp ? acc[2] : acc[0], k1 = kp ? acc[3] : acc[1];
#pragma unroll
        for (int g = 0; g < 4; ++g) {
            *(LAS f32x4*)(lds + 16384 + wid * 8192 + g * 1024 + lane * 16) = (f32x4){s0[4 * g], s0[4 * g + 1], s0[4 * g + 2], s0[4 * g + 3]};
            *(LAS f32x4*)(lds + 16384 + wid * 8192 + 4096 + g * 1024 + lane * 16) = (f32x4){s1[4 * g], s1[4 * g + 1], s1[4 * g + 2], s1[4 * g + 3]};
        }
        const size_t orow = (size_t)(tm * 128 + rb * 32 + r) * DM + tn * 128 + kp * 64 + 4 * hh;
        u32x2 xv[8];
        if (rb == sq) {
#pragma unroll
            for (int g = 0; g < 8; ++g) xv[g] = *(const u32x2*)(x + orow + 8 * g);
        }
        WAIT_L(0); BAR();
#pragma unroll
        for (int g = 0; g < 4; ++g) {
            const f32x4 a = *(const LAS f32x4*)(lds + 16384 + (wid ^ 4) * 8192 + g * 1024 + lane * 16), b = *(const LAS f32x4*)(lds + 16384 + (wid ^ 4) * 8192 + 4096 + g * 1024 + lane * 16);
#pragma unroll
            for (int e = 0; e < 4; ++e) { k0[4 * g + e] += a[e]; k1[4 * g + e] += b[e]; }
        }
        const unsigned pb = WS_P + ((tm * 4 + tn) * 4 + rb) * 65536 + kp * 8192 + lane * 16;
        const int fp = F_P + (((tm * 4 + tn) * 4 + rb) * 2 + kp) * 4;
        if (rb != sq) {
#pragma unroll
            for (int g = 0; g < 4; ++g) {
                st16(rws, pb + sq * 16384 + g * 1024, __builtin_bit_cast(u32x4, (f32x4){k0[4 * g], k0[4 * g + 1], k0[4 * g + 2], k0[4 * g + 3]}));
                st16(rws, pb + sq * 16384 + 4096 + g * 1024, __builtin_bit_cast(u32x4, (f32x4){k1[4 * g], k1[4 * g + 1], k1[4 * g + 2], k1[4 * g + 3]}));
            }
            WAIT_V(0);
            if (lane == 0) set_flag(ws, fp + sq, tag);
        }
        PROFO(11);
        if (rb == sq) {
            poll_flags(ws, fp + (lane & 3), (lane & 3) != sq, tag);
            PROFO(12);
            u32x4 pp[3][8];
#pragma unroll
            for (int j = 0; j < 3; ++j)
#pragma unroll
                for (int g = 0; g < 8; ++g) pp[j][g] = ld16(rws, pb + ((sq + 1 + j) & 3) * 16384 + g * 1024);
#pragma unroll
            for (int g = 0; g < 8; ++g) {
                const f32x16& k = g < 4 ? k0 : k1; const int e0 = 4 * (g & 3);
                const f32x4 a = __builtin_bit_cast(f32x4, pp[0][g]), b = __builtin_bit_cast(f32x4, pp[1][g]), c = __builtin_bit_cast(f32x4, pp[2][g]);
                const f32x4 t = ((f32x4){k[e0], k[e0 + 1], k[e0 + 2], k[e0 + 3]} + a) + (b + c);
                *(u32x2*)(out + orow + 8 * g) = (u32x2){cvtpk(bflo(xv[g][0]) + t[0], bfhi(xv[g][0]) + t[1]), cvtpk(bflo(xv[g][1]) + t[2], bfhi(xv[g][1]) + t[3])};
            }
        }
    }
    PROF_END(ws);
}

extern "C" void kernel_launch(void* const* d_in, const int* in_sizes, int n_in, void* d_out, int out_size, void* d_ws, size_t ws_size, hipStream_t stream) {
    (void)in_sizes; (void)n_in; (void)out_size; (void)ws_size;
    fused_fwd<<<256, 512, 0, stream>>>((const bf16_t*)d_in[0], (const bf16_t*)d_in[1], (const bf16_t*)d_in[2], (const bf16_t*)d_in[3], (const bf16_t*)d_in[4], (const bf16_t*)d_in[5],
                                       (const bf16_t*)d_in[7], (const bf16_t*)d_in[8], (const bf16_t*)d_in[9], (const bf16_t*)d_in[10], (bf16_t*)d_out, (unsigned char*)d_ws);
}
```

```cpp
#include <hip/hip_runtime.h>
#include <hip/hip_bf16.h>
#include <stdint.h>

#define LAS __attribute__((address_space(3)))
#define DI __device__ __forceinline__
typedef unsigned short bf16_t;
typedef unsigned long long u64;
typedef __attribute__((address_space(1))) u64 gu64;
typedef short bf16x8 __attribute__((ext_vector_type(8)));
typedef short s16x4 __attribute__((ext_vector_type(4)));
typedef __bf16 bf16x2_t __attribute__((ext_vector_type(2)));
typedef float f32x16 __attribute__((ext_vector_type(16)));
typedef float f32x4 __attribute__((ext_vector_type(4)));
typedef float f32x2 __attribute__((ext_vector_type(2)));
typedef unsigned u32x4 __attribute__((ext_vector_type(4)));
typedef unsigned u32x2 __attribute__((ext_vector_type(2)));
typedef LAS unsigned char* lds_p;
typedef const LAS unsigned char* lds_cp;
typedef __amdgpu_buffer_rsrc_t rsrc_t;

constexpr int SEQ = 2048, DM = 512, NH = 32, FH = 688, FF = 1376;
constexpr int NKS = 299;
constexpr int F_E = 0;
constexpr int F_S = 512;
constexpr int F_X = 1024;
constexpr int F_L = 2048;
constexpr int F_U = 3072;
constexpr int F_K = 3584;
constexpr int F_C = 4096;
constexpr int F_P = 5120;
constexpr unsigned WS_G   = 65536;
constexpr unsigned WS_XN  = 131072;
constexpr unsigned WS_QF  = WS_XN + SEQ * DM * 2;
constexpr unsigned WS_KVF = WS_QF + NH * 64 * 8192;
constexpr unsigned WS_Z2N = WS_KVF + NH * 64 * 16384;
constexpr unsigned WS_AF  = WS_Z2N + SEQ * 768 * 2;
constexpr unsigned WS_P   = WS_AF + 304 * 64 * 1024;
constexpr unsigned WS_END = WS_P + 256 * 65536;

#define MFMA32(a, b, c) __builtin_amdgcn_mfma_f32_32x32x16_bf16((a), (b), (c), 0, 0, 0)
#define SBAR() __builtin_amdgcn_sched_barrier(0)
#define PIN(x) asm volatile("" : "+v"(x))
#define WAIT_V(n) asm volatile("s_waitcnt vmcnt(" #n ")" ::: "memory")
#define WAIT_L(n) asm volatile("s_waitcnt lgkmcnt(" #n ")" ::: "memory")
#define BAR() asm volatile("s_barrier" ::: "memory")
#define SC1 16

DI unsigned cvtpk(float lo, float hi) { f32x2 v = {lo, hi}; bf16x2_t b = __builtin_convertvector(v, bf16x2_t); return __builtin_bit_cast(unsigned, b); }
DI float bflo(unsigned w) { return __uint_as_float(w << 16); }
DI float bfhi(unsigned w) { return __uint_as_float(w & 0xffff0000u); }
DI u32x4 pack8(const f32x16& a, int s, float sc) {
    u32x4 w;
    w[0] = cvtpk(a[8 * s + 0] * sc, a[8 * s + 1] * sc); w[1] = cvtpk(a[8 * s + 2] * sc, a[8 * s + 3] * sc);
    w[2] = cvtpk(a[8 * s + 4] * sc, a[8 * s + 5] * sc); w[3] = cvtpk(a[8 * s + 6] * sc, a[8 * s + 7] * sc);
    return w;
}
DI bf16x8 ld128(lds_cp p) { return *(const LAS bf16x8*)p; }
typedef short v4i16_t __attribute__((ext_vector_type(4)));
DI s16x4 ldtr(lds_cp p) { return __builtin_bit_cast(s16x4, __builtin_amdgcn_ds_read_tr16_b64_v4i16((LAS v4i16_t*)p)); }
DI bf16x8 ldtr2(lds_cp p, int step) { const s16x4 lo = ldtr(p), hi = ldtr(p + step); return __builtin_shufflevector(lo, hi, 0, 1, 2, 3, 4, 5, 6, 7); }
template <int I0, int I1> DI void dma_pair(unsigned v0, unsigned v1, const void* sbase, unsigned l) {
    asm volatile("s_add_u32 m0, %3, %4\n\ts_nop 0\n\tglobal_load_lds_dwordx4 %0, %2 sc1\n\ts_add_u32 m0, %3, %5\n\ts_nop 0\n\tglobal_load_lds_dwordx4 %1, %2 sc1"
                 :: "v"(v0), "v"(v1), "s"(sbase), "s"(l), "i"(I0), "i"(I1) : "memory", "scc");
}
template <int I, int O> DI void bdma1(unsigned voff, rsrc_t rs, unsigned soff, unsigned l) {
    asm volatile("s_add_u32 m0, %3, %4\n\ts_nop 0\n\tbuffer_load_dwordx4 %0, %1, %2 offen offset:%5 sc1 lds" :: "v"(voff), "s"(rs), "s"(soff), "s"(l), "i"(I), "i"(O) : "memory", "scc");
}
DI void dma_words(unsigned voff, const void* sbase, unsigned l) {
    asm volatile("s_mov_b32 m0, %2\n\ts_nop 0\n\tglobal_load_lds_dword %0, %1 sc1" :: "v"(voff), "s"(sbase), "s"(l) : "memory");
}
DI void st16(rsrc_t rs, unsigned off, u32x4 v) { __builtin_amdgcn_raw_buffer_store_b128(v, rs, off, 0, SC1); }
DI u32x4 ld16(rsrc_t rs, unsigned off) { return __builtin_amdgcn_raw_buffer_load_b128(rs, off, 0, SC1); }
DI float swap_add(float v) { auto r = __builtin_amdgcn_permlane32_swap(__float_as_uint(v), __float_as_uint(v), false, false); return __uint_as_float(r[0]) + __uint_as_float(r[1]); }
DI float swap_max(float v) { auto r = __builtin_amdgcn_permlane32_swap(__float_as_uint(v), __float_as_uint(v), false, false); return __builtin_fmaxf(__uint_as_float(r[0]), __uint_as_float(r[1])); }
DI f32x2 unpk(unsigned w) { return (f32x2){bflo(w), bfhi(w)}; }
DI unsigned cvt2(f32x2 v) { return __builtin_bit_cast(unsigned, __builtin_convertvector(v, bf16x2_t)); }
DI float sum_pair(unsigned w, float acc) { return __builtin_amdgcn_fdot2_f32_bf16(__builtin_bit_cast(bf16x2_t, w), __builtin_bit_cast(bf16x2_t, 0x3f803f80u), acc, false); }
DI unsigned gelu2(float a, float b) {
    const f32x2 v = {a, b};
    const f32x2 t = v * __builtin_elementwise_fma(v * v, (f32x2){-2.302208198f * 0.044715f, -2.302208198f * 0.044715f}, (f32x2){-2.302208198f, -2.302208198f});
    const f32x2 d = (f32x2){__builtin_amdgcn_exp2f(t[0]), __builtin_amdgcn_exp2f(t[1])} + (f32x2){1.0f, 1.0f};
    return cvt2(v * (f32x2){__builtin_amdgcn_rcpf(d[0]), __builtin_amdgcn_rcpf(d[1])});
}
DI u32x4 gelu8(const f32x16& a, int s) { return (u32x4){gelu2(a[8 * s], a[8 * s + 1]), gelu2(a[8 * s + 2], a[8 * s + 3]), gelu2(a[8 * s + 4], a[8 * s + 5]), gelu2(a[8 * s + 6], a[8 * s + 7])}; }
DI void set_flag(unsigned char* ws, int idx, u64 tag) { __hip_atomic_store((gu64*)ws + idx, tag, __ATOMIC_RELAXED, __HIP_MEMORY_SCOPE_AGENT); }
DI void poll_flags(unsigned char* ws, int idx, bool active, u64 tag) {
#pragma unroll 1
    for (unsigned spins = 0; spins < (1u << 17); ++spins) {
        const u64 v = __hip_atomic_load((gu64*)ws + idx, __ATOMIC_RELAXED, __HIP_MEMORY_SCOPE_AGENT);
        if (__all(!active || v == tag)) break;
        __builtin_amdgcn_s_sleep(4);
    }
}
#define ACQUIRE() asm volatile("buffer_inv sc1" ::: "memory")

constexpr int LDS_TAG = 163584, LDS_TOUCH = 163328, LDS_PARK = 163072;
DI void touch(unsigned voff, const void* sbase, unsigned lds0) { dma_words(voff, sbase, lds0 + LDS_TOUCH); }
template <int I> DI void dma_one(unsigned v0, const void* sbase, unsigned l) {
    asm volatile("s_add_u32 m0, %2, %3\n\ts_nop 0\n\tglobal_load_lds_dwordx4 %0, %1 sc1" :: "v"(v0), "s"(sbase), "s"(l), "i"(I) : "memory", "scc");
}
DI void z2_half(const lds_p lds, const unsigned lds0, f32x16 (&acc)[3], const bf16_t* __restrict__ x, const bf16_t* __restrict__ w_in, const unsigned char* fx, const int rg, const int hf,
                const int tid, const int wid, const int lane
                ) {
    constexpr int ZS = 704, ZST = 32 * ZS;
    constexpr unsigned WROW = FF * 2;
    const int row = tid >> 3, seg = tid & 7;
    const u32x4* src = (const u32x4*)(x + (size_t)(rg * 64 + row) * DM) + seg;
    u32x4 v[8];
#pragma unroll
    for (int j = 0; j < 8; ++j) asm volatile("global_load_dwordx4 %0, %1, off offset:%2" : "=v"(v[j]) : "v"(src), "i"(j * 128) : "memory");
    unsigned bv[3];
    const int p2 = wid < 6 ? wid + 16 : wid + 8;
#pragma unroll
    for (int i = 0; i < 3; ++i) { const int c = (i == 2 ? p2 : wid + 8 * i) * 64 + lane, zr = c / 44, zc = c - zr * 44; bv[i] = zr * WROW + (zc > 42 ? 42 : zc) * 16; }
    const unsigned lw = lds0 + 65536 + wid * 1024, lw2 = lds0 + 65536 + p2 * 1024;
    const char* wg = (const char*)w_in + FH * 2 + hf * FH;
#define Z_STAGE(slot, g_) do { dma_pair<(slot) * ZST, (slot) * ZST + 8192>(bv[0], bv[1], (g_), lw); dma_one<(slot) * ZST>(bv[2], (g_), lw2); } while (0)
    BAR();
    Z_STAGE(0, wg); Z_STAGE(1, wg + 32 * WROW); Z_STAGE(2, wg + 64 * WROW); Z_STAGE(3, wg + 96 * WROW);
#pragma unroll
    for (int i = 0; i < 2; ++i) { int c = (2 * wid + i) * 64 + lane; c = c > 895 ? 895 : c; const int zr = c / 7, zc = c - zr * 7; touch(((rg & 3) * 128 + zr) * WROW + (zc == 6 ? 684 : zc * 128), wg, lds0); }
    SBAR();
    {
        asm volatile("s_waitcnt vmcnt(14)" : "+v"(v[0]), "+v"(v[1]), "+v"(v[2]), "+v"(v[3]), "+v"(v[4]), "+v"(v[5]), "+v"(v[6]), "+v"(v[7]));
        float s = 0.f, s1 = 0.f;
#pragma unroll
        for (int j = 0; j < 8; ++j) { s = sum_pair(v[j][0], s); s1 = sum_pair(v[j][1], s1); s = sum_pair(v[j][2], s); s1 = sum_pair(v[j][3], s1); }
        s += s1;
        s += __shfl_xor(s, 1); s += __shfl_xor(s, 2); s += __shfl_xor(s, 4);
        const float mean = s * (1.0f / DM);
        f32x2 d[32], q2 = {0.f, 0.f}, q3 = {0.f, 0.f};
#pragma unroll
        for (int i = 0; i < 32; i += 2) { d[i] = unpk(v[i >> 2][i & 3]) - (f32x2){mean, mean}; d[i + 1] = unpk(v[i >> 2][(i & 3) + 1]) - (f32x2){mean, mean};
            q2 = __builtin_elementwise_fma(d[i], d[i], q2); q3 = __builtin_elementwise_fma(d[i + 1], d[i + 1], q3); }
        q2 += q3;
        float q = q2[0] + q2[1];
        q += __shfl_xor(q, 1); q += __shfl_xor(q, 2); q += __shfl_xor(q, 4);
        const float rstd = 1.0f / sqrtf(q * (1.0f / DM) + 1e-5f);
#pragma unroll
        for (int j = 0; j < 8; ++j) {
            u32x4 o;
#pragma unroll
            for (int e = 0; e < 4; ++e) o[e] = cvt2(d[4 * j + e] * (f32x2){rstd, rstd});
            *(LAS u32x4*)(lds + j * 8192 + row * 128 + ((seg ^ ((row >> 1) & 7)) << 4)) = o;
        }
    }
    const int r = lane & 31, hh = lane >> 5, blk = (lane >> 4) & 1, qq = (lane & 15) >> 2, p = lane & 3;
    const int asw = (r >> 1) & 7, oth = (wid & 1) ? -4096 : 4096;
    lds_cp ab[4];
#pragma unroll
    for (int k = 0; k < 4; ++k) ab[k] = lds + (wid & 1) * 4096 + r * 128 + (((2 * k + hh) ^ asw) << 4);
    const lds_cp bb = lds + 65536 + (8 * hh + qq) * ZS + wid * 64 + blk * 32 + p * 8;
    const lds_cp bx = bb + (8 + (wid >> 1) - wid) * 64;
    const bool w3 = wid < 6;
#pragma unroll
    for (int i = 0; i < 3; ++i) acc[i] = f32x16{};
    bf16x8 fa[2][2][2], fb[2][2][2];
#define Z_READ(set, slot, k0, aoff_) do { _Pragma("unroll") for (int k_ = 0; k_ < 2; ++k_) { \
        fa[set][k_][0] = ld128(ab[(k0) + k_] + (aoff_)); fa[set][k_][1] = ld128(ab[(k0) + k_] + oth + (aoff_)); \
        fb[set][k_][0] = ldtr2(bb + (slot) * ZST + k_ * 16 * ZS, 4 * ZS); if (w3) fb[set][k_][1] = ldtr2(bx + (slot) * ZST + k_ * 16 * ZS, 4 * ZS); } } while (0)
#define Z_MMA(set, k_) do { acc[0] = MFMA32(fb[set][k_][0], fa[set][k_][0], acc[0]); acc[1] = MFMA32(fb[set][k_][0], fa[set][k_][1], acc[1]); \
        if (w3) acc[2] = MFMA32(fb[set][k_][1], fa[set][k_][0], acc[2]); } while (0)
#define Z_SYNC(n) asm volatile("s_waitcnt vmcnt(" #n ") lgkmcnt(0)\n\ts_barrier" ::: "memory")
    Z_SYNC(11);
    Z_READ(0, 0, 0, 0);
    const char* g = wg + 128 * WROW;
#pragma unroll 1
    for (int kt = 0; kt < 3; ++kt) {
        Z_SYNC(6); Z_READ(1, 1, 2, 0);     SBAR(); Z_MMA(0, 0); SBAR(); Z_STAGE(0, g);             SBAR(); Z_MMA(0, 1); SBAR();
        Z_SYNC(6); Z_READ(0, 2, 0, 8192);  SBAR(); Z_MMA(1, 0); SBAR(); Z_STAGE(1, g + 32 * WROW); SBAR(); Z_MMA(1, 1); SBAR();
        Z_SYNC(6); Z_READ(1, 3, 2, 8192);  SBAR(); Z_MMA(0, 0); SBAR(); Z_STAGE(2, g + 64 * WROW); SBAR(); Z_MMA(0, 1); SBAR();
        Z_SYNC(6); Z_READ(0, 0, 0, 16384); SBAR(); Z_MMA(1, 0); SBAR(); Z_STAGE(3, g + 96 * WROW); SBAR(); Z_MMA(1, 1); SBAR();
        g += 128 * WROW;
#pragma unroll
        for (int k = 0; k < 4; ++k) ab[k] += 16384;
    }
    if (wid == 0) dma_words(lane * 4, fx, lds0 + LDS_PARK);
    Z_SYNC(6); Z_READ(1, 1, 2, 0);    SBAR(); Z_MMA(0, 0); Z_MMA(0, 1); SBAR();
    Z_SYNC(3); Z_READ(0, 2, 0, 8192); SBAR(); Z_MMA(1, 0); Z_MMA(1, 1); SBAR();
    Z_SYNC(0); Z_READ(1, 3, 2, 8192); SBAR(); Z_MMA(0, 0); Z_MMA(0, 1); SBAR();
    Z_SYNC(0);                                Z_MMA(1, 0); Z_MMA(1, 1);
#undef Z_STAGE
#undef Z_READ
#undef Z_MMA
#undef Z_SYNC
}

struct Run { const char* A; const char* B; unsigned dA1, dA2, dB1, dB2, vA0, vA1, vB0, vB1; };
constexpr int HTB = 16384;
constexpr unsigned LDA = DM * 2, LDB = 4096 * 2;
#define G_SA(b, h) (((b) * 2 + (h)) * HTB)
#define G_SB(b, h) ((4 + (b) * 2 + (h)) * HTB)
#define G_STAGE(bufoff, gb, v0, v1) dma_pair<(bufoff), (bufoff) + 8192>((v0), (v1), (gb), ldsw)
#define G_LDA(dst, b, h) do { _Pragma("unroll") for (int m_ = 0; m_ < 2; ++m_) _Pragma("unroll") for (int k_ = 0; k_ < 4; ++k_) dst[m_][k_] = ld128(lds + G_SA(b, h) + aoff[k_] + m_ * 4096); } while (0)
#define G_LDB(dst, b, h) do { _Pragma("unroll") for (int k_ = 0; k_ < 4; ++k_) dst[k_] = ldtr2(lds + G_SB(b, h) + boff + k_ * 4096, 1024); } while (0)
#define G_MMA(ai, bj, At, Bt) do { __builtin_amdgcn_s_setprio(1); _Pragma("unroll") for (int k_ = 0; k_ < 4; ++k_) _Pragma("unroll") for (int m_ = 0; m_ < 2; ++m_) \
        acc[ai][bj][m_] = SWAP ? MFMA32(At[m_][k_], Bt[k_], acc[ai][bj][m_]) : MFMA32(Bt[k_], At[m_][k_], acc[ai][bj][m_]); __builtin_amdgcn_s_setprio(0); } while (0)

DI void g_prologue_b(const Run& u, const unsigned ldsw) {
    G_STAGE(G_SB(0, 0), u.B, u.vB0, u.vB1); G_STAGE(G_SB(0, 1), u.B + u.dB1, u.vB0, u.vB1);
}
DI void g_prologue_rest(const Run& u, const unsigned ldsw) {
    G_STAGE(G_SA(0, 0), u.A, u.vA0, u.vA1); G_STAGE(G_SA(0, 1), u.A + u.dA1, u.vA0, u.vA1);
    G_STAGE(G_SB(1, 0), u.B + u.dB2, u.vB0, u.vB1); G_STAGE(G_SA(1, 0), u.A + u.dA2, u.vA0, u.vA1); G_STAGE(G_SB(1, 1), u.B + u.dB2 + u.dB1, u.vB0, u.vB1);
}
template <int LO> DI void check_parked(const int HI, lds_cp parked, unsigned char* ws, const int fidx, const u64 tag, const int lane) {
    const unsigned w = *(const LAS unsigned*)(parked + lane * 4);
    const int sl = (lane >> 1) & 15;
    if (!__all(sl < LO || sl >= HI || w == ((lane & 1) ? (unsigned)(tag >> 32) : (unsigned)tag))) poll_flags(ws, fidx + (lane & 31), lane < 32 && (lane & 15) >= LO && (lane & 15) < HI, tag);
}

template <bool SWAP, int ROUND>
DI void g_kloop(const lds_p lds, const unsigned lds0, f32x16 (&acc)[2][2][2], const Run& cur, const Run& nxt, const int (&aoff)[4], const int boff, const unsigned ldsw,
                unsigned char* ws, const u64 tag, const int fl0, const int fatt, const int lane, const int wid, const unsigned tch) {
    bf16x8 At[2][4], B0[4], B1[4];
#pragma unroll
    for (int i = 0; i < 8; ++i) acc[i >> 2][(i >> 1) & 1][i & 1] = f32x16{};
#pragma unroll 1
    for (int t = 0; t < 8; t += 2) {
        const bool last = (t == 6);
        const char* a1 = cur.A + (t + 1) * 128 + 128 * LDA;
        const char* a2 = last ? nxt.A : cur.A + (t + 2) * 128;
        const char* b2 = last ? nxt.B : cur.B + (size_t)(t + 2) * 64 * LDB;
        const unsigned dA1 = last ? nxt.dA1 : cur.dA1, dA2 = last ? nxt.dA2 : cur.dA2, dB1 = last ? nxt.dB1 : cur.dB1, dB2 = last ? nxt.dB2 : cur.dB2;
        const unsigned vA0 = last ? nxt.vA0 : cur.vA0, vA1 = last ? nxt.vA1 : cur.vA1, vB0 = last ? nxt.vB0 : cur.vB0, vB1 = last ? nxt.vB1 : cur.vB1;
        if (ROUND == 1 && last && wid == 0) dma_words(lane * 4, ws + (size_t)fatt * 8, lds0 + 131072);
        if (t == 2 && wid == 0 && lane == 0) set_flag(ws, fl0, tag);
        if (t == 0) G_LDB(B0, 0, 0);
        SBAR(); G_LDA(At, 0, 0); if (!(ROUND == 1 && t == 0)) G_STAGE(G_SA(1, 1), a1, cur.vA0, cur.vA1);
        if (ROUND == 0 && t == 2) touch(tch, nxt.A, lds0);
        if (ROUND == 1 && t == 4 && wid == 0) { check_parked<0>(8, lds + 131072, ws, fatt, tag, lane); ACQUIRE(); }
        WAIT_L(8); BAR(); WAIT_L(0); SBAR(); G_MMA(0, 0, At, B0); SBAR(); BAR();
        G_LDB(B1, 0, 1);
        G_STAGE(G_SB(0, 0), b2, vB0, vB1);
        BAR(); WAIT_L(0); SBAR(); G_MMA(0, 1, At, B1); SBAR(); BAR();
        G_LDA(At, 0, 1); G_STAGE(G_SA(0, 0), a2, vA0, vA1);
        if (ROUND == 1 && t == 0) WAIT_V(26); else WAIT_V(10);
        BAR(); WAIT_L(0); SBAR(); G_MMA(1, 0, At, B0); SBAR(); BAR();
        G_LDB(B0, 1, 0); G_STAGE(G_SB(0, 1), b2 + dB1, vB0, vB1);
        if (ROUND == 1 && t == 0) WAIT_V(22); else if ((ROUND == 1 && t == 4 && wid == 0) || (ROUND == 0 && t == 2)) WAIT_V(7); else WAIT_V(6);
        BAR(); SBAR(); G_MMA(1, 1, At, B1); SBAR(); BAR();
        if (ROUND == 1 && wid == 0) {
            if (t == 2) dma_words(lane * 4, ws + (size_t)fatt * 8, lds0 + 131072);
            if (last) check_parked<8>(12, lds + 131072, ws, fatt, tag, lane);
        }
        G_LDA(At, 1, 0); G_STAGE(G_SA(0, 1), a2 + dA1, vA0, vA1);
        BAR(); WAIT_L(0); SBAR(); G_MMA(0, 0, At, B0); SBAR(); BAR();
        G_LDB(B1, 1, 1); G_STAGE(G_SB(1, 0), b2 + dB2, vB0, vB1);
        BAR(); WAIT_L(0); SBAR(); G_MMA(0, 1, At, B1); SBAR(); BAR();
        G_LDA(At, 1, 1); G_STAGE(G_SA(1, 0), a2 + dA2, vA0, vA1);
        if (!last) WAIT_V(10);
        BAR(); WAIT_L(0); SBAR(); G_MMA(1, 0, At, B0); SBAR(); BAR();
        if (!last) G_LDB(B0, 0, 0);
        G_STAGE(G_SB(1, 1), b2 + dB2 + dB1, vB0, vB1);
        if (!(ROUND == 1 && last)) WAIT_V(6);
        BAR(); SBAR(); G_MMA(1, 1, At, B1); SBAR();
        if (!(last && wid >= 4)) BAR();
    }
}

DI void g_epilogue(const f32x16 (&acc)[2][2][2], const rsrc_t rws, const int type, const int pn, const int pm, const int wr, const int wc, const int lane) {
    constexpr float QS = 0.08838834764831845f * 1.4426950408889634f;
    const float sc = type == 2 ? QS : 1.0f;
    const unsigned base = (type == 2 ? WS_QF : WS_KVF + (type == 1 ? 8192 : 0)) + wc * 2048 + lane * 16;
    const unsigned hstride = type == 2 ? 8192 : 16384;
#pragma unroll
    for (int ai = 0; ai < 2; ++ai)
#pragma unroll
        for (int bj = 0; bj < 2; ++bj)
#pragma unroll
            for (int m = 0; m < 2; ++m) {
                const unsigned dst = base + (unsigned)((2 * pn + bj) * 64 + pm * 8 + ai * 4 + wr * 2 + m) * hstride;
                st16(rws, dst, pack8(acc[ai][bj][m], 0, sc)); st16(rws, dst + 1024, pack8(acc[ai][bj][m], 1, sc));
            }
}

#define NOP_ (void)0
#define SP_CHECK() if (t == 36) { poll_flags(ws, fatt + lane, (lane & 15) < 12, tag); ACQUIRE(); }
#define EX2(v) __builtin_amdgcn_exp2f(v)
#define MX3(a, b, c) __builtin_fmaxf(__builtin_fmaxf((a), (b)), (c))
constexpr float ATT_THR = 32.0f;
DI void att_mask(f32x16& s, int dq) {
#pragma unroll
    for (int e = 0; e < 16; ++e) if ((e & 3) + 8 * (e >> 2) > dq) s[e] = -__builtin_inff();
}

struct OutArgs { rsrc_t ra; const char* wo; const char* wu; lds_cp rd[4]; unsigned wv, uv[6], va, sc, su, ldsw; };
template <int T> DI void out_w(const OutArgs& o) {
    constexpr int S = (T % 9) * 16384;
    if constexpr (T < 16) dma_pair<S, S + 8192>(o.wv, o.wv + 32768, o.wo + T * 65536, o.ldsw);
    else if constexpr (T < 19) dma_pair<S, S + 8192>(o.uv[2 * (T - 16)], o.uv[2 * (T - 16) + 1], o.wu, o.ldsw);
}
template <int T> DI void out_a(const OutArgs& o, u32x4 (&af)[6][2]) {
    if constexpr (T < 19) {
        const unsigned so = T < 16 ? o.sc + T * 262144 : o.su + (T - 16) * 262144;
        asm volatile("buffer_load_dwordx4 %0, %1, %2, %3 offen sc1" : "=v"(af[T % 6][0]) : "v"(o.va), "s"(o.ra), "s"(so) : "memory");
        asm volatile("buffer_load_dwordx4 %0, %1, %2, %3 offen sc1" : "=v"(af[T % 6][1]) : "v"(o.va), "s"(o.ra), "s"(so + 65536) : "memory");
    }
}
constexpr int out_cnt(int i) { return (i + 7 < 19 ? 2 : 0) + (i + 5 < 19 ? 2 : 0); }
constexpr int out_allow(int t) { int n = t < 4 ? 2 * (4 - t) : 0; for (int i = t < 4 ? 0 : t - 4; i < t; ++i) n += out_cnt(i); return n; }
template <int T> DI void out_step(const OutArgs& o, u32x4 (&af)[6][2], bf16x8 (&wf)[2][4], f32x16 (&acc)[4]) {
    asm volatile("s_waitcnt vmcnt(%2)" : "+v"(af[T % 6][0]), "+v"(af[T % 6][1]) : "n"(out_allow(T)) : "memory");
    if constexpr (T % 2 == 0) BAR();
    const bf16x8 a0 = __builtin_bit_cast(bf16x8, af[T % 6][0]), a1 = __builtin_bit_cast(bf16x8, af[T % 6][1]);
#pragma unroll
    for (int c = 0; c < 4; ++c) wf[1][c] = ldtr2(o.rd[c] + (T % 9) * 16384 + 4096, 2048);
    SBAR();
    acc[0] = MFMA32(wf[0][0], a0, acc[0]); acc[1] = MFMA32(wf[0][1], a0, acc[1]); SBAR();
    out_w<T + 7>(o); SBAR();
    acc[2] = MFMA32(wf[0][2], a0, acc[2]); acc[3] = MFMA32(wf[0][3], a0, acc[3]); SBAR();
    if constexpr (T < 18) {
#pragma unroll
        for (int c = 0; c < 4; ++c) wf[0][c] = ldtr2(o.rd[c] + ((T + 1) % 9) * 16384, 2048);
    }
    SBAR();
    acc[0] = MFMA32(wf[1][0], a1, acc[0]); acc[1] = MFMA32(wf[1][1], a1, acc[1]); SBAR();
    out_a<T + 5>(o, af); SBAR();
    acc[2] = MFMA32(wf[1][2], a1, acc[2]); acc[3] = MFMA32(wf[1][3], a1, acc[3]); SBAR();
}

#define IT(h1, i1, h2, i2) ((h1) | (i1) << 2 | (h2) << 6 | (i2) << 8)
__device__ constexpr unsigned ITEMS[32] = {
    IT(0, 15, 0, 4), IT(1, 15, 1, 4), IT(2, 15, 2, 4), IT(3, 15, 3, 4), IT(0, 13, 0, 5), IT(1, 13, 1, 5), IT(2, 13, 2, 5), IT(3, 13, 3, 5),
    IT(0, 12, 0, 6), IT(1, 12, 1, 6), IT(2, 12, 2, 6), IT(3, 12, 3, 6), IT(0, 11, 0, 7), IT(1, 11, 1, 7), IT(2, 11, 2, 7), IT(3, 11, 3, 7),
    IT(0, 14, 0, 3), IT(1, 14, 1, 3), IT(2, 14, 2, 3), IT(3, 14, 3, 3), IT(0, 9, 0, 8), IT(1, 9, 1, 8), IT(2, 9, 2, 8), IT(3, 9, 3, 8),
    IT(0, 2, 0, 0), IT(1, 2, 1, 0), IT(2, 2, 2, 0), IT(3, 2, 3, 0), IT(0, 1, 1, 1), IT(2, 1, 3, 1), IT(0, 10, 1, 10), IT(2, 10, 3, 10)
};

__global__ __launch_bounds__(512, 2) void fused_fwd(const bf16_t* __restrict__ x, const bf16_t* __restrict__ ckv, const bf16_t* __restrict__ wq, const bf16_t* __restrict__ wk,
                                                    const bf16_t* __restrict__ wv, const bf16_t* __restrict__ wo, const bf16_t* __restrict__ w_in, const bf16_t* __restrict__ wsp,
                                                    const bf16_t* __restrict__ bsp, const bf16_t* __restrict__ wout, bf16_t* __restrict__ out, unsigned char* ws) {
    __shared__ __attribute__((aligned(16))) unsigned char smem[163840];
    const lds_p lds = (lds_p)smem;
    const unsigned lds0 = (unsigned)(uintptr_t)smem;
    const int tid = threadIdx.x, wid = __builtin_amdgcn_readfirstlane(tid >> 6), lane = tid & 63;
    const int bid = blockIdx.x, x8 = bid & 7, kk = bid >> 3;
    const int r = lane & 31, hh = lane >> 5, blk = (lane >> 4) & 1, qq = (lane & 15) >> 2, p = lane & 3;
    const rsrc_t rws = __builtin_amdgcn_make_buffer_rsrc((void*)ws, (short)0, WS_END, 0x00020000);
    const unsigned ldsw = lds0 + (unsigned)wid * 1024u;
    dma_words(lane * 4, ws, lds0 + LDS_TAG);
    u64 tag;
#define MAKE_TAG() do { const u32x2 e_ = *(const LAS u32x2*)(lds + LDS_TAG); \
        const u64 ep_ = ((u64)(unsigned)__builtin_amdgcn_readfirstlane(e_[1]) << 32) | (unsigned)__builtin_amdgcn_readfirstlane(e_[0]); \
        u64 z_ = ep_ + 0x9E3779B97F4A7C15ull; z_ = (z_ ^ (z_ >> 30)) * 0xBF58476D1CE4E5B9ull; z_ = (z_ ^ (z_ >> 27)) * 0x94D049BB133111EBull; z_ ^= z_ >> 31; tag = z_ == ep_ ? ~z_ : z_; } while (0)
    const unsigned item = ITEMS[kk];
    const int h1 = item & 3, i1 = (item >> 2) & 15, h2 = (item >> 6) & 3, i2 = (item >> 8) & 15;
    const unsigned char* kvx = ws + WS_KVF + (size_t)x8 * 4 * 64 * 16384;
    const unsigned char* kvh = kvx + (size_t)h1 * 64 * 16384;
    const char* q1src = (const char*)ws + WS_QF + (size_t)((4 * x8 + h1) * 64 + 4 * i1 + (wid & 3)) * 8192;
    bf16x8 q[8];
#define Q1_LOAD() do { _Pragma("unroll") for (int d = 0; d < 8; ++d) asm volatile("global_load_dwordx4 %0, %1, %2 offset:%3 sc1" : "=v"(q[d]) : "v"(lane * 16 + (d >> 2) * 4096), "s"(q1src), "i"((d & 3) * 1024) : "memory"); } while (0)
#define Q1_WAIT(n) asm volatile("s_waitcnt vmcnt(" #n ")" : "+v"(q[0]), "+v"(q[1]), "+v"(q[2]), "+v"(q[3]), "+v"(q[4]), "+v"(q[5]), "+v"(q[6]), "+v"(q[7]) :: "memory")
    const int fatt = F_K + x8 * 64;
#define Q2_LOAD() do { const char* q2src = (const char*)ws + WS_QF + (size_t)((4 * x8 + h2) * 64 + 4 * i2) * 8192 + wid * 4096; \
        const unsigned lq_ = __builtin_amdgcn_readfirstlane(lds0 + wid * 4096); \
        dma_pair<131072, 131072 + 1024>(lane * 16, lane * 16 + 1024, q2src, lq_); dma_pair<131072 + 2048, 131072 + 3072>(lane * 16 + 2048, lane * 16 + 3072, q2src, lq_); } while (0)
    Run rat;
    rat.A = (const char*)kvh; rat.B = (const char*)kvh + 32768; rat.dA1 = rat.dB1 = 65536; rat.dA2 = rat.dB2 = 8192;
    rat.vA0 = rat.vB0 = wid * 1024 + lane * 16; rat.vA1 = rat.vB1 = rat.vA0 + 16384;
    int pf;
    int aoff[4];
    const unsigned arow = 8 * wid + (lane >> 3), achk = ((lane & 7) ^ (((wid & 1) << 2) + (lane >> 4))) << 4;
    const unsigned brow = 4 * wid + (lane >> 4), bchk = ((lane & 15) ^ (((lane >> 4) & 3) << 2)) << 4;

    if (kk < 24) {
        const int type = kk >> 3, pn = 2 * x8 + ((kk >> 2) & 1), pm0 = (kk & 3) + (type == 2 ? 4 : 0), pm1 = pm0 ^ 4, wr = wid >> 2, wc = wid & 3;
        Run u0, u1;
        const char* Ab = type == 2 ? (const char*)ws + WS_XN : (const char*)ckv;
        u0.B = (const char*)(type == 0 ? wk : type == 1 ? wv : wq) + pn * 512; u0.A = Ab + (size_t)pm0 * 256 * LDA;
        u0.dA1 = 128 * LDA; u0.dA2 = 128; u0.dB1 = 256; u0.dB2 = 64 * LDB;
        u0.vA0 = arow * LDA + achk; u0.vA1 = u0.vA0 + 64 * LDA; u0.vB0 = brow * LDB + bchk; u0.vB1 = u0.vB0 + 32 * LDB;
        u1 = u0; u1.A = Ab + (size_t)pm1 * 256 * LDA;
        const unsigned tch = (type == 2 ? ((kk >> 2) & 1) * 2 : type * 2 + ((kk >> 2) & 1)) * 65536 + tid * 128, bl = ((kk & 3) * 128 + wid * 16 + (lane >> 2)) * LDB + (lane & 3) * 128;
        if (type < 2) {
            g_prologue_b(u0, ldsw); g_prologue_rest(u0, ldsw);
            WAIT_V(6);
            MAKE_TAG();
        } else {
            const int j = x8 * 8 + (kk - 16), row = 32 * j + 4 * wid + (lane >> 4);
            const u32x4* src = (const u32x4*)(x + (size_t)row * DM) + (lane & 15);
            u32x4 v[4];
#pragma unroll
            for (int i = 0; i < 4; ++i) v[i] = src[16 * i];
            SBAR();
            g_prologue_b(u0, ldsw); touch(bl, u0.B, lds0);
            SBAR();
            float s = 0.f;
#pragma unroll
            for (int i = 0; i < 4; ++i)
#pragma unroll
                for (int e = 0; e < 4; ++e) s += bflo(v[i][e]) + bfhi(v[i][e]);
            s += __shfl_xor(s, 1); s += __shfl_xor(s, 2); s += __shfl_xor(s, 4); s += __shfl_xor(s, 8);
            const float mean = s * (1.0f / DM);
            float q = 0.f;
#pragma unroll
            for (int i = 0; i < 4; ++i)
#pragma unroll
                for (int e = 0; e < 4; ++e) { const float a = bflo(v[i][e]) - mean, b = bfhi(v[i][e]) - mean; q += a * a + b * b; }
            q += __shfl_xor(q, 1); q += __shfl_xor(q, 2); q += __shfl_xor(q, 4); q += __shfl_xor(q, 8);
            const float rstd = 1.0f / sqrtf(q * (1.0f / DM) + 1e-5f);
#pragma unroll
            for (int i = 0; i < 4; ++i) {
                u32x4 o;
#pragma unroll
                for (int e = 0; e < 4; ++e) o[e] = cvtpk((bflo(v[i][e]) - mean) * rstd, (bfhi(v[i][e]) - mean) * rstd);
                st16(rws, WS_XN + row * 1024 + ((lane & 15) + 16 * i) * 16, o);
            }
            WAIT_V(0);
            MAKE_TAG();
            BAR();
            if (tid == 0) set_flag(ws, F_X + j, tag);
            if (wid == 0) { poll_flags(ws, F_X + ((lane & 8) ? pm1 : pm0) * 8 + (lane & 7), lane < 16, tag); ACQUIRE(); WAIT_V(0); }
            BAR();
            g_prologue_rest(u0, ldsw);
            WAIT_V(6);
        }
#pragma unroll
        for (int k = 0; k < 4; ++k) aoff[k] = (wr * 64 + r) * 128 + (((2 * k + hh) ^ ((r >> 1) & 7)) << 4);
        const int boff = (8 * hh + qq) * 256 + ((wc ^ qq) << 6) + blk * 32 + p * 8;
        BAR();
        if (wr == 1) BAR();
        f32x16 acc[2][2][2];
        const int fk = fatt + (pn & 1) * 16 + type * 4 + (kk & 3);
        if (type == 1) g_kloop<true, 0>(lds, lds0, acc, u0, u1, aoff, boff, ldsw, ws, tag, F_S + bid, fatt, lane, wid, tch);
        else           g_kloop<false, 0>(lds, lds0, acc, u0, u1, aoff, boff, ldsw, ws, tag, F_S + bid, fatt, lane, wid, tch);
        G_STAGE(G_SA(1, 1), u1.A + 128 + 128 * LDA, u1.vA0, u1.vA1);
        SBAR();
        g_epilogue(acc, rws, type, pn, pm0, wr, wc, lane);
        if (wr == 1) BAR();
        if (type == 1) g_kloop<true, 1>(lds, lds0, acc, u1, rat, aoff, boff, ldsw, ws, tag, fk, fatt, lane, wid, tch);
        else           g_kloop<false, 1>(lds, lds0, acc, u1, rat, aoff, boff, ldsw, ws, tag, fk, fatt, lane, wid, tch);
        Q1_LOAD();
        G_STAGE(G_SA(1, 1), rat.A + rat.dA1 + rat.dA2, rat.vA0, rat.vA1);
        SBAR();
        g_epilogue(acc, rws, type, pn, pm1, wr, wc, lane);
        pf = fk + 32;
        Q1_WAIT(18);
    } else {
        const int pm = x8, pn = kk - 24, grp = wid >> 2;
        constexpr unsigned SLA = SEQ * 2, SLB = 768 * 2, WROW = FF * 2;
        constexpr int RB = 65536, RA2 = 131072;
        const char* Ap = (const char*)ws + WS_XN + (size_t)pm * 256 * LDA;
        const char* Bp = (const char*)w_in + pn * 256;
        unsigned vA0 = arow * LDA + achk, vA1 = vA0 + 64 * LDA, vB0 = brow * WROW + bchk, vB1 = vB0 + 32 * WROW, dA = 128 * LDA, dB = 64 * WROW;
        const int tb0 = 8 * pm + 2 * (wid & 3), fs0 = 8 * pn + 4 * grp;
        float bias[2];
        const int flz = F_L + (4 * x8 + (pn >> 1)) * 2 + (kk & 1);
        {
            f32x16 za[3];
            const int rg = 4 * x8 + (pn >> 1), hf = kk & 1, m0_ = wid & 1;
#pragma unroll
            for (int m = 0; m < 2; ++m) bias[m] = __uint_as_float((unsigned)bsp[(tb0 + m) * 32 + r] << 16);
            z2_half(lds, lds0, za, x, w_in, ws + (F_X + 8 * pm) * 8, rg, hf, tid, wid, lane
                );
            PIN(bias[0]); PIN(bias[1]);
            MAKE_TAG();
            if (wid == 0 && kk < 30) {
                const unsigned w = *(const LAS unsigned*)(lds + LDS_PARK + lane * 4);
                if (!__all(lane >= 16 || w == ((lane & 1) ? (unsigned)(tag >> 32) : (unsigned)tag))) poll_flags(ws, F_X + 8 * pm + (lane & 7), true, tag);
                ACQUIRE();
            }
            {
                const char* slab = (const char*)wsp + (size_t)x8 * 1048576 + pn * 131072;
                touch(tid * 128, slab, lds0); touch(tid * 128 + 65536, slab, lds0);
            }
            u32x4 zp[3][2];
#pragma unroll
            for (int i = 0; i < 3; ++i) { zp[i][0] = gelu8(za[i], 0); zp[i][1] = gelu8(za[i], 1); }
            const int nx = wid < 4 ? 8 : wid < 6 ? 6 : 0;
            LAS float* red = (LAS float*)lds;
            float s[2] = {0.f, 0.f}, mean[2];
#pragma unroll
            for (int i = 0; i < 24; ++i) if (i < 16 || i - 16 < nx) s[(i >> 3) & 1] = sum_pair(zp[i >> 3][(i >> 2) & 1][i & 3], s[(i >> 3) & 1]);
            red[wid * 128 + m0_ * 64 + lane] = s[0]; red[wid * 128 + (m0_ ^ 1) * 64 + lane] = s[1];
            WAIT_L(0); BAR();
#pragma unroll
            for (int m = 0; m < 2; ++m) { float t = 0.f;
#pragma unroll
                for (int w = 0; w < 8; ++w) t += red[w * 128 + (m0_ ^ m) * 64 + lane];
                mean[m] = swap_add(t) * (2.0f / FH); }
            f32x2 d[24], q2[2] = {{0.f, 0.f}, {0.f, 0.f}};
#pragma unroll
            for (int i = 0; i < 24; ++i) { const int m = (i >> 3) & 1; d[i] = unpk(zp[i >> 3][(i >> 2) & 1][i & 3]) - (f32x2){mean[m], mean[m]};
                if (i < 16 || i - 16 < nx) q2[m] = __builtin_elementwise_fma(d[i], d[i], q2[m]); }
            red[1024 + wid * 128 + m0_ * 64 + lane] = q2[0][0] + q2[0][1]; red[1024 + wid * 128 + (m0_ ^ 1) * 64 + lane] = q2[1][0] + q2[1][1];
            WAIT_V(2); WAIT_L(0); BAR();
            LAS f32x2* tab = (LAS f32x2*)(lds + 160768);
            if (wid == 0) {
#pragma unroll
                for (int m = 0; m < 2; ++m) { float t = 0.f;
#pragma unroll
                    for (int w = 0; w < 8; ++w) t += red[1024 + w * 128 + m * 64 + lane];
                    s[m] = swap_add(t); }
                const float mo = hh ? mean[1] : mean[0], qo = hh ? s[1] : s[0];
                const unsigned t32 = (unsigned)tag;
                st16(rws, WS_G + (rg * 2 + hf) * 1024 + lane * 16, (u32x4){__float_as_uint(mo), t32, __float_as_uint(qo), t32});
                u32x4 o;
#pragma unroll 1
                for (unsigned spins = 0; spins < (1u << 17); ++spins) {
                    o = ld16(rws, WS_G + (rg * 2 + (hf ^ 1)) * 1024 + lane * 16);
                    if (__all(o[1] == t32 && o[3] == t32)) break;
                    __builtin_amdgcn_s_sleep(2);
                }
                const float mp = __uint_as_float(o[0]), dl = mo - mp;
                tab[lane] = (f32x2){0.5f * (mo + mp), 1.0f / sqrtf((qo + __uint_as_float(o[2]) + dl * dl * (FH * 0.25f)) * (1.0f / FH) + 1e-5f)};
            }
            WAIT_L(0); BAR();
            if (kk < 30) {
                dma_pair<0, 8192>(vA0, vA1, Ap, ldsw); dma_pair<16384, 24576>(vA0, vA1, Ap + 128 * LDA, ldsw); dma_pair<RB, RB + 8192>(vB0, vB1, Bp, ldsw);
                dma_pair<32768, 32768 + 8192>(vA0, vA1, Ap + 128, ldsw); dma_pair<32768 + 16384, 32768 + 24576>(vA0, vA1, Ap + 128 + 128 * LDA, ldsw);
                dma_pair<RB + 16384, RB + 16384 + 8192>(vB0, vB1, Bp + 64 * WROW, ldsw); dma_pair<RB + 32768, RB + 32768 + 8192>(vB0, vB1, Bp + 128 * WROW, ldsw);
            }
            f32x2 rs[2], of[2];
#pragma unroll
            for (int m = 0; m < 2; ++m) { const f32x2 t = tab[(m0_ ^ m) * 32 + r]; const float o = (mean[m] - t[0]) * t[1]; rs[m] = (f32x2){t[1], t[1]}; of[m] = (f32x2){o, o}; }
            const lds_p tr = lds + 114688;
#pragma unroll
            for (int i = 0; i < 12; ++i)
                if (i < 8 || 2 * (i - 8) < nx) {
                    const int b = i >> 2, m = b & 1;
                    *(LAS u32x2*)(tr + ((m0_ ^ m) * 32 + r) * 720 + (b == 2 ? 8 + (wid >> 1) : wid) * 64 + (i & 3) * 16 + hh * 8) =
                        (u32x2){cvt2(__builtin_elementwise_fma(d[2 * i], rs[m], of[m])), cvt2(__builtin_elementwise_fma(d[2 * i + 1], rs[m], of[m]))};
                }
            WAIT_L(0); BAR();
#pragma unroll
            for (int i = 0; i < 6; ++i) { int c = tid + 512 * i; const bool ok = c < 2752; c = ok ? c : 0; const int zr = c / 43, zc = c - zr * 43;
                st16(rws, ok ? WS_Z2N + (rg * 64 + zr) * SLB + hf * 688 + zc * 16 : 0x80000000u, *(const LAS u32x4*)(tr + zr * 720 + zc * 16)); }
#pragma unroll
            for (int i = 0; i < 2; ++i) { const int c = tid + 512 * i, zr = c / 10;
                st16(rws, hf == 1 && c < 640 ? WS_Z2N + (rg * 64 + zr) * SLB + 1376 + (c - zr * 10) * 16 : 0x80000000u, (u32x4){0u, 0u, 0u, 0u}); }
        }
        if (kk < 30) {
            u32x4 z1[2][2][2];
            const bool busy = fs0 < 43;
#pragma unroll
            for (int k = 0; k < 4; ++k) aoff[k] = ((wid >> 1) & 1) * 16384 + ((wid & 1) * 64 + r) * 128 + (((2 * k + hh) ^ ((r >> 1) & 7)) << 4);
            const int bo0 = RB + (8 * hh + qq) * 256 + (((2 * grp) ^ qq) << 6) + blk * 32 + p * 8, bo1 = RB + (8 * hh + qq) * 256 + (((2 * grp + 1) ^ qq) << 6) + blk * 32 + p * 8;
            f32x16 acc[2][2];
#pragma unroll
            for (int c = 0; c < 4; ++c) acc[c >> 1][c & 1] = f32x16{};
#pragma unroll
            for (int c = 0; c < 8; ++c) z1[c >> 2][(c >> 1) & 1][c & 1] = (u32x4){0u, 0u, 0u, 0u};
            Ap += 256; Bp += 192 * WROW;
            unsigned sa = 0, sa1 = 32768, sa2 = RA2;
            unsigned la = ldsw + RA2, la1 = ldsw, la2 = ldsw + 32768, lb = 49152;
            const char* Aq = Ap; unsigned lq = la, lq1 = la1, lq2 = la2, vQ0 = vA0, vQ1 = vA1, dQ = dA;
#define SP_SRC(s_) do { if ((s_) == 6) { Ap = (const char*)wsp + (size_t)pm * 256 * SLA; vA1 = arow * SLA + achk + 64 * SLA; dA = 128 * SLA; } \
                if ((s_) == 5) { Bp = (const char*)ws + WS_Z2N + pn * 256; vB0 = brow * SLB + bchk; vB1 = vB0 + 32 * SLB; dB = 64 * SLB; } \
                if (t == 6) { Aq = (const char*)wsp + (size_t)pm * 256 * SLA; vQ0 = arow * SLA + achk; vQ1 = vQ0 + 64 * SLA; dQ = 128 * SLA; } } while (0)
#define SP_LATE() do { dma_pair<0, 8192>(vQ0, vQ1, Aq, lq); dma_one<16384>(vQ0, Aq + dQ, lq); Aq += 128; const unsigned l_ = lq; lq = lq1; lq1 = lq2; lq2 = l_; } while (0)
#define SP_A1() dma_one<24576>(vA1, Ap + dA, la)
#define SP_B0() dma_one<RB>(vB0, Bp, ldsw + lb)
#define SP_B1() dma_one<RB + 8192>(vB1, Bp, ldsw + lb)
#define SP_ADV() do { Ap += 128; Bp += dB; const unsigned l_ = la; la = la1; la1 = la2; la2 = l_; lb = (lb + 16384) & 49152; } while (0)
#define SP_GELU() do { _Pragma("unroll") for (int c = 0; c < 4; ++c) { f32x16& a = acc[c >> 1][c & 1]; z1[c >> 1][c & 1][0] = gelu8(a, 0); z1[c >> 1][c & 1][1] = gelu8(a, 1); a = f32x16{}; } } while (0)
#define SP_MMA(k) do { if (busy) { _Pragma("unroll") for (int c = 0; c < 4; ++c) acc[c >> 1][c & 1] = MFMA32(Bt[c & 1][k], At[c >> 1][k], acc[c >> 1][c & 1]); } } while (0)
#define SP_STEP(PRE, LATE, WAIT0, WAIT1, D0, D1, D2, POST, LASTBAR) do { \
                bf16x8 At[2][4], Bt[2][4]; \
                PRE; \
                const lds_cp pa = lds + sa, pb = lds + (t & 3) * 16384; \
                if (busy) { _Pragma("unroll") for (int k = 0; k < 4; ++k) { Bt[0][k] = ldtr2(pb + bo0 + k * 4096, 1024); At[0][k] = ld128(pa + aoff[k]); At[1][k] = ld128(pa + aoff[k] + 4096); Bt[1][k] = ldtr2(pb + bo1 + k * 4096, 1024); } } \
                LATE; \
                if (grp == 1) { WAIT1; } \
                WAIT_L(0); BAR(); SBAR(); \
                __builtin_amdgcn_s_setprio(1); \
                SP_MMA(0); SBAR(); D0; SBAR(); SP_MMA(1); SBAR(); D1; SBAR(); SP_MMA(2); SBAR(); D2; SBAR(); SP_MMA(3); \
                __builtin_amdgcn_s_setprio(0); \
                SBAR(); \
                POST; \
                if (grp == 0) { WAIT0; } \
                if (LASTBAR) BAR(); \
                const unsigned s_ = sa; sa = sa1; sa1 = sa2; sa2 = s_; } while (0)
            asm volatile("s_waitcnt vmcnt(16) lgkmcnt(0)\n\ts_barrier" ::: "memory");
            if (grp == 1) { SP_A1(); SP_B0(); SP_B1(); SP_ADV(); BAR(); }
            int t = 0;
#pragma unroll 1
            for (; t < 8; ++t)
                SP_STEP({ if (wid == 0) { if (t == 2 && lane == 0) { set_flag(ws, flz, tag); set_flag(ws, F_S + bid, tag); } if (t == 3) { poll_flags(ws, F_L + lane, true, tag); ACQUIRE(); } } SP_SRC(t + grp); },
                        SP_LATE(), { if (t == 0) WAIT_V(16); else WAIT_V(8); }, { if (t == 0) WAIT_V(16); else WAIT_V(6); }, SP_A1(), SP_B0(), SP_B1(), { SP_ADV(); if (t == 7 && grp == 1) SP_GELU(); }, true);
            if (grp == 0) SP_GELU();
#pragma unroll 1
            for (; t < 36; ++t) SP_STEP(NOP_, SP_LATE(), WAIT_V(8), WAIT_V(6), SP_A1(), SP_B0(), SP_B1(), SP_ADV(), true);
#pragma unroll 1
            for (; t < 38; ++t)
                SP_STEP({ if (wid == 0) { SP_CHECK(); } }, SP_LATE(), { if (t == 36) WAIT_V(8); else WAIT_V(6); }, { if (t == 36) WAIT_V(6); else WAIT_V(4); },
                        if (t + grp < 38) SP_A1(), if (t + grp < 37) SP_B0(), if (t + grp < 37) SP_B1(), SP_ADV(), true);
            Q1_LOAD();
#pragma unroll 1
            for (; t < 40; ++t) SP_STEP(NOP_, NOP_, WAIT_V(8), WAIT_V(8), NOP_, NOP_, NOP_, NOP_, !(t == 39 && grp == 1));
            G_STAGE(G_SA(0, 0), rat.A, rat.vA0, rat.vA1); G_STAGE(G_SB(0, 0), rat.B, rat.vA0, rat.vA1); G_STAGE(G_SA(1, 0), rat.A + 8192, rat.vA0, rat.vA1); G_STAGE(G_SB(1, 0), rat.B + 8192, rat.vA0, rat.vA1);
            G_STAGE(G_SA(0, 1), rat.A + 65536, rat.vA0, rat.vA1); G_STAGE(G_SB(0, 1), rat.B + 65536, rat.vA0, rat.vA1); G_STAGE(G_SA(1, 1), rat.A + 73728, rat.vA0, rat.vA1); G_STAGE(G_SB(1, 1), rat.B + 73728, rat.vA0, rat.vA1);
            Q2_LOAD();
            SBAR();
#pragma unroll
            for (int m = 0; m < 2; ++m)
#pragma unroll
                for (int c = 0; c < 4; ++c) {
                    const f32x16& a = acc[m][c >> 1]; const u32x4 z = z1[m][c >> 1][c & 1]; const int s8 = 8 * (c & 1);
                    u32x4 o;
#pragma unroll
                    for (int e = 0; e < 4; ++e) o[e] = cvtpk(bflo(z[e]) * (a[s8 + 2 * e] + bias[m]), bfhi(z[e]) * (a[s8 + 2 * e + 1] + bias[m]));
                    st16(rws, fs0 + c < 43 ? WS_AF + ((256 + fs0 + c) * 64 + tb0 + m) * 1024 + lane * 16 : 0x80000000u, o);
                }
            pf = F_U + pm * 8 + pn;
            Q1_WAIT(20);
        } else {
            WAIT_V(0); WAIT_L(0); BAR();
            if (tid == 0) { set_flag(ws, flz, tag); set_flag(ws, F_S + bid, tag); }
            {
                const int aw = x8 * 2 + (kk - 30);
#pragma unroll
                for (int i = 0; i < 4; ++i) touch(tid * 128 + i * 65536, (const char*)wo + aw * 262144, lds0);
                if (tid < 344) touch(tid * 128, (const char*)wout + aw * 44032, lds0);
            }
            if (wid == 0) { poll_flags(ws, fatt + (lane & 31), lane < 32 && (lane & 15) < 12, tag); ACQUIRE(); }
            WAIT_V(0); BAR();
            g_prologue_b(rat, ldsw); g_prologue_rest(rat, ldsw); G_STAGE(G_SA(1, 1), rat.A + rat.dA1 + rat.dA2, rat.vA0, rat.vA1);
            Q1_LOAD();
            pf = -1;
            Q1_WAIT(0);
        }
    }
    BAR();
    {
        const int kh = wid >> 2, rg = wid & 3;
        const bool early = kk < 24 || kk >= 30;
        const rsrc_t rkv = __builtin_amdgcn_make_buffer_rsrc((void*)kvx, (short)0, 4 * 64 * 16384, 0x00020000);
        const unsigned dv0 = rg * 2048 + lane * 16 + kh * 32768;
        unsigned wrapoff = (unsigned)(h1 * 16 + i1 + 1) * 65536u, wrapto = (unsigned)h2 << 20;
        unsigned sA_ = 0, sB_ = ((unsigned)h1 << 20) + 65536u, lA = lds0 + kh * 65536 + rg * 2048, lB = lA + 16384;
#define A_ADV() do { sA_ = sB_; sB_ += 65536u; if (sB_ == wrapoff) { sB_ = wrapto; wrapoff = (unsigned)(h2 * 16 + i2 + 1) * 65536u; wrapto = 0x40000000u; } const unsigned t_ = lA; lA = lB; lB = t_; } while (0)
#define D_KO(h) (bdma1<8192, (h) * 1024>(dv0, rkv, sA_ + 16384, lA))
#define D_VE(h) (bdma1<32768, (h) * 1024>(dv0, rkv, sA_ + 8192, lA))
#define D_KE(h) (bdma1<0, (h) * 1024>(dv0, rkv, sB_, lB))
#define D_VO(h) (bdma1<40960, (h) * 1024>(dv0, rkv, sA_ + 24576, lA))
#define PUBLISH() do { if (pf >= 0) { if (wid == 0 && lane == 0) set_flag(ws, pf, tag); pf = -1; } } while (0)
        const lds_cp qrd = lds + 131072 + rg * 8192 + lane * 16;
        const lds_cp rbase = lds + kh * 65536 + lane * 16;
        int so = 0;
#pragma unroll 1
        for (int blkno = 0; blkno < 2; ++blkno) {
            const int ib = blkno ? i2 : i1, head = 4 * x8 + (blkno ? h2 : h1);
            const int qrow = 128 * ib + 32 * rg + r - 4 * hh - 64 * kh;
            f32x16 o[4], sA, sB, negm;
            bf16x8 fr[8];
            u32x4 pw0, pw1;
            float mref, l0 = 0.f, l1 = 0.f, resc = 1.0f, ma, mb;
            bool dead = false, pend = false;
            lds_cp pc = rbase + so, pn = rbase + (so ^ 16384);
#pragma unroll
            for (int d = 0; d < 8; ++d) fr[d] = ld128(pc + d * 1024);
            sA = f32x16{};
#pragma unroll
            for (int d = 0; d < 8; ++d) sA = MFMA32(fr[d], q[d], sA);
            asm volatile("s_waitcnt lgkmcnt(0)\n\ts_barrier" ::: "memory");
            A_ADV();
            if (blkno == 1) { D_KO(0); D_KO(1); }
            if (blkno == 1 && wid < 5) {
                if (wid < 4) touch(lane * 128, (const char*)wo + (size_t)(x8 >> 1) * 1048576 + kk * 32768 + wid * 8192, lds0);
                else { const unsigned ur = 192 * (x8 >> 1) + 6 * kk + (lane >> 3); touch((ur > FH - 1 ? FH - 1 : ur) * 1024 + (lane & 7) * 128, wout, lds0); }
            }
#pragma unroll
            for (int d = 0; d < 4; ++d) fr[d] = ld128(pc + 8192 + d * 1024);
#pragma unroll
            for (int d = 0; d < 4; ++d) o[d] = f32x16{};
            if (ib == 0) att_mask(sA, qrow);
            {
                float m = MX3(sA[0], sA[1], sA[2]);
#pragma unroll
                for (int e = 3; e < 15; e += 2) m = MX3(m, sA[e], sA[e + 1]);
                m = swap_max(__builtin_fmaxf(m, sA[15]));
                dead = m == -__builtin_inff();
                mref = dead ? 0.f : m;
#pragma unroll
                for (int e = 0; e < 16; ++e) { sA[e] -= mref; negm[e] = -mref; }
                PIN(negm);
#pragma unroll
                for (int e = 0; e < 6; ++e) sA[e] = EX2(sA[e]);
                pw0[0] = cvtpk(sA[0], sA[1]); pw0[1] = cvtpk(sA[2], sA[3]);
            }
#define A_RD(j, ptr) do { fr[j] = ld128(ptr); } while (0)
#define XE(S, i) S[i] = EX2(S[i])
#define A_P1(SC, SN, KP, VP, E0, E1, E2) do { \
            A_RD(4, (KP) + 4096); SBAR(); SN = MFMA32(fr[0], q[0], negm); XE(SC, 6); XE(SC, 7); pw0[2] = cvtpk(SC[4], SC[5]); PIN(SC); PIN(pw0); SBAR(); \
            A_RD(5, (KP) + 5120); SBAR(); SN = MFMA32(fr[1], q[1], SN); XE(SC, 8); XE(SC, 9); pw0[3] = cvtpk(SC[6], SC[7]); PIN(SC); PIN(pw0); E0; SBAR(); \
            A_RD(6, (KP) + 6144); SBAR(); SN = MFMA32(fr[2], q[2], SN); XE(SC, 10); XE(SC, 11); l0 += SC[0]; PIN(SC); PIN(l0); SBAR(); \
            A_RD(7, (KP) + 7168); SBAR(); SN = MFMA32(fr[3], q[3], SN); XE(SC, 12); XE(SC, 13); l1 += SC[1]; PIN(SC); PIN(l1); SBAR(); \
            A_RD(0, (VP));        SBAR(); SN = MFMA32(fr[4], q[4], SN); XE(SC, 14); XE(SC, 15); pw1[0] = cvtpk(SC[8], SC[9]); PIN(SC); PIN(pw1); E1; SBAR(); \
            A_RD(1, (VP) + 2048); SBAR(); SN = MFMA32(fr[5], q[5], SN); l0 += SC[2]; l1 += SC[3]; l0 += SC[4]; l1 += SC[5]; l0 += SC[6]; PIN(l0); PIN(l1); SBAR(); \
            A_RD(2, (VP) + 4096); SBAR(); SN = MFMA32(fr[6], q[6], SN); l1 += SC[7]; l0 += SC[8]; l1 += SC[9]; l0 += SC[10]; pw1[1] = cvtpk(SC[10], SC[11]); PIN(l0); PIN(l1); PIN(pw1); SBAR(); \
            A_RD(3, (VP) + 6144); SBAR(); SN = MFMA32(fr[7], q[7], SN); l1 += SC[11]; l0 += SC[12]; l1 += SC[13]; pw1[2] = cvtpk(SC[12], SC[13]); PIN(l0); PIN(l1); PIN(pw1); E2; SBAR(); } while (0)
#define PWF(x) __builtin_bit_cast(bf16x8, x)
#define A_P2A(SC, SN, VP, MASK, DQ) do { \
            A_RD(4, (VP) + 1024); A_RD(5, (VP) + 3072); SBAR(); o[0] = MFMA32(fr[0], PWF(pw0), o[0]); l0 += SC[14]; l1 += SC[15]; pw1[3] = cvtpk(SC[14], SC[15]); PIN(l0); PIN(l1); PIN(pw1); SBAR(); \
            A_RD(6, (VP) + 5120); A_RD(7, (VP) + 7168); SBAR(); o[1] = MFMA32(fr[1], PWF(pw0), o[1]); \
            if (MASK) att_mask(SN, DQ); \
            ma = MX3(SN[0], SN[1], SN[2]); mb = MX3(SN[3], SN[4], SN[5]); PIN(ma); PIN(mb); SBAR(); \
            o[2] = MFMA32(fr[2], PWF(pw0), o[2]); ma = MX3(ma, SN[6], SN[7]); mb = MX3(mb, SN[8], SN[9]); PIN(ma); PIN(mb); ma = MX3(ma, SN[10], SN[11]); PIN(ma); SBAR(); \
            o[3] = MFMA32(fr[3], PWF(pw0), o[3]); mb = MX3(mb, SN[12], SN[13]); ma = MX3(ma, SN[14], SN[15]); PIN(ma); PIN(mb); SBAR(); } while (0)
#define A_CHECK(SN) do { if (__builtin_expect(((__builtin_amdgcn_fcmpf(ma, ATT_THR, 2) | __builtin_amdgcn_fcmpf(mb, ATT_THR, 2)) != 0), 0)) {            \
                const float dl_ = __builtin_fmaxf(swap_max(__builtin_fmaxf(ma, mb)), 0.f); mref += dl_; resc = EX2(-dl_); l0 *= resc; l1 *= resc; pend = true; \
                _Pragma("unroll") for (int e = 0; e < 16; ++e) { SN[e] -= dl_; negm[e] = -mref; } PIN(negm); } SBAR(); } while (0)
#define A_RESC() do { if (__builtin_expect(pend, 0)) { pend = false;         \
            _Pragma("unroll") for (int d = 0; d < 4; ++d) _Pragma("unroll") for (int e = 0; e < 16; ++e) o[d][e] *= resc; } } while (0)
#define A_P2B(SN, RD0, RD1, RD2, RD3, D0, D1) do { \
            RD0; SBAR(); o[0] = MFMA32(fr[4], PWF(pw1), o[0]); SBAR(); A_CHECK(SN); D0; SBAR(); \
            RD1; SBAR(); o[1] = MFMA32(fr[5], PWF(pw1), o[1]); XE(SN, 0); XE(SN, 1); PIN(SN); SBAR(); \
            RD2; SBAR(); o[2] = MFMA32(fr[6], PWF(pw1), o[2]); XE(SN, 2); XE(SN, 3); pw0[0] = cvtpk(SN[0], SN[1]); PIN(SN); PIN(pw0); D1; SBAR(); \
            RD3; SBAR(); o[3] = MFMA32(fr[7], PWF(pw1), o[3]); XE(SN, 4); XE(SN, 5); pw0[1] = cvtpk(SN[2], SN[3]); PIN(SN); PIN(pw0); SBAR(); \
            A_RESC(); } while (0)
#define A_P2K(SN, KN, D0, D1) A_P2B(SN, A_RD(0, (KN)), A_RD(1, (KN) + 1024), A_RD(2, (KN) + 2048), A_RD(3, (KN) + 3072), D0, D1)
#pragma unroll 1
            for (int n = 0; n < ib; ++n) {
                A_P1(sA, sB, pc + 8192, pc + 32768, D_VE(0), D_VE(1), D_KE(0));
                A_P2A(sA, sB, pc + 32768, false, 0);
                A_P2K(sB, pn, D_KE(1), D_VO(0));
                A_P1(sB, sA, pn, pc + 32768 + 8192, D_VO(1), NOP_, NOP_);
                A_P2A(sB, sA, pc + 32768 + 8192, (n == ib - 1), qrow - 128 * ib);
                const int ev = blkno == 0 && (early || n == 0) ? n : 6;
                if (ev != 0) WAIT_V(0); else if (kk < 24) WAIT_V(22); else WAIT_V(14);
                asm volatile("s_waitcnt lgkmcnt(0)\n\ts_barrier" ::: "memory");
#define A_EV() do { A_ADV(); if (ev != 0) PUBLISH(); \
                if (ev < 6) { \
                    if (ev == 3 && wid == 0) dma_words(lane * 4, ws + (size_t)(fatt + 32) * 8, lds0 + 131072); \
                    if (ev == 4 && wid == 0) { check_parked<0>(i2 >= 8 ? 8 : 12, lds + 131072, ws, fatt + 32, tag, lane); ACQUIRE(); } \
                    if (ev == 5) Q2_LOAD(); } \
                D_KO(0); } while (0)
                A_P2B(sA, A_RD(0, pn + 8192), A_RD(1, pn + 9216), A_RD(2, pn + 10240), A_RD(3, pn + 11264), A_EV(), D_KO(1));
                so ^= 16384;
                const lds_cp t_ = pc; pc = pn; pn = t_;
            }
            lA = __builtin_amdgcn_readfirstlane(lA); lB = __builtin_amdgcn_readfirstlane(lB); sA_ = __builtin_amdgcn_readfirstlane(sA_); sB_ = __builtin_amdgcn_readfirstlane(sB_);
#define A_QNEXT() do { if (blkno == 0) { _Pragma("unroll") for (int d = 0; d < 8; ++d) q[d] = ld128(qrd + d * 1024); } } while (0)
#define A_LASTBAR() do { asm volatile("s_waitcnt vmcnt(0) lgkmcnt(0)\n\ts_barrier" ::: "memory"); PUBLISH(); } while (0)
            const bool some = kh == 0 || rg >= 2, live = 2 * kh + 1 <= rg;
            const lds_cp vp = pc + 32768 + 8192;
            if (some) A_P1(sA, sB, pc + 8192, pc + 32768, if (blkno == 0) { D_VE(0); D_VE(1); }, if (blkno == 0) { D_KE(0); D_KE(1); }, if (blkno == 0) { D_VO(0); D_VO(1); });
            else if (blkno == 0) { D_VE(0); D_VE(1); D_KE(0); D_KE(1); D_VO(0); D_VO(1); }
            A_QNEXT();
            if (some) {
                A_P2A(sA, sB, pc + 32768, true, qrow - 128 * ib - 32);
                A_P2B(sB, A_RD(0, vp), A_RD(1, vp + 2048), A_RD(2, vp + 4096), A_RD(3, vp + 6144), NOP_, NOP_);
            }
            if (live) {
#pragma unroll
                for (int j = 0; j < 4; ++j) fr[4 + j] = ld128(vp + j * 2048 + 1024);
#pragma unroll
                for (int e = 6; e < 16; ++e) sB[e] = EX2(sB[e]);
#pragma unroll
                for (int e = 0; e < 16; e += 2) { l0 += sB[e]; l1 += sB[e + 1]; }
#pragma unroll
                for (int e = 0; e < 4; ++e) { if (e >= 2) pw0[e] = cvtpk(sB[2 * e], sB[2 * e + 1]); pw1[e] = cvtpk(sB[8 + 2 * e], sB[9 + 2 * e]); }
            }
            A_LASTBAR();
            if (live) {
#pragma unroll
                for (int j = 0; j < 4; ++j) o[j] = MFMA32(fr[j], PWF(pw0), o[j]);
#pragma unroll
                for (int j = 0; j < 4; ++j) o[j] = MFMA32(fr[4 + j], PWF(pw1), o[j]);
            }
            {
                const int sb_own = kh == 0 ? 131072 + rg * 8192 : (rg >> 1) * 65536 + 32768 + so + (rg & 1) * 8192;
                const int sb_par = kh == 1 ? 131072 + rg * 8192 : (rg >> 1) * 65536 + 32768 + so + (rg & 1) * 8192;
                const float mown = dead ? -__builtin_inff() : mref, lown = l0 + l1;
                *(LAS f32x2*)(lds + kh * 65536 + so + 8192 + rg * 512 + lane * 8) = (f32x2){mown, lown};
                const f32x16 s0 = kh ? o[0] : o[2], s1 = kh ? o[1] : o[3], k0 = kh ? o[2] : o[0], k1 = kh ? o[3] : o[1];
#pragma unroll
                for (int qd = 0; qd < 4; ++qd) {
                    *(LAS f32x4*)(lds + sb_own + qd * 1024 + lane * 16) = (f32x4){s0[4 * qd], s0[4 * qd + 1], s0[4 * qd + 2], s0[4 * qd + 3]};
                    *(LAS f32x4*)(lds + sb_own + 4096 + qd * 1024 + lane * 16) = (f32x4){s1[4 * qd], s1[4 * qd + 1], s1[4 * qd + 2], s1[4 * qd + 3]};
                }
                asm volatile("s_waitcnt lgkmcnt(0)\n\ts_barrier" ::: "memory");
                const f32x2 ml = *(const LAS f32x2*)(lds + (kh ^ 1) * 65536 + so + 8192 + rg * 512 + lane * 8);
                f32x16 p0, p1;
#pragma unroll
                for (int qd = 0; qd < 4; ++qd) {
                    const f32x4 a = *(const LAS f32x4*)(lds + sb_par + qd * 1024 + lane * 16), b = *(const LAS f32x4*)(lds + sb_par + 4096 + qd * 1024 + lane * 16);
#pragma unroll
                    for (int e = 0; e < 4; ++e) { p0[4 * qd + e] = a[e]; p1[4 * qd + e] = b[e]; }
                }
                const float mf = __builtin_fmaxf(mown, ml[0]);
                const float fo = EX2(mown - mf), fp = EX2(ml[0] - mf);
                const float inv = 1.0f / swap_add(fo * lown + fp * ml[1]);
                const float ca = fo * inv, cb = fp * inv;
#pragma unroll
                for (int e = 0; e < 16; ++e) { p0[e] = ca * k0[e] + cb * p0[e]; p1[e] = ca * k1[e] + cb * p1[e]; }
                const unsigned dst = WS_AF + ((head * 8 + 4 * kh) * 64 + 4 * ib + rg) * 1024 + lane * 16;
                st16(rws, dst, pack8(p0, 0, 1.0f)); st16(rws, dst + 65536, pack8(p0, 1, 1.0f));
                st16(rws, dst + 2 * 65536, pack8(p1, 0, 1.0f)); st16(rws, dst + 3 * 65536, pack8(p1, 1, 1.0f));
                pf = F_C + ib * 32 + head;
            }
            so ^= 16384;
        }
#undef A_RD
#undef A_P1
#undef A_P2A
#undef A_P2B
#undef A_P2K
#undef A_CHECK
#undef XE
#undef PWF
    }

    {
        int lane;
        asm volatile("v_mbcnt_lo_u32_b32 %0, -1, 0\n\tv_mbcnt_hi_u32_b32 %0, -1, %0" : "=v"(lane));
        const int r = lane & 31, hh = lane >> 5, blk = (lane >> 4) & 1, qq = (lane & 15) >> 2, p = lane & 3;
        const unsigned brow = 4 * wid + (lane >> 4), bchk = ((lane & 15) ^ (((lane >> 4) & 3) << 2)) << 4;
        const int sq = x8 >> 1, tm = (x8 & 1) * 8 + (kk >> 2), tn = kk & 3, rb = wid & 3, kp = wid >> 2;
        OutArgs oa;
        oa.ra = __builtin_amdgcn_make_buffer_rsrc((void*)(ws + WS_AF), (short)0, NKS * 65536, 0x00020000);
        oa.wo = (const char*)wo + (size_t)sq * 1048576 + tn * 256; oa.wu = (const char*)wout + tn * 256;
        oa.wv = brow * 1024 + bchk;
#pragma unroll
        for (int j = 0; j < 6; ++j) { const unsigned ur = 192 * sq + 32 * j + brow; oa.uv[j] = (ur > FH - 1 ? FH - 1 : ur) * 1024 + bchk; }
        oa.va = kp * 131072 + (tm * 4 + rb) * 1024 + lane * 16; oa.sc = sq * 64 * 65536; oa.su = (256 + 12 * sq) * 65536; oa.ldsw = ldsw;
#pragma unroll
        for (int c = 0; c < 4; ++c) oa.rd[c] = lds + (kp * 32 + 4 * hh + qq) * 256 + ((c ^ qq) << 6) + blk * 32 + p * 8;
        WAIT_L(0); BAR();
        out_w<0>(oa); out_w<1>(oa); out_w<2>(oa); out_w<3>(oa);
        WAIT_V(8); BAR();
        if (wid == 0 && lane == 0) set_flag(ws, pf, tag);
        if (wid == 1) {
            if (bid == 0) {
#pragma unroll 1
                for (int i = 0; i < 4; ++i) poll_flags(ws, F_S + i * 64 + lane, true, tag);
                if (lane == 0) set_flag(ws, F_E, tag);
            }
            poll_flags(ws, lane < 8 ? F_C + tm * 32 + 8 * sq + lane : F_U + (tm >> 1) * 8 + ((3 * sq) >> 1) + (lane & 1), lane < 10, tag);
        }
        out_w<4>(oa); out_w<5>(oa); out_w<6>(oa);
        WAIT_V(10); BAR();
        u32x4 af[6][2];
        bf16x8 wf[2][4];
        f32x16 acc[4] = {};
        out_a<0>(oa, af); out_a<1>(oa, af); out_a<2>(oa, af); out_a<3>(oa, af); out_a<4>(oa, af);
#pragma unroll
        for (int c = 0; c < 4; ++c) wf[0][c] = ldtr2(oa.rd[c], 2048);
#define OS(t) out_step<t>(oa, af, wf, acc)
        OS(0); OS(1); OS(2); OS(3); OS(4); OS(5); OS(6); OS(7); OS(8); OS(9); OS(10); OS(11); OS(12); OS(13); OS(14); OS(15); OS(16); OS(17); OS(18);
#undef OS
        const f32x16 s0 = kp ? acc[0] : acc[2], s1 = kp ? acc[1] : acc[3];
        f32x16 k0 = kp ? acc[2] : acc[0], k1 = kp ? acc[3] : acc[1];
#pragma unroll
        for (int g = 0; g < 4; ++g) {
            *(LAS f32x4*)(lds + 16384 + wid * 8192 + g * 1024 + lane * 16) = (f32x4){s0[4 * g], s0[4 * g + 1], s0[4 * g + 2], s0[4 * g + 3]};
            *(LAS f32x4*)(lds + 16384 + wid * 8192 + 4096 + g * 1024 + lane * 16) = (f32x4){s1[4 * g], s1[4 * g + 1], s1[4 * g + 2], s1[4 * g + 3]};
        }
        const size_t orow = (size_t)(tm * 128 + rb * 32 + r) * DM + tn * 128 + kp * 64 + 4 * hh;
        u32x2 xv[8];
        if (rb == sq) {
#pragma unroll
            for (int g = 0; g < 8; ++g) xv[g] = *(const u32x2*)(x + orow + 8 * g);
        }
        WAIT_L(0); BAR();
#pragma unroll
        for (int g = 0; g < 4; ++g) {
            const f32x4 a = *(const LAS f32x4*)(lds + 16384 + (wid ^ 4) * 8192 + g * 1024 + lane * 16), b = *(const LAS f32x4*)(lds + 16384 + (wid ^ 4) * 8192 + 4096 + g * 1024 + lane * 16);
#pragma unroll
            for (int e = 0; e < 4; ++e) { k0[4 * g + e] += a[e]; k1[4 * g + e] += b[e]; }
        }
        const unsigned pb = WS_P + ((tm * 4 + tn) * 4 + rb) * 65536 + kp * 8192 + lane * 16;
        const int fp = F_P + (((tm * 4 + tn) * 4 + rb) * 2 + kp) * 4;
        if (rb != sq) {
#pragma unroll
            for (int g = 0; g < 4; ++g) {
                st16(rws, pb + sq * 16384 + g * 1024, __builtin_bit_cast(u32x4, (f32x4){k0[4 * g], k0[4 * g + 1], k0[4 * g + 2], k0[4 * g + 3]}));
                st16(rws, pb + sq * 16384 + 4096 + g * 1024, __builtin_bit_cast(u32x4, (f32x4){k1[4 * g], k1[4 * g + 1], k1[4 * g + 2], k1[4 * g + 3]}));
            }
            WAIT_V(0);
            if (lane == 0) set_flag(ws, fp + sq, tag);
        }
        if (rb == sq) {
            poll_flags(ws, fp + (lane & 3), (lane & 3) != sq, tag);
            u32x4 pp[3][8];
#pragma unroll
            for (int j = 0; j < 3; ++j)
#pragma unroll
                for (int g = 0; g < 8; ++g) pp[j][g] = ld16(rws, pb + ((sq + 1 + j) & 3) * 16384 + g * 1024);
#pragma unroll
            for (int g = 0; g < 8; ++g) {
                const f32x16& k = g < 4 ? k0 : k1; const int e0 = 4 * (g & 3);
                const f32x4 a = __builtin_bit_cast(f32x4, pp[0][g]), b = __builtin_bit_cast(f32x4, pp[1][g]), c = __builtin_bit_cast(f32x4, pp[2][g]);
                const f32x4 t = ((f32x4){k[e0], k[e0 + 1], k[e0 + 2], k[e0 + 3]} + a) + (b + c);
                *(u32x2*)(out + orow + 8 * g) = (u32x2){cvtpk(bflo(xv[g][0]) + t[0], bfhi(xv[g][0]) + t[1]), cvtpk(bflo(xv[g][1]) + t[2], bfhi(xv[g][1]) + t[3])};
            }
        }
    }
}

extern "C" void kernel_launch(void* const* d_in, const int* in_sizes, int n_in, void* d_out, int out_size, void* d_ws, size_t ws_size, hipStream_t stream) {
    (void)in_sizes; (void)n_in; (void)out_size; (void)ws_size;
    fused_fwd<<<256, 512, 0, stream>>>((const bf16_t*)d_in[0], (const bf16_t*)d_in[1], (const bf16_t*)d_in[2], (const bf16_t*)d_in[3], (const bf16_t*)d_in[4], (const bf16_t*)d_in[5],
                                       (const bf16_t*)d_in[7], (const bf16_t*)d_in[8], (const bf16_t*)d_in[9], (const bf16_t*)d_in[10], (bf16_t*)d_out, (unsigned char*)d_ws);
}
```

```cpp
#include <hip/hip_runtime.h>
#include <hip/hip_bf16.h>
#include <stdint.h>

#define LAS __attribute__((address_space(3)))
#define DI __device__ __forceinline__
typedef unsigned short bf16_t;
typedef unsigned long long u64;
typedef __attribute__((address_space(1))) u64 gu64;
typedef short bf16x8 __attribute__((ext_vector_type(8)));
typedef short s16x4 __attribute__((ext_vector_type(4)));
typedef __bf16 bf16x2_t __attribute__((ext_vector_type(2)));
typedef float f32x16 __attribute__((ext_vector_type(16)));
typedef float f32x4 __attribute__((ext_vector_type(4)));
typedef float f32x2 __attribute__((ext_vector_type(2)));
typedef unsigned u32x4 __attribute__((ext_vector_type(4)));
typedef unsigned u32x2 __attribute__((ext_vector_type(2)));
typedef LAS unsigned char* lds_p;
typedef const LAS unsigned char* lds_cp;
typedef __amdgpu_buffer_rsrc_t rsrc_t;

constexpr int SEQ = 2048, DM = 512, NH = 32, FH = 688, FF = 1376;
constexpr int NKS = 299;
constexpr int F_E = 0;
constexpr int F_S = 512;
constexpr int F_X = 1024;
constexpr int F_L = 2048;
constexpr int F_U = 3072;
constexpr int F_K = 3584;
constexpr int F_C = 4096;
constexpr int F_P = 5120;
constexpr unsigned WS_G   = 65536;
constexpr unsigned WS_XN  = 131072;
constexpr unsigned WS_QF  = WS_XN + SEQ * DM * 2;
constexpr unsigned WS_KVF = WS_QF + NH * 64 * 8192;
constexpr unsigned WS_Z2N = WS_KVF + NH * 64 * 16384;
constexpr unsigned WS_AF  = WS_Z2N + SEQ * 768 * 2;
constexpr unsigned WS_P   = WS_AF + 304 * 64 * 1024;
constexpr unsigned WS_END = WS_P + 256 * 65536;

#define MFMA32(a, b, c) __builtin_amdgcn_mfma_f32_32x32x16_bf16((a), (b), (c), 0, 0, 0)
#define SBAR() __builtin_amdgcn_sched_barrier(0)
#define PIN(x) asm volatile("" : "+v"(x))
#define WAIT_V(n) asm volatile("s_waitcnt vmcnt(" #n ")" ::: "memory")
#define WAIT_L(n) asm volatile("s_waitcnt lgkmcnt(" #n ")" ::: "memory")
#define BAR() asm volatile("s_barrier" ::: "memory")
#define SC1 16

DI unsigned cvtpk(float lo, float hi) { f32x2 v = {lo, hi}; bf16x2_t b = __builtin_convertvector(v, bf16x2_t); return __builtin_bit_cast(unsigned, b); }
DI float bflo(unsigned w) { return __uint_as_float(w << 16); }
DI float bfhi(unsigned w) { return __uint_as_float(w & 0xffff0000u); }
DI u32x4 pack8(const f32x16& a, int s, float sc) {
    u32x4 w;
    w[0] = cvtpk(a[8 * s + 0] * sc, a[8 * s + 1] * sc); w[1] = cvtpk(a[8 * s + 2] * sc, a[8 * s + 3] * sc);
    w[2] = cvtpk(a[8 * s + 4] * sc, a[8 * s + 5] * sc); w[3] = cvtpk(a[8 * s + 6] * sc, a[8 * s + 7] * sc);
    return w;
}
DI bf16x8 ld128(lds_cp p) { return *(const LAS bf16x8*)p; }
typedef short v4i16_t __attribute__((ext_vector_type(4)));
DI s16x4 ldtr(lds_cp p) { return __builtin_bit_cast(s16x4, __builtin_amdgcn_ds_read_tr16_b64_v4i16((LAS v4i16_t*)p)); }
DI bf16x8 ldtr2(lds_cp p, int step) { const s16x4 lo = ldtr(p), hi = ldtr(p + step); return __builtin_shufflevector(lo, hi, 0, 1, 2, 3, 4, 5, 6, 7); }
template <int I0, int I1> DI void dma_pair(unsigned v0, unsigned v1, const void* sbase, unsigned l) {
    asm volatile("s_add_u32 m0, %3, %4\n\ts_nop 0\n\tglobal_load_lds_dwordx4 %0, %2 sc1\n\ts_add_u32 m0, %3, %5\n\ts_nop 0\n\tglobal_load_lds_dwordx4 %1, %2 sc1"
                 :: "v"(v0), "v"(v1), "s"(sbase), "s"(l), "i"(I0), "i"(I1) : "memory", "scc");
}
template <int I, int O> DI void bdma1(unsigned voff, rsrc_t rs, unsigned soff, unsigned l) {
    asm volatile("s_add_u32 m0, %3, %4\n\ts_nop 0\n\tbuffer_load_dwordx4 %0, %1, %2 offen offset:%5 sc1 lds" :: "v"(voff), "s"(rs), "s"(soff), "s"(l), "i"(I), "i"(O) : "memory", "scc");
}
DI void dma_words(unsigned voff, const void* sbase, unsigned l) {
    asm volatile("s_mov_b32 m0, %2\n\ts_nop 0\n\tglobal_load_lds_dword %0, %1 sc1" :: "v"(voff), "s"(sbase), "s"(l) : "memory");
}
DI void st16(rsrc_t rs, unsigned off, u32x4 v) { __builtin_amdgcn_raw_buffer_store_b128(v, rs, off, 0, SC1); }
DI u32x4 ld16(rsrc_t rs, unsigned off) { return __builtin_amdgcn_raw_buffer_load_b128(rs, off, 0, SC1); }
DI float swap_add(float v) { auto r = __builtin_amdgcn_permlane32_swap(__float_as_uint(v), __float_as_uint(v), false, false); return __uint_as_float(r[0]) + __uint_as_float(r[1]); }
DI float swap_max(float v) { auto r = __builtin_amdgcn_permlane32_swap(__float_as_uint(v), __float_as_uint(v), false, false); return __builtin_fmaxf(__uint_as_float(r[0]), __uint_as_float(r[1])); }
DI f32x2 unpk(unsigned w) { return (f32x2){bflo(w), bfhi(w)}; }
DI unsigned cvt2(f32x2 v) { return __builtin_bit_cast(unsigned, __builtin_convertvector(v, bf16x2_t)); }
DI float sum_pair(unsigned w, float acc) { return __builtin_amdgcn_fdot2_f32_bf16(__builtin_bit_cast(bf16x2_t, w), __builtin_bit_cast(bf16x2_t, 0x3f803f80u), acc, false); }
DI unsigned gelu2(float a, float b) {
    const f32x2 v = {a, b};
    const f32x2 t = v * __builtin_elementwise_fma(v * v, (f32x2){-2.302208198f * 0.044715f, -2.302208198f * 0.044715f}, (f32x2){-2.302208198f, -2.302208198f});
    const f32x2 d = (f32x2){__builtin_amdgcn_exp2f(t[0]), __builtin_amdgcn_exp2f(t[1])} + (f32x2){1.0f, 1.0f};
    return cvt2(v * (f32x2){__builtin_amdgcn_rcpf(d[0]), __builtin_amdgcn_rcpf(d[1])});
}
DI u32x4 gelu8(const f32x16& a, int s) { return (u32x4){gelu2(a[8 * s], a[8 * s + 1]), gelu2(a[8 * s + 2], a[8 * s + 3]), gelu2(a[8 * s + 4], a[8 * s + 5]), gelu2(a[8 * s + 6], a[8 * s + 7])}; }
DI void set_flag(unsigned char* ws, int idx, u64 tag) { __hip_atomic_store((gu64*)ws + idx, tag, __ATOMIC_RELAXED, __HIP_MEMORY_SCOPE_AGENT); }
DI void poll_flags(unsigned char* ws, int idx, bool active, u64 tag) {
#pragma unroll 1
    for (unsigned spins = 0; spins < (1u << 17); ++spins) {
        const u64 v = __hip_atomic_load((gu64*)ws + idx, __ATOMIC_RELAXED, __HIP_MEMORY_SCOPE_AGENT);
        if (__all(!active || v == tag)) break;
        __builtin_amdgcn_s_sleep(4);
    }
    asm volatile("" ::: "memory");
}
#define ACQUIRE() asm volatile("buffer_inv sc1" ::: "memory")

constexpr int LDS_TAG = 163584, LDS_TOUCH = 163328, LDS_PARK = 163072;
DI void touch(unsigned voff, const void* sbase, unsigned lds0) { dma_words(voff, sbase, lds0 + LDS_TOUCH); }
template <int I> DI void dma_one(unsigned v0, const void* sbase, unsigned l) {
    asm volatile("s_add_u32 m0, %2, %3\n\ts_nop 0\n\tglobal_load_lds_dwordx4 %0, %1 sc1" :: "v"(v0), "s"(sbase), "s"(l), "i"(I) : "memory", "scc");
}
DI void z2_half(const lds_p lds, const unsigned lds0, f32x16 (&acc)[3], const bf16_t* __restrict__ x, const bf16_t* __restrict__ w_in, const unsigned char* fx, const int rg, const int hf,
                const int tid, const int wid, const int lane
                ) {
    constexpr int ZS = 704, ZST = 32 * ZS;
    constexpr unsigned WROW = FF * 2;
    const int row = tid >> 3, seg = tid & 7;
    const u32x4* src = (const u32x4*)(x + (size_t)(rg * 64 + row) * DM) + seg;
    u32x4 v[8];
#pragma unroll
    for (int j = 0; j < 8; ++j) asm volatile("global_load_dwordx4 %0, %1, off offset:%2" : "=v"(v[j]) : "v"(src), "i"(j * 128) : "memory");
    unsigned bv[3];
    const int p2 = wid < 6 ? wid + 16 : wid + 8;
#pragma unroll
    for (int i = 0; i < 3; ++i) { const int c = (i == 2 ? p2 : wid + 8 * i) * 64 + lane, zr = c / 44, zc = c - zr * 44; bv[i] = zr * WROW + (zc > 42 ? 42 : zc) * 16; }
    const unsigned lw = lds0 + 65536 + wid * 1024, lw2 = lds0 + 65536 + p2 * 1024;
    const char* wg = (const char*)w_in + FH * 2 + hf * FH;
#define Z_STAGE(slot, g_) do { dma_pair<(slot) * ZST, (slot) * ZST + 8192>(bv[0], bv[1], (g_), lw); dma_one<(slot) * ZST>(bv[2], (g_), lw2); } while (0)
    BAR();
    Z_STAGE(0, wg); Z_STAGE(1, wg + 32 * WROW); Z_STAGE(2, wg + 64 * WROW); Z_STAGE(3, wg + 96 * WROW);
#pragma unroll
    for (int i = 0; i < 2; ++i) { int c = (2 * wid + i) * 64 + lane; c = c > 895 ? 895 : c; const int zr = c / 7, zc = c - zr * 7; touch(((rg & 3) * 128 + zr) * WROW + (zc == 6 ? 684 : zc * 128), wg, lds0); }
    SBAR();
    {
        asm volatile("s_waitcnt vmcnt(14)" : "+v"(v[0]), "+v"(v[1]), "+v"(v[2]), "+v"(v[3]), "+v"(v[4]), "+v"(v[5]), "+v"(v[6]), "+v"(v[7]));
        float s = 0.f, s1 = 0.f;
#pragma unroll
        for (int j = 0; j < 8; ++j) { s = sum_pair(v[j][0], s); s1 = sum_pair(v[j][1], s1); s = sum_pair(v[j][2], s); s1 = sum_pair(v[j][3], s1); }
        s += s1;
        s += __shfl_xor(s, 1); s += __shfl_xor(s, 2); s += __shfl_xor(s, 4);
        const float mean = s * (1.0f / DM);
        f32x2 d[32], q2 = {0.f, 0.f}, q3 = {0.f, 0.f};
#pragma unroll
        for (int i = 0; i < 32; i += 2) { d[i] = unpk(v[i >> 2][i & 3]) - (f32x2){mean, mean}; d[i + 1] = unpk(v[i >> 2][(i & 3) + 1]) - (f32x2){mean, mean};
            q2 = __builtin_elementwise_fma(d[i], d[i], q2); q3 = __builtin_elementwise_fma(d[i + 1], d[i + 1], q3); }
        q2 += q3;
        float q = q2[0] + q2[1];
        q += __shfl_xor(q, 1); q += __shfl_xor(q, 2); q += __shfl_xor(q, 4);
        const float rstd = 1.0f / sqrtf(q * (1.0f / DM) + 1e-5f);
#pragma unroll
        for (int j = 0; j < 8; ++j) {
            u32x4 o;
#pragma unroll
            for (int e = 0; e < 4; ++e) o[e] = cvt2(d[4 * j + e] * (f32x2){rstd, rstd});
            *(LAS u32x4*)(lds + j * 8192 + row * 128 + ((seg ^ ((row >> 1) & 7)) << 4)) = o;
        }
    }
    const int r = lane & 31, hh = lane >> 5, blk = (lane >> 4) & 1, qq = (lane & 15) >> 2, p = lane & 3;
    const int asw = (r >> 1) & 7, oth = (wid & 1) ? -4096 : 4096;
    lds_cp ab[4];
#pragma unroll
    for (int k = 0; k < 4; ++k) ab[k] = lds + (wid & 1) * 4096 + r * 128 + (((2 * k + hh) ^ asw) << 4);
    const lds_cp bb = lds + 65536 + (8 * hh + qq) * ZS + wid * 64 + blk * 32 + p * 8;
    const lds_cp bx = bb + (8 + (wid >> 1) - wid) * 64;
    const bool w3 = wid < 6;
#pragma unroll
    for (int i = 0; i < 3; ++i) acc[i] = f32x16{};
    bf16x8 fa[2][2][2], fb[2][2][2];
#define Z_READ(set, slot, k0, aoff_) do { _Pragma("unroll") for (int k_ = 0; k_ < 2; ++k_) { \
        fa[set][k_][0] = ld128(ab[(k0) + k_] + (aoff_)); fa[set][k_][1] = ld128(ab[(k0) + k_] + oth + (aoff_)); \
        fb[set][k_][0] = ldtr2(bb + (slot) * ZST + k_ * 16 * ZS, 4 * ZS); if (w3) fb[set][k_][1] = ldtr2(bx + (slot) * ZST + k_ * 16 * ZS, 4 * ZS); } } while (0)
#define Z_MMA(set, k_) do { acc[0] = MFMA32(fb[set][k_][0], fa[set][k_][0], acc[0]); acc[1] = MFMA32(fb[set][k_][0], fa[set][k_][1], acc[1]); \
        if (w3) acc[2] = MFMA32(fb[set][k_][1], fa[set][k_][0], acc[2]); } while (0)
#define Z_SYNC(n) asm volatile("s_waitcnt vmcnt(" #n ") lgkmcnt(0)\n\ts_barrier" ::: "memory")
    Z_SYNC(11);
    Z_READ(0, 0, 0, 0);
    const char* g = wg + 128 * WROW;
#pragma unroll 1
    for (int kt = 0; kt < 3; ++kt) {
        Z_SYNC(6); Z_READ(1, 1, 2, 0);     SBAR(); Z_MMA(0, 0); SBAR(); Z_STAGE(0, g);             SBAR(); Z_MMA(0, 1); SBAR();
        Z_SYNC(6); Z_READ(0, 2, 0, 8192);  SBAR(); Z_MMA(1, 0); SBAR(); Z_STAGE(1, g + 32 * WROW); SBAR(); Z_MMA(1, 1); SBAR();
        Z_SYNC(6); Z_READ(1, 3, 2, 8192);  SBAR(); Z_MMA(0, 0); SBAR(); Z_STAGE(2, g + 64 * WROW); SBAR(); Z_MMA(0, 1); SBAR();
        Z_SYNC(6); Z_READ(0, 0, 0, 16384); SBAR(); Z_MMA(1, 0); SBAR(); Z_STAGE(3, g + 96 * WROW); SBAR(); Z_MMA(1, 1); SBAR();
        g += 128 * WROW;
#pragma unroll
        for (int k = 0; k < 4; ++k) ab[k] += 16384;
    }
    if (wid == 0) dma_words(lane * 4, fx, lds0 + LDS_PARK);
    Z_SYNC(6); Z_READ(1, 1, 2, 0);    SBAR(); Z_MMA(0, 0); Z_MMA(0, 1); SBAR();
    Z_SYNC(3); Z_READ(0, 2, 0, 8192); SBAR(); Z_MMA(1, 0); Z_MMA(1, 1); SBAR();
    Z_SYNC(0); Z_READ(1, 3, 2, 8192); SBAR(); Z_MMA(0, 0); Z_MMA(0, 1); SBAR();
    Z_SYNC(0);                                Z_MMA(1, 0); Z_MMA(1, 1);
#undef Z_STAGE
#undef Z_READ
#undef Z_MMA
#undef Z_SYNC
}

struct Run { const char* A; const char* B; unsigned dA1, dA2, dB1, dB2, vA0, vA1, vB0, vB1; };
constexpr int HTB = 16384;
constexpr unsigned LDA = DM * 2, LDB = 4096 * 2;
#define G_SA(b, h) (((b) * 2 + (h)) * HTB)
#define G_SB(b, h) ((4 + (b) * 2 + (h)) * HTB)
#define G_STAGE(bufoff, gb, v0, v1) dma_pair<(bufoff), (bufoff) + 8192>((v0), (v1), (gb), ldsw)
#define G_LDA(dst, b, h) do { _Pragma("unroll") for (int m_ = 0; m_ < 2; ++m_) _Pragma("unroll") for (int k_ = 0; k_ < 4; ++k_) dst[m_][k_] = ld128(lds + G_SA(b, h) + aoff[k_] + m_ * 4096); } while (0)
#define G_LDB(dst, b, h) do { _Pragma("unroll") for (int k_ = 0; k_ < 4; ++k_) dst[k_] = ldtr2(lds + G_SB(b, h) + boff + k_ * 4096, 1024); } while (0)
#define G_MMA(ai, bj, At, Bt) do { __builtin_amdgcn_s_setprio(1); _Pragma("unroll") for (int k_ = 0; k_ < 4; ++k_) _Pragma("unroll") for (int m_ = 0; m_ < 2; ++m_) \
        acc[ai][bj][m_] = SWAP ? MFMA32(At[m_][k_], Bt[k_], acc[ai][bj][m_]) : MFMA32(Bt[k_], At[m_][k_], acc[ai][bj][m_]); __builtin_amdgcn_s_setprio(0); } while (0)

DI void g_prologue_b(const Run& u, const unsigned ldsw) {
    G_STAGE(G_SB(0, 0), u.B, u.vB0, u.vB1); G_STAGE(G_SB(0, 1), u.B + u.dB1, u.vB0, u.vB1);
}
DI void g_prologue_rest(const Run& u, const unsigned ldsw) {
    G_STAGE(G_SA(0, 0), u.A, u.vA0, u.vA1); G_STAGE(G_SA(0, 1), u.A + u.dA1, u.vA0, u.vA1);
    G_STAGE(G_SB(1, 0), u.B + u.dB2, u.vB0, u.vB1); G_STAGE(G_SA(1, 0), u.A + u.dA2, u.vA0, u.vA1); G_STAGE(G_SB(1, 1), u.B + u.dB2 + u.dB1, u.vB0, u.vB1);
}
template <int LO> DI void check_parked(const int HI, lds_cp parked, unsigned char* ws, const int fidx, const u64 tag, const int lane) {
    const unsigned w = *(const LAS unsigned*)(parked + lane * 4);
    const int sl = (lane >> 1) & 15;
    if (!__all(sl < LO || sl >= HI || w == ((lane & 1) ? (unsigned)(tag >> 32) : (unsigned)tag))) poll_flags(ws, fidx + (lane & 31), lane < 32 && (lane & 15) >= LO && (lane & 15) < HI, tag);
}

template <bool SWAP, int ROUND>
DI void g_kloop(const lds_p lds, const unsigned lds0, f32x16 (&acc)[2][2][2], const Run& cur, const Run& nxt, const int (&aoff)[4], const int boff, const unsigned ldsw,
                unsigned char* ws, const u64 tag, const int fl0, const int fatt, const int lane, const int wid, const unsigned tch) {
    bf16x8 At[2][4], B0[4], B1[4];
#pragma unroll
    for (int i = 0; i < 8; ++i) acc[i >> 2][(i >> 1) & 1][i & 1] = f32x16{};
#pragma unroll 1
    for (int t = 0; t < 8; t += 2) {
        const bool last = (t == 6);
        const char* a1 = cur.A + (t + 1) * 128 + 128 * LDA;
        const char* a2 = last ? nxt.A : cur.A + (t + 2) * 128;
        const char* b2 = last ? nxt.B : cur.B + (size_t)(t + 2) * 64 * LDB;
        const unsigned dA1 = last ? nxt.dA1 : cur.dA1, dA2 = last ? nxt.dA2 : cur.dA2, dB1 = last ? nxt.dB1 : cur.dB1, dB2 = last ? nxt.dB2 : cur.dB2;
        const unsigned vA0 = last ? nxt.vA0 : cur.vA0, vA1 = last ? nxt.vA1 : cur.vA1, vB0 = last ? nxt.vB0 : cur.vB0, vB1 = last ? nxt.vB1 : cur.vB1;
        if (ROUND == 1 && last && wid == 0) dma_words(lane * 4, ws + (size_t)fatt * 8, lds0 + 131072);
        if (t == 2 && wid == 0 && lane == 0) set_flag(ws, fl0, tag);
        if (t == 0) G_LDB(B0, 0, 0);
        SBAR(); G_LDA(At, 0, 0); if (!(ROUND == 1 && t == 0)) G_STAGE(G_SA(1, 1), a1, cur.vA0, cur.vA1);
        if (ROUND == 0 && t == 2) touch(tch, nxt.A, lds0);
        if (ROUND == 1 && t == 4 && wid == 0) { check_parked<0>(8, lds + 131072, ws, fatt, tag, lane); ACQUIRE(); }
        WAIT_L(8); BAR(); WAIT_L(0); SBAR(); G_MMA(0, 0, At, B0); SBAR(); BAR();
        G_LDB(B1, 0, 1);
        G_STAGE(G_SB(0, 0), b2, vB0, vB1);
        BAR(); WAIT_L(0); SBAR(); G_MMA(0, 1, At, B1); SBAR(); BAR();
        G_LDA(At, 0, 1); G_STAGE(G_SA(0, 0), a2, vA0, vA1);
        if (ROUND == 1 && t == 0) WAIT_V(26); else WAIT_V(10);
        BAR(); WAIT_L(0); SBAR(); G_MMA(1, 0, At, B0); SBAR(); BAR();
        G_LDB(B0, 1, 0); G_STAGE(G_SB(0, 1), b2 + dB1, vB0, vB1);
        if (ROUND == 1 && t == 0) WAIT_V(22); else if ((ROUND == 1 && t == 4 && wid == 0) || (ROUND == 0 && t == 2)) WAIT_V(7); else WAIT_V(6);
        BAR(); SBAR(); G_MMA(1, 1, At, B1); SBAR(); BAR();
        if (ROUND == 1 && wid == 0) {
            if (t == 2) dma_words(lane * 4, ws + (size_t)fatt * 8, lds0 + 131072);
            if (last) check_parked<8>(12, lds + 131072, ws, fatt, tag, lane);
        }
        G_LDA(At, 1, 0); G_STAGE(G_SA(0, 1), a2 + dA1, vA0, vA1);
        BAR(); WAIT_L(0); SBAR(); G_MMA(0, 0, At, B0); SBAR(); BAR();
        G_LDB(B1, 1, 1); G_STAGE(G_SB(1, 0), b2 + dB2, vB0, vB1);
        BAR(); WAIT_L(0); SBAR(); G_MMA(0, 1, At, B1); SBAR(); BAR();
        G_LDA(At, 1, 1); G_STAGE(G_SA(1, 0), a2 + dA2, vA0, vA1);
        if (!last) WAIT_V(10);
        BAR(); WAIT_L(0); SBAR(); G_MMA(1, 0, At, B0); SBAR(); BAR();
        if (!last) G_LDB(B0, 0, 0);
        G_STAGE(G_SB(1, 1), b2 + dB2 + dB1, vB0, vB1);
        if (!(ROUND == 1 && last)) WAIT_V(6);
        BAR(); SBAR(); G_MMA(1, 1, At, B1); SBAR();
        if (!(last && wid >= 4)) BAR();
    }
}

DI void g_epilogue(const f32x16 (&acc)[2][2][2], const rsrc_t rws, const int type, const int pn, const int pm, const int wr, const int wc, const int lane) {
    constexpr float QS = 0.08838834764831845f * 1.4426950408889634f;
    const float sc = type == 2 ? QS : 1.0f;
    const unsigned base = (type == 2 ? WS_QF : WS_KVF + (type == 1 ? 8192 : 0)) + wc * 2048 + lane * 16;
    const unsigned hstride = type == 2 ? 8192 : 16384;
#pragma unroll
    for (int ai = 0; ai < 2; ++ai)
#pragma unroll
        for (int bj = 0; bj < 2; ++bj)
#pragma unroll
            for (int m = 0; m < 2; ++m) {
                const unsigned dst = base + (unsigned)((2 * pn + bj) * 64 + pm * 8 + ai * 4 + wr * 2 + m) * hstride;
                st16(rws, dst, pack8(acc[ai][bj][m], 0, sc)); st16(rws, dst + 1024, pack8(acc[ai][bj][m], 1, sc));
            }
}

#define NOP_ (void)0
#define SP_CHECK() if (t == 36) { poll_flags(ws, fatt + lane, (lane & 15) < 12, tag); ACQUIRE(); }
#define EX2(v) __builtin_amdgcn_exp2f(v)
#define MX3(a, b, c) __builtin_fmaxf(__builtin_fmaxf((a), (b)), (c))
constexpr float ATT_THR = 32.0f;
DI void att_mask(f32x16& s, int dq) {
#pragma unroll
    for (int e = 0; e < 16; ++e) if ((e & 3) + 8 * (e >> 2) > dq) s[e] = -__builtin_inff();
}

struct OutArgs { rsrc_t ra; const char* wo; const char* wu; lds_cp rd[4]; unsigned wv, uv[6], va, sc, su, ldsw; };
template <int T> DI void out_w(const OutArgs& o) {
    constexpr int S = (T % 9) * 16384;
    if constexpr (T < 16) dma_pair<S, S + 8192>(o.wv, o.wv + 32768, o.wo + T * 65536, o.ldsw);
    else if constexpr (T < 19) dma_pair<S, S + 8192>(o.uv[2 * (T - 16)], o.uv[2 * (T - 16) + 1], o.wu, o.ldsw);
}
template <int T> DI void out_a(const OutArgs& o, u32x4 (&af)[6][2]) {
    if constexpr (T < 19) {
        const unsigned so = T < 16 ? o.sc + T * 262144 : o.su + (T - 16) * 262144;
        asm volatile("buffer_load_dwordx4 %0, %1, %2, %3 offen sc1" : "=v"(af[T % 6][0]) : "v"(o.va), "s"(o.ra), "s"(so) : "memory");
        asm volatile("buffer_load_dwordx4 %0, %1, %2, %3 offen sc1" : "=v"(af[T % 6][1]) : "v"(o.va), "s"(o.ra), "s"(so + 65536) : "memory");
    }
}
constexpr int out_cnt(int i) { return (i + 7 < 19 ? 2 : 0) + (i + 5 < 19 ? 2 : 0); }
constexpr int out_allow(int t) { int n = t < 4 ? 2 * (4 - t) : 0; for (int i = t < 4 ? 0 : t - 4; i < t; ++i) n += out_cnt(i); return n; }
template <int T> DI void out_step(const OutArgs& o, u32x4 (&af)[6][2], bf16x8 (&wf)[2][4], f32x16 (&acc)[4]) {
    asm volatile("s_waitcnt vmcnt(%2)" : "+v"(af[T % 6][0]), "+v"(af[T % 6][1]) : "n"(out_allow(T)) : "memory");
    if constexpr (T % 2 == 0) BAR();
    const bf16x8 a0 = __builtin_bit_cast(bf16x8, af[T % 6][0]), a1 = __builtin_bit_cast(bf16x8, af[T % 6][1]);
#pragma unroll
    for (int c = 0; c < 4; ++c) wf[1][c] = ldtr2(o.rd[c] + (T % 9) * 16384 + 4096, 2048);
    SBAR();
    acc[0] = MFMA32(wf[0][0], a0, acc[0]); acc[1] = MFMA32(wf[0][1], a0, acc[1]); SBAR();
    out_w<T + 7>(o); SBAR();
    acc[2] = MFMA32(wf[0][2], a0, acc[2]); acc[3] = MFMA32(wf[0][3], a0, acc[3]); SBAR();
    if constexpr (T < 18) {
#pragma unroll
        for (int c = 0; c < 4; ++c) wf[0][c] = ldtr2(o.rd[c] + ((T + 1) % 9) * 16384, 2048);
    }
    SBAR();
    acc[0] = MFMA32(wf[1][0], a1, acc[0]); acc[1] = MFMA32(wf[1][1], a1, acc[1]); SBAR();
    out_a<T + 5>(o, af); SBAR();
    acc[2] = MFMA32(wf[1][2], a1, acc[2]); acc[3] = MFMA32(wf[1][3], a1, acc[3]); SBAR();
}

__global__ __launch_bounds__(512, 2) void fused_fwd(const bf16_t* __restrict__ x, const bf16_t* __restrict__ ckv, const bf16_t* __restrict__ wq, const bf16_t* __restrict__ wk,
                                                    const bf16_t* __restrict__ wv, const bf16_t* __restrict__ wo, const bf16_t* __restrict__ w_in, const bf16_t* __restrict__ wsp,
                                                    const bf16_t* __restrict__ bsp, const bf16_t* __restrict__ wout, bf16_t* __restrict__ out, unsigned char* ws) {
    __shared__ __attribute__((aligned(16))) unsigned char smem[163840];
    asm volatile("" :: "s"(x), "s"(ckv), "s"(wq), "s"(wk), "s"(wv), "s"(w_in), "s"(bsp), "s"(wout), "s"(ws));
    const lds_p lds = (lds_p)smem;
    const unsigned lds0 = (unsigned)(uintptr_t)smem;
    const int tid = threadIdx.x, wid = __builtin_amdgcn_readfirstlane(tid >> 6), lane = tid & 63;
    const int bid = blockIdx.x, x8 = bid & 7, kk = bid >> 3;
    const int r = lane & 31, hh = lane >> 5, blk = (lane >> 4) & 1, qq = (lane & 15) >> 2, p = lane & 3;
    const rsrc_t rws = __builtin_amdgcn_make_buffer_rsrc((void*)ws, (short)0, WS_END, 0x00020000);
    const unsigned ldsw = lds0 + (unsigned)wid * 1024u;
    dma_words(lane * 4, ws, lds0 + LDS_TAG);
    u64 tag;
#define MAKE_TAG() do { const u32x2 e_ = *(const LAS u32x2*)(lds + LDS_TAG); \
        const u64 ep_ = ((u64)(unsigned)__builtin_amdgcn_readfirstlane(e_[1]) << 32) | (unsigned)__builtin_amdgcn_readfirstlane(e_[0]); \
        u64 z_ = ep_ + 0x9E3779B97F4A7C15ull; z_ = (z_ ^ (z_ >> 30)) * 0xBF58476D1CE4E5B9ull; z_ = (z_ ^ (z_ >> 27)) * 0x94D049BB133111EBull; z_ ^= z_ >> 31; tag = z_ == ep_ ? ~z_ : z_; } while (0)
    const bool tail4 = kk >= 28;
    const int h1 = tail4 ? 2 * (kk & 1) : kk & 3, h2 = tail4 ? h1 + 1 : h1;
    const int i1 = tail4 ? ((kk & 2) ? 10 : 1) : (0x29EBCDF >> (kk & 28)) & 15, i2 = tail4 ? i1 : (0x0837654 >> (kk & 28)) & 15;
    const unsigned char* kvx = ws + WS_KVF + (size_t)x8 * 4 * 64 * 16384;
    const unsigned char* kvh = kvx + (size_t)h1 * 64 * 16384;
    const char* q1src = (const char*)ws + WS_QF + (size_t)((4 * x8 + h1) * 64 + 4 * i1 + (wid & 3)) * 8192;
    bf16x8 q[8];
#define Q1_LOAD() do { _Pragma("unroll") for (int d = 0; d < 8; ++d) asm volatile("global_load_dwordx4 %0, %1, %2 offset:%3 sc1" : "=v"(q[d]) : "v"(lane * 16 + (d >> 2) * 4096), "s"(q1src), "i"((d & 3) * 1024) : "memory"); } while (0)
#define Q1_WAIT(n) asm volatile("s_waitcnt vmcnt(" #n ")" : "+v"(q[0]), "+v"(q[1]), "+v"(q[2]), "+v"(q[3]), "+v"(q[4]), "+v"(q[5]), "+v"(q[6]), "+v"(q[7]) :: "memory")
    const int fatt = F_K + x8 * 64;
#define Q2_LOAD() do { const char* q2src = (const char*)ws + WS_QF + (size_t)((4 * x8 + h2) * 64 + 4 * i2) * 8192 + wid * 4096; \
        const unsigned lq_ = __builtin_amdgcn_readfirstlane(lds0 + wid * 4096); \
        dma_pair<131072, 131072 + 1024>(lane * 16, lane * 16 + 1024, q2src, lq_); dma_pair<131072 + 2048, 131072 + 3072>(lane * 16 + 2048, lane * 16 + 3072, q2src, lq_); } while (0)
    Run rat;
    rat.A = (const char*)kvh; rat.B = (const char*)kvh + 32768; rat.dA1 = rat.dB1 = 65536; rat.dA2 = rat.dB2 = 8192;
    rat.vA0 = rat.vB0 = wid * 1024 + lane * 16; rat.vA1 = rat.vB1 = rat.vA0 + 16384;
    int pf;
    int aoff[4];
    const unsigned arow = 8 * wid + (lane >> 3), achk = ((lane & 7) ^ (((wid & 1) << 2) + (lane >> 4))) << 4;
    const unsigned brow = 4 * wid + (lane >> 4), bchk = ((lane & 15) ^ (((lane >> 4) & 3) << 2)) << 4;

    if (kk < 24) {
        const int type = kk >> 3, pn = 2 * x8 + ((kk >> 2) & 1), pm0 = (kk & 3) + (type == 2 ? 4 : 0), pm1 = pm0 ^ 4, wr = wid >> 2, wc = wid & 3;
        Run u0, u1;
        const char* Ab = type == 2 ? (const char*)ws + WS_XN : (const char*)ckv;
        u0.B = (const char*)(type == 0 ? wk : type == 1 ? wv : wq) + pn * 512; u0.A = Ab + (size_t)pm0 * 256 * LDA;
        u0.dA1 = 128 * LDA; u0.dA2 = 128; u0.dB1 = 256; u0.dB2 = 64 * LDB;
        u0.vA0 = arow * LDA + achk; u0.vA1 = u0.vA0 + 64 * LDA; u0.vB0 = brow * LDB + bchk; u0.vB1 = u0.vB0 + 32 * LDB;
        u1 = u0; u1.A = Ab + (size_t)pm1 * 256 * LDA;
        const unsigned tch = (type == 2 ? ((kk >> 2) & 1) * 2 : type * 2 + ((kk >> 2) & 1)) * 65536 + tid * 128, bl = ((kk & 3) * 128 + wid * 16 + (lane >> 2)) * LDB + (lane & 3) * 128;
        if (type < 2) {
            g_prologue_b(u0, ldsw); g_prologue_rest(u0, ldsw);
            WAIT_V(6);
            MAKE_TAG();
        } else {
            const int j = x8 * 8 + (kk - 16), row = 32 * j + 4 * wid + (lane >> 4);
            const u32x4* src = (const u32x4*)(x + (size_t)row * DM) + (lane & 15);
            u32x4 v[4];
#pragma unroll
            for (int i = 0; i < 4; ++i) v[i] = src[16 * i];
            SBAR();
            g_prologue_b(u0, ldsw); touch(bl, u0.B, lds0);
            SBAR();
            float s = 0.f;
#pragma unroll
            for (int i = 0; i < 4; ++i)
#pragma unroll
                for (int e = 0; e < 4; ++e) s += bflo(v[i][e]) + bfhi(v[i][e]);
            s += __shfl_xor(s, 1); s += __shfl_xor(s, 2); s += __shfl_xor(s, 4); s += __shfl_xor(s, 8);
            const float mean = s * (1.0f / DM);
            float q = 0.f;
#pragma unroll
            for (int i = 0; i < 4; ++i)
#pragma unroll
                for (int e = 0; e < 4; ++e) { const float a = bflo(v[i][e]) - mean, b = bfhi(v[i][e]) - mean; q += a * a + b * b; }
            q += __shfl_xor(q, 1); q += __shfl_xor(q, 2); q += __shfl_xor(q, 4); q += __shfl_xor(q, 8);
            const float rstd = 1.0f / sqrtf(q * (1.0f / DM) + 1e-5f);
#pragma unroll
            for (int i = 0; i < 4; ++i) {
                u32x4 o;
#pragma unroll
                for (int e = 0; e < 4; ++e) o[e] = cvtpk((bflo(v[i][e]) - mean) * rstd, (bfhi(v[i][e]) - mean) * rstd);
                st16(rws, WS_XN + row * 1024 + ((lane & 15) + 16 * i) * 16, o);
            }
            WAIT_V(0);
            MAKE_TAG();
            BAR();
            if (tid == 0) set_flag(ws, F_X + j, tag);
            if (wid == 0) { poll_flags(ws, F_X + ((lane & 8) ? pm1 : pm0) * 8 + (lane & 7), lane < 16, tag); ACQUIRE(); WAIT_V(0); }
            BAR();
            g_prologue_rest(u0, ldsw);
            WAIT_V(6);
        }
#pragma unroll
        for (int k = 0; k < 4; ++k) aoff[k] = (wr * 64 + r) * 128 + (((2 * k + hh) ^ ((r >> 1) & 7)) << 4);
        const int boff = (8 * hh + qq) * 256 + ((wc ^ qq) << 6) + blk * 32 + p * 8;
        BAR();
        if (wr == 1) BAR();
        f32x16 acc[2][2][2];
        const int fk = fatt + (pn & 1) * 16 + type * 4 + (kk & 3);
        if (type == 1) g_kloop<true, 0>(lds, lds0, acc, u0, u1, aoff, boff, ldsw, ws, tag, F_S + bid, fatt, lane, wid, tch);
        else           g_kloop<false, 0>(lds, lds0, acc, u0, u1, aoff, boff, ldsw, ws, tag, F_S + bid, fatt, lane, wid, tch);
        G_STAGE(G_SA(1, 1), u1.A + 128 + 128 * LDA, u1.vA0, u1.vA1);
        SBAR();
        g_epilogue(acc, rws, type, pn, pm0, wr, wc, lane);
        if (wr == 1) BAR();
        if (type == 1) g_kloop<true, 1>(lds, lds0, acc, u1, rat, aoff, boff, ldsw, ws, tag, fk, fatt, lane, wid, tch);
        else           g_kloop<false, 1>(lds, lds0, acc, u1, rat, aoff, boff, ldsw, ws, tag, fk, fatt, lane, wid, tch);
        Q1_LOAD();
        G_STAGE(G_SA(1, 1), rat.A + rat.dA1 + rat.dA2, rat.vA0, rat.vA1);
        SBAR();
        g_epilogue(acc, rws, type, pn, pm1, wr, wc, lane);
        pf = fk + 32;
        Q1_WAIT(18);
    } else {
        const int pm = x8, pn = kk - 24, grp = wid >> 2;
        constexpr unsigned SLA = SEQ * 2, SLB = 768 * 2, WROW = FF * 2;
        constexpr int RB = 65536, RA2 = 131072;
        const char* Ap = (const char*)ws + WS_XN + (size_t)pm * 256 * LDA;
        const char* Bp = (const char*)w_in + pn * 256;
        unsigned vA0 = arow * LDA + achk, vA1 = vA0 + 64 * LDA, vB0 = brow * WROW + bchk, vB1 = vB0 + 32 * WROW, dA = 128 * LDA, dB = 64 * WROW;
        const int tb0 = 8 * pm + 2 * (wid & 3), fs0 = 8 * pn + 4 * grp;
        float bias[2];
        const int flz = F_L + (4 * x8 + (pn >> 1)) * 2 + (kk & 1);
        {
            f32x16 za[3];
            const int rg = 4 * x8 + (pn >> 1), hf = kk & 1, m0_ = wid & 1;
#pragma unroll
            for (int m = 0; m < 2; ++m) bias[m] = __uint_as_float((unsigned)bsp[(tb0 + m) * 32 + r] << 16);
            z2_half(lds, lds0, za, x, w_in, ws + (F_X + 8 * pm) * 8, rg, hf, tid, wid, lane
                );
            PIN(bias[0]); PIN(bias[1]);
            MAKE_TAG();
            if (wid == 0 && kk < 30) {
                const unsigned w = *(const LAS unsigned*)(lds + LDS_PARK + lane * 4);
                if (!__all(lane >= 16 || w == ((lane & 1) ? (unsigned)(tag >> 32) : (unsigned)tag))) poll_flags(ws, F_X + 8 * pm + (lane & 7), true, tag);
                ACQUIRE();
            }
            {
                const char* slab = (const char*)wsp + (size_t)x8 * 1048576 + pn * 131072;
                touch(tid * 128, slab, lds0); touch(tid * 128 + 65536, slab, lds0);
            }
            u32x4 zp[3][2];
#pragma unroll
            for (int i = 0; i < 3; ++i) { zp[i][0] = gelu8(za[i], 0); zp[i][1] = gelu8(za[i], 1); }
            const int nx = wid < 4 ? 8 : wid < 6 ? 6 : 0;
            LAS float* red = (LAS float*)lds;
            float s[2] = {0.f, 0.f}, mean[2];
#pragma unroll
            for (int i = 0; i < 24; ++i) if (i < 16 || i - 16 < nx) s[(i >> 3) & 1] = sum_pair(zp[i >> 3][(i >> 2) & 1][i & 3], s[(i >> 3) & 1]);
            red[wid * 128 + m0_ * 64 + lane] = s[0]; red[wid * 128 + (m0_ ^ 1) * 64 + lane] = s[1];
            WAIT_L(0); BAR();
#pragma unroll
            for (int m = 0; m < 2; ++m) { float t = 0.f;
#pragma unroll
                for (int w = 0; w < 8; ++w) t += red[w * 128 + (m0_ ^ m) * 64 + lane];
                mean[m] = swap_add(t) * (2.0f / FH); }
            f32x2 d[24], q2[2] = {{0.f, 0.f}, {0.f, 0.f}};
#pragma unroll
            for (int i = 0; i < 24; ++i) { const int m = (i >> 3) & 1; d[i] = unpk(zp[i >> 3][(i >> 2) & 1][i & 3]) - (f32x2){mean[m], mean[m]};
                if (i < 16 || i - 16 < nx) q2[m] = __builtin_elementwise_fma(d[i], d[i], q2[m]); }
            red[1024 + wid * 128 + m0_ * 64 + lane] = q2[0][0] + q2[0][1]; red[1024 + wid * 128 + (m0_ ^ 1) * 64 + lane] = q2[1][0] + q2[1][1];
            WAIT_V(2); WAIT_L(0); BAR();
            LAS f32x2* tab = (LAS f32x2*)(lds + 160768);
            if (wid == 0) {
#pragma unroll
                for (int m = 0; m < 2; ++m) { float t = 0.f;
#pragma unroll
                    for (int w = 0; w < 8; ++w) t += red[1024 + w * 128 + m * 64 + lane];
                    s[m] = swap_add(t); }
                const float mo = hh ? mean[1] : mean[0], qo = hh ? s[1] : s[0];
                const unsigned t32 = (unsigned)tag;
                st16(rws, WS_G + (rg * 2 + hf) * 1024 + lane * 16, (u32x4){__float_as_uint(mo), t32, __float_as_uint(qo), t32});
                u32x4 o;
#pragma unroll 1
                for (unsigned spins = 0; spins < (1u << 17); ++spins) {
                    o = ld16(rws, WS_G + (rg * 2 + (hf ^ 1)) * 1024 + lane * 16);
                    if (__all(o[1] == t32 && o[3] == t32)) break;
                    __builtin_amdgcn_s_sleep(2);
                }
                const float mp = __uint_as_float(o[0]), dl = mo - mp;
                tab[lane] = (f32x2){0.5f * (mo + mp), 1.0f / sqrtf((qo + __uint_as_float(o[2]) + dl * dl * (FH * 0.25f)) * (1.0f / FH) + 1e-5f)};
            }
            WAIT_L(0); BAR();
            if (kk < 30) {
                dma_pair<0, 8192>(vA0, vA1, Ap, ldsw); dma_pair<16384, 24576>(vA0, vA1, Ap + 128 * LDA, ldsw); dma_pair<RB, RB + 8192>(vB0, vB1, Bp, ldsw);
                dma_pair<32768, 32768 + 8192>(vA0, vA1, Ap + 128, ldsw); dma_pair<32768 + 16384, 32768 + 24576>(vA0, vA1, Ap + 128 + 128 * LDA, ldsw);
                dma_pair<RB + 16384, RB + 16384 + 8192>(vB0, vB1, Bp + 64 * WROW, ldsw); dma_pair<RB + 32768, RB + 32768 + 8192>(vB0, vB1, Bp + 128 * WROW, ldsw);
            }
            f32x2 rs[2], of[2];
#pragma unroll
            for (int m = 0; m < 2; ++m) { const f32x2 t = tab[(m0_ ^ m) * 32 + r]; const float o = (mean[m] - t[0]) * t[1]; rs[m] = (f32x2){t[1], t[1]}; of[m] = (f32x2){o, o}; }
            const lds_p tr = lds + 114688;
#pragma unroll
            for (int i = 0; i < 12; ++i)
                if (i < 8 || 2 * (i - 8) < nx) {
                    const int b = i >> 2, m = b & 1;
                    *(LAS u32x2*)(tr + ((m0_ ^ m) * 32 + r) * 720 + (b == 2 ? 8 + (wid >> 1) : wid) * 64 + (i & 3) * 16 + hh * 8) =
                        (u32x2){cvt2(__builtin_elementwise_fma(d[2 * i], rs[m], of[m])), cvt2(__builtin_elementwise_fma(d[2 * i + 1], rs[m], of[m]))};
                }
            WAIT_L(0); BAR();
#pragma unroll
            for (int i = 0; i < 6; ++i) { int c = tid + 512 * i; const bool ok = c < 2752; c = ok ? c : 0; const int zr = c / 43, zc = c - zr * 43;
                st16(rws, ok ? WS_Z2N + (rg * 64 + zr) * SLB + hf * 688 + zc * 16 : 0x80000000u, *(const LAS u32x4*)(tr + zr * 720 + zc * 16)); }
#pragma unroll
            for (int i = 0; i < 2; ++i) { const int c = tid + 512 * i, zr = c / 10;
                st16(rws, hf == 1 && c < 640 ? WS_Z2N + (rg * 64 + zr) * SLB + 1376 + (c - zr * 10) * 16 : 0x80000000u, (u32x4){0u, 0u, 0u, 0u}); }
        }
        if (kk < 30) {
            u32x4 z1[2][2][2];
            const bool busy = fs0 < 43;
#pragma unroll
            for (int k = 0; k < 4; ++k) aoff[k] = ((wid >> 1) & 1) * 16384 + ((wid & 1) * 64 + r) * 128 + (((2 * k + hh) ^ ((r >> 1) & 7)) << 4);
            const int bo0 = RB + (8 * hh + qq) * 256 + (((2 * grp) ^ qq) << 6) + blk * 32 + p * 8, bo1 = RB + (8 * hh + qq) * 256 + (((2 * grp + 1) ^ qq) << 6) + blk * 32 + p * 8;
            f32x16 acc[2][2];
#pragma unroll
            for (int c = 0; c < 4; ++c) acc[c >> 1][c & 1] = f32x16{};
#pragma unroll
            for (int c = 0; c < 8; ++c) z1[c >> 2][(c >> 1) & 1][c & 1] = (u32x4){0u, 0u, 0u, 0u};
            Ap += 256; Bp += 192 * WROW;
            unsigned sa = 0, sa1 = 32768, sa2 = RA2;
            unsigned la = ldsw + RA2, la1 = ldsw, la2 = ldsw + 32768, lb = 49152;
            const char* Aq = Ap; unsigned lq = la, lq1 = la1, lq2 = la2, vQ0 = vA0, vQ1 = vA1, dQ = dA;
#define SP_SRC(s_) do { if ((s_) == 6) { Ap = (const char*)wsp + (size_t)pm * 256 * SLA; vA1 = arow * SLA + achk + 64 * SLA; dA = 128 * SLA; } \
                if ((s_) == 5) { Bp = (const char*)ws + WS_Z2N + pn * 256; vB0 = brow * SLB + bchk; vB1 = vB0 + 32 * SLB; dB = 64 * SLB; } \
                if (t == 6) { Aq = (const char*)wsp + (size_t)pm * 256 * SLA; vQ0 = arow * SLA + achk; vQ1 = vQ0 + 64 * SLA; dQ = 128 * SLA; } } while (0)
#define SP_LATE() do { dma_pair<0, 8192>(vQ0, vQ1, Aq, lq); dma_one<16384>(vQ0, Aq + dQ, lq); Aq += 128; const unsigned l_ = lq; lq = lq1; lq1 = lq2; lq2 = l_; } while (0)
#define SP_A1() dma_one<24576>(vA1, Ap + dA, la)
#define SP_B0() dma_one<RB>(vB0, Bp, ldsw + lb)
#define SP_B1() dma_one<RB + 8192>(vB1, Bp, ldsw + lb)
#define SP_ADV() do { Ap += 128; Bp += dB; const unsigned l_ = la; la = la1; la1 = la2; la2 = l_; lb = (lb + 16384) & 49152; } while (0)
#define SP_GELU() do { _Pragma("unroll") for (int c = 0; c < 4; ++c) { f32x16& a = acc[c >> 1][c & 1]; z1[c >> 1][c & 1][0] = gelu8(a, 0); z1[c >> 1][c & 1][1] = gelu8(a, 1); a = f32x16{}; } } while (0)
#define SP_MMA(k) do { if (busy) { _Pragma("unroll") for (int c = 0; c < 4; ++c) acc[c >> 1][c & 1] = MFMA32(Bt[c & 1][k], At[c >> 1][k], acc[c >> 1][c & 1]); } } while (0)
#define SP_STEP(PRE, LATE, WAIT0, WAIT1, D0, D1, D2, POST, LASTBAR) do { \
                bf16x8 At[2][4], Bt[2][4]; \
                PRE; \
                const lds_cp pa = lds + sa, pb = lds + (t & 3) * 16384; \
                if (busy) { _Pragma("unroll") for (int k = 0; k < 4; ++k) { Bt[0][k] = ldtr2(pb + bo0 + k * 4096, 1024); At[0][k] = ld128(pa + aoff[k]); At[1][k] = ld128(pa + aoff[k] + 4096); Bt[1][k] = ldtr2(pb + bo1 + k * 4096, 1024); } } \
                LATE; \
                if (grp == 1) { WAIT1; } \
                WAIT_L(0); BAR(); SBAR(); \
                __builtin_amdgcn_s_setprio(1); \
                SP_MMA(0); SBAR(); D0; SBAR(); SP_MMA(1); SBAR(); D1; SBAR(); SP_MMA(2); SBAR(); D2; SBAR(); SP_MMA(3); \
                __builtin_amdgcn_s_setprio(0); \
                SBAR(); \
                POST; \
                if (grp == 0) { WAIT0; } \
                if (LASTBAR) BAR(); \
                const unsigned s_ = sa; sa = sa1; sa1 = sa2; sa2 = s_; } while (0)
            asm volatile("s_waitcnt vmcnt(16) lgkmcnt(0)\n\ts_barrier" ::: "memory");
            if (grp == 1) { SP_A1(); SP_B0(); SP_B1(); SP_ADV(); BAR(); }
            int t = 0;
#pragma unroll 1
            for (; t < 8; ++t)
                SP_STEP({ if (wid == 0) { if (t == 2 && lane == 0) { set_flag(ws, flz, tag); set_flag(ws, F_S + bid, tag); } if (t == 3) { poll_flags(ws, F_L + lane, true, tag); ACQUIRE(); } } SP_SRC(t + grp); },
                        SP_LATE(), { if (t == 0) WAIT_V(16); else WAIT_V(8); }, { if (t == 0) WAIT_V(16); else WAIT_V(6); }, SP_A1(), SP_B0(), SP_B1(), { SP_ADV(); if (t == 7 && grp == 1) SP_GELU(); }, true);
            if (grp == 0) SP_GELU();
#pragma unroll 1
            for (; t < 36; ++t) SP_STEP(NOP_, SP_LATE(), WAIT_V(8), WAIT_V(6), SP_A1(), SP_B0(), SP_B1(), SP_ADV(), true);
#pragma unroll 1
            for (; t < 38; ++t)
                SP_STEP({ if (wid == 0) { SP_CHECK(); } }, SP_LATE(), { if (t == 36) WAIT_V(8); else WAIT_V(6); }, { if (t == 36) WAIT_V(6); else WAIT_V(4); },
                        if (t + grp < 38) SP_A1(), if (t + grp < 37) SP_B0(), if (t + grp < 37) SP_B1(), SP_ADV(), true);
            Q1_LOAD();
#pragma unroll 1
            for (; t < 40; ++t) SP_STEP(NOP_, NOP_, WAIT_V(8), WAIT_V(8), NOP_, NOP_, NOP_, NOP_, !(t == 39 && grp == 1));
            G_STAGE(G_SA(0, 0), rat.A, rat.vA0, rat.vA1); G_STAGE(G_SB(0, 0), rat.B, rat.vA0, rat.vA1); G_STAGE(G_SA(1, 0), rat.A + 8192, rat.vA0, rat.vA1); G_STAGE(G_SB(1, 0), rat.B + 8192, rat.vA0, rat.vA1);
            G_STAGE(G_SA(0, 1), rat.A + 65536, rat.vA0, rat.vA1); G_STAGE(G_SB(0, 1), rat.B + 65536, rat.vA0, rat.vA1); G_STAGE(G_SA(1, 1), rat.A + 73728, rat.vA0, rat.vA1); G_STAGE(G_SB(1, 1), rat.B + 73728, rat.vA0, rat.vA1);
            Q2_LOAD();
            SBAR();
#pragma unroll
            for (int m = 0; m < 2; ++m)
#pragma unroll
                for (int c = 0; c < 4; ++c) {
                    const f32x16& a = acc[m][c >> 1]; const u32x4 z = z1[m][c >> 1][c & 1]; const int s8 = 8 * (c & 1);
                    u32x4 o;
#pragma unroll
                    for (int e = 0; e < 4; ++e) o[e] = cvtpk(bflo(z[e]) * (a[s8 + 2 * e] + bias[m]), bfhi(z[e]) * (a[s8 + 2 * e + 1] + bias[m]));
                    st16(rws, fs0 + c < 43 ? WS_AF + ((256 + fs0 + c) * 64 + tb0 + m) * 1024 + lane * 16 : 0x80000000u, o);
                }
            pf = F_U + pm * 8 + pn;
            Q1_WAIT(20);
        } else {
            WAIT_V(0); WAIT_L(0); BAR();
            if (tid == 0) { set_flag(ws, flz, tag); set_flag(ws, F_S + bid, tag); }
            {
                const int aw = x8 * 2 + (kk - 30);
#pragma unroll
                for (int i = 0; i < 4; ++i) touch(tid * 128 + i * 65536, (const char*)wo + aw * 262144, lds0);
                if (tid < 344) touch(tid * 128, (const char*)wout + aw * 44032, lds0);
            }
            if (wid == 0) { poll_flags(ws, fatt + (lane & 31), lane < 32 && (lane & 15) < 12, tag); ACQUIRE(); }
            WAIT_V(0); BAR();
            g_prologue_b(rat, ldsw); g_prologue_rest(rat, ldsw); G_STAGE(G_SA(1, 1), rat.A + rat.dA1 + rat.dA2, rat.vA0, rat.vA1);
            Q1_LOAD();
            pf = -1;
            Q1_WAIT(0);
        }
    }
    BAR();
    {
        const int kh = wid >> 2, rg = wid & 3;
        const bool early = kk < 24 || kk >= 30;
        const rsrc_t rkv = __builtin_amdgcn_make_buffer_rsrc((void*)kvx, (short)0, 4 * 64 * 16384, 0x00020000);
        const unsigned dv0 = rg * 2048 + lane * 16 + kh * 32768;
        unsigned wrapoff = (unsigned)(h1 * 16 + i1 + 1) * 65536u, wrapto = (unsigned)h2 << 20;
        unsigned sA_ = 0, sB_ = ((unsigned)h1 << 20) + 65536u, lA = lds0 + kh * 65536 + rg * 2048, lB = lA + 16384;
#define A_ADV() do { sA_ = sB_; sB_ += 65536u; if (sB_ == wrapoff) { sB_ = wrapto; wrapoff = (unsigned)(h2 * 16 + i2 + 1) * 65536u; wrapto = 0x40000000u; } const unsigned t_ = lA; lA = lB; lB = t_; } while (0)
#define D_KO(h) (bdma1<8192, (h) * 1024>(dv0, rkv, sA_ + 16384, lA))
#define D_VE(h) (bdma1<32768, (h) * 1024>(dv0, rkv, sA_ + 8192, lA))
#define D_KE(h) (bdma1<0, (h) * 1024>(dv0, rkv, sB_, lB))
#define D_VO(h) (bdma1<40960, (h) * 1024>(dv0, rkv, sA_ + 24576, lA))
#define PUBLISH() do { if (pf >= 0) { if (wid == 0 && lane == 0) set_flag(ws, pf, tag); pf = -1; } } while (0)
        const lds_cp qrd = lds + 131072 + rg * 8192 + lane * 16;
        const lds_cp rbase = lds + kh * 65536 + lane * 16;
        int so = 0;
#pragma unroll 1
        for (int blkno = 0; blkno < 2; ++blkno) {
            const int ib = blkno ? i2 : i1, head = 4 * x8 + (blkno ? h2 : h1);
            const int qrow = 128 * ib + 32 * rg + r - 4 * hh - 64 * kh;
            f32x16 o[4], sA, sB, negm;
            bf16x8 fr[8];
            u32x4 pw0, pw1;
            float mref, l0 = 0.f, l1 = 0.f, resc = 1.0f, ma, mb;
            bool dead = false, pend = false;
            lds_cp pc = rbase + so, pn = rbase + (so ^ 16384);
#pragma unroll
            for (int d = 0; d < 8; ++d) fr[d] = ld128(pc + d * 1024);
            sA = f32x16{};
#pragma unroll
            for (int d = 0; d < 8; ++d) sA = MFMA32(fr[d], q[d], sA);
            asm volatile("s_waitcnt lgkmcnt(0)\n\ts_barrier" ::: "memory");
            A_ADV();
            if (blkno == 1) { D_KO(0); D_KO(1); }
            if (blkno == 1 && wid < 5) {
                if (wid < 4) touch(lane * 128, (const char*)wo + (size_t)(x8 >> 1) * 1048576 + kk * 32768 + wid * 8192, lds0);
                else { const unsigned ur = 192 * (x8 >> 1) + 6 * kk + (lane >> 3); touch((ur > FH - 1 ? FH - 1 : ur) * 1024 + (lane & 7) * 128, wout, lds0); }
            }
#pragma unroll
            for (int d = 0; d < 4; ++d) fr[d] = ld128(pc + 8192 + d * 1024);
#pragma unroll
            for (int d = 0; d < 4; ++d) o[d] = f32x16{};
            if (ib == 0) att_mask(sA, qrow);
            {
                float m = MX3(sA[0], sA[1], sA[2]);
#pragma unroll
                for (int e = 3; e < 15; e += 2) m = MX3(m, sA[e], sA[e + 1]);
                m = swap_max(__builtin_fmaxf(m, sA[15]));
                dead = m == -__builtin_inff();
                mref = dead ? 0.f : m;
#pragma unroll
                for (int e = 0; e < 16; ++e) { sA[e] -= mref; negm[e] = -mref; }
                PIN(negm);
#pragma unroll
                for (int e = 0; e < 6; ++e) sA[e] = EX2(sA[e]);
                pw0[0] = cvtpk(sA[0], sA[1]); pw0[1] = cvtpk(sA[2], sA[3]);
            }
#define A_RD(j, ptr) do { fr[j] = ld128(ptr); } while (0)
#define XE(S, i) S[i] = EX2(S[i])
#define A_P1(SC, SN, KP, VP, E0, E1, E2) do { \
            A_RD(4, (KP) + 4096); SBAR(); SN = MFMA32(fr[0], q[0], negm); XE(SC, 6); XE(SC, 7); pw0[2] = cvtpk(SC[4], SC[5]); PIN(SC); PIN(pw0); SBAR(); \
            A_RD(5, (KP) + 5120); SBAR(); SN = MFMA32(fr[1], q[1], SN); XE(SC, 8); XE(SC, 9); pw0[3] = cvtpk(SC[6], SC[7]); PIN(SC); PIN(pw0); E0; SBAR(); \
            A_RD(6, (KP) + 6144); SBAR(); SN = MFMA32(fr[2], q[2], SN); XE(SC, 10); XE(SC, 11); l0 += SC[0]; PIN(SC); PIN(l0); SBAR(); \
            A_RD(7, (KP) + 7168); SBAR(); SN = MFMA32(fr[3], q[3], SN); XE(SC, 12); XE(SC, 13); l1 += SC[1]; PIN(SC); PIN(l1); SBAR(); \
            A_RD(0, (VP));        SBAR(); SN = MFMA32(fr[4], q[4], SN); XE(SC, 14); XE(SC, 15); pw1[0] = cvtpk(SC[8], SC[9]); PIN(SC); PIN(pw1); E1; SBAR(); \
            A_RD(1, (VP) + 2048); SBAR(); SN = MFMA32(fr[5], q[5], SN); l0 += SC[2]; l1 += SC[3]; l0 += SC[4]; l1 += SC[5]; l0 += SC[6]; PIN(l0); PIN(l1); SBAR(); \
            A_RD(2, (VP) + 4096); SBAR(); SN = MFMA32(fr[6], q[6], SN); l1 += SC[7]; l0 += SC[8]; l1 += SC[9]; l0 += SC[10]; pw1[1] = cvtpk(SC[10], SC[11]); PIN(l0); PIN(l1); PIN(pw1); SBAR(); \
            A_RD(3, (VP) + 6144); SBAR(); SN = MFMA32(fr[7], q[7], SN); l1 += SC[11]; l0 += SC[12]; l1 += SC[13]; pw1[2] = cvtpk(SC[12], SC[13]); PIN(l0); PIN(l1); PIN(pw1); E2; SBAR(); } while (0)
#define PWF(x) __builtin_bit_cast(bf16x8, x)
#define A_P2A(SC, SN, VP, MASK, DQ) do { \
            A_RD(4, (VP) + 1024); A_RD(5, (VP) + 3072); SBAR(); o[0] = MFMA32(fr[0], PWF(pw0), o[0]); l0 += SC[14]; l1 += SC[15]; pw1[3] = cvtpk(SC[14], SC[15]); PIN(l0); PIN(l1); PIN(pw1); SBAR(); \
            A_RD(6, (VP) + 5120); A_RD(7, (VP) + 7168); SBAR(); o[1] = MFMA32(fr[1], PWF(pw0), o[1]); \
            if (MASK) att_mask(SN, DQ); \
            ma = MX3(SN[0], SN[1], SN[2]); mb = MX3(SN[3], SN[4], SN[5]); PIN(ma); PIN(mb); SBAR(); \
            o[2] = MFMA32(fr[2], PWF(pw0), o[2]); ma = MX3(ma, SN[6], SN[7]); mb = MX3(mb, SN[8], SN[9]); PIN(ma); PIN(mb); ma = MX3(ma, SN[10], SN[11]); PIN(ma); SBAR(); \
            o[3] = MFMA32(fr[3], PWF(pw0), o[3]); mb = MX3(mb, SN[12], SN[13]); ma = MX3(ma, SN[14], SN[15]); PIN(ma); PIN(mb); SBAR(); } while (0)
#define A_CHECK(SN) do { if (__builtin_expect(((__builtin_amdgcn_fcmpf(ma, ATT_THR, 2) | __builtin_amdgcn_fcmpf(mb, ATT_THR, 2)) != 0), 0)) {            \
                const float dl_ = __builtin_fmaxf(swap_max(__builtin_fmaxf(ma, mb)), 0.f); mref += dl_; resc = EX2(-dl_); l0 *= resc; l1 *= resc; pend = true; \
                _Pragma("unroll") for (int e = 0; e < 16; ++e) { SN[e] -= dl_; negm[e] = -mref; } PIN(negm); } SBAR(); } while (0)
#define A_RESC() do { if (__builtin_expect(pend, 0)) { pend = false;         \
            _Pragma("unroll") for (int d = 0; d < 4; ++d) _Pragma("unroll") for (int e = 0; e < 16; ++e) o[d][e] *= resc; } } while (0)
#define A_P2B(SN, RD0, RD1, RD2, RD3, D0, D1) do { \
            RD0; SBAR(); o[0] = MFMA32(fr[4], PWF(pw1), o[0]); SBAR(); A_CHECK(SN); D0; SBAR(); \
            RD1; SBAR(); o[1] = MFMA32(fr[5], PWF(pw1), o[1]); XE(SN, 0); XE(SN, 1); PIN(SN); SBAR(); \
            RD2; SBAR(); o[2] = MFMA32(fr[6], PWF(pw1), o[2]); XE(SN, 2); XE(SN, 3); pw0[0] = cvtpk(SN[0], SN[1]); PIN(SN); PIN(pw0); D1; SBAR(); \
            RD3; SBAR(); o[3] = MFMA32(fr[7], PWF(pw1), o[3]); XE(SN, 4); XE(SN, 5); pw0[1] = cvtpk(SN[2], SN[3]); PIN(SN); PIN(pw0); SBAR(); \
            A_RESC(); } while (0)
#define A_P2K(SN, KN, D0, D1) A_P2B(SN, A_RD(0, (KN)), A_RD(1, (KN) + 1024), A_RD(2, (KN) + 2048), A_RD(3, (KN) + 3072), D0, D1)
#pragma unroll 1
            for (int n = 0; n < ib; ++n) {
                A_P1(sA, sB, pc + 8192, pc + 32768, D_VE(0), D_VE(1), D_KE(0));
                A_P2A(sA, sB, pc + 32768, false, 0);
                A_P2K(sB, pn, D_KE(1), D_VO(0));
                A_P1(sB, sA, pn, pc + 32768 + 8192, D_VO(1), NOP_, NOP_);
                A_P2A(sB, sA, pc + 32768 + 8192, (n == ib - 1), qrow - 128 * ib);
                const int ev = blkno == 0 && (early || n == 0) ? n : 6;
                if (ev != 0) WAIT_V(0); else if (kk < 24) WAIT_V(22); else WAIT_V(14);
                asm volatile("s_waitcnt lgkmcnt(0)\n\ts_barrier" ::: "memory");
#define A_EV() do { A_ADV(); if (ev != 0) PUBLISH(); \
                if (ev < 6) { \
                    if (ev == 3 && wid == 0) dma_words(lane * 4, ws + (size_t)(fatt + 32) * 8, lds0 + 131072); \
                    if (ev == 4 && wid == 0) { check_parked<0>(i2 >= 8 ? 8 : 12, lds + 131072, ws, fatt + 32, tag, lane); ACQUIRE(); } \
                    if (ev == 5) Q2_LOAD(); } \
                D_KO(0); } while (0)
                A_P2B(sA, A_RD(0, pn + 8192), A_RD(1, pn + 9216), A_RD(2, pn + 10240), A_RD(3, pn + 11264), A_EV(), D_KO(1));
                so ^= 16384;
                const lds_cp t_ = pc; pc = pn; pn = t_;
            }
            lA = __builtin_amdgcn_readfirstlane(lA); lB = __builtin_amdgcn_readfirstlane(lB); sA_ = __builtin_amdgcn_readfirstlane(sA_); sB_ = __builtin_amdgcn_readfirstlane(sB_);
#define A_QNEXT() do { if (blkno == 0) { _Pragma("unroll") for (int d = 0; d < 8; ++d) q[d] = ld128(qrd + d * 1024); } } while (0)
#define A_LASTBAR() do { asm volatile("s_waitcnt vmcnt(0) lgkmcnt(0)\n\ts_barrier" ::: "memory"); PUBLISH(); } while (0)
            const bool some = kh == 0 || rg >= 2, live = 2 * kh + 1 <= rg;
            const lds_cp vp = pc + 32768 + 8192;
            if (some) A_P1(sA, sB, pc + 8192, pc + 32768, if (blkno == 0) { D_VE(0); D_VE(1); }, if (blkno == 0) { D_KE(0); D_KE(1); }, if (blkno == 0) { D_VO(0); D_VO(1); });
            else if (blkno == 0) { D_VE(0); D_VE(1); D_KE(0); D_KE(1); D_VO(0); D_VO(1); }
            A_QNEXT();
            if (some) {
                A_P2A(sA, sB, pc + 32768, true, qrow - 128 * ib - 32);
                A_P2B(sB, A_RD(0, vp), A_RD(1, vp + 2048), A_RD(2, vp + 4096), A_RD(3, vp + 6144), NOP_, NOP_);
            }
            if (live) {
#pragma unroll
                for (int j = 0; j < 4; ++j) fr[4 + j] = ld128(vp + j * 2048 + 1024);
#pragma unroll
                for (int e = 6; e < 16; ++e) sB[e] = EX2(sB[e]);
#pragma unroll
                for (int e = 0; e < 16; e += 2) { l0 += sB[e]; l1 += sB[e + 1]; }
#pragma unroll
                for (int e = 0; e < 4; ++e) { if (e >= 2) pw0[e] = cvtpk(sB[2 * e], sB[2 * e + 1]); pw1[e] = cvtpk(sB[8 + 2 * e], sB[9 + 2 * e]); }
            }
            A_LASTBAR();
            if (live) {
#pragma unroll
                for (int j = 0; j < 4; ++j) o[j] = MFMA32(fr[j], PWF(pw0), o[j]);
#pragma unroll
                for (int j = 0; j < 4; ++j) o[j] = MFMA32(fr[4 + j], PWF(pw1), o[j]);
            }
            {
                const int sb_own = kh == 0 ? 131072 + rg * 8192 : (rg >> 1) * 65536 + 32768 + so + (rg & 1) * 8192;
                const int sb_par = kh == 1 ? 131072 + rg * 8192 : (rg >> 1) * 65536 + 32768 + so + (rg & 1) * 8192;
                const float mown = dead ? -__builtin_inff() : mref, lown = l0 + l1;
                *(LAS f32x2*)(lds + kh * 65536 + so + 8192 + rg * 512 + lane * 8) = (f32x2){mown, lown};
                const f32x16 s0 = kh ? o[0] : o[2], s1 = kh ? o[1] : o[3], k0 = kh ? o[2] : o[0], k1 = kh ? o[3] : o[1];
#pragma unroll
                for (int qd = 0; qd < 4; ++qd) {
                    *(LAS f32x4*)(lds + sb_own + qd * 1024 + lane * 16) = (f32x4){s0[4 * qd], s0[4 * qd + 1], s0[4 * qd + 2], s0[4 * qd + 3]};
                    *(LAS f32x4*)(lds + sb_own + 4096 + qd * 1024 + lane * 16) = (f32x4){s1[4 * qd], s1[4 * qd + 1], s1[4 * qd + 2], s1[4 * qd + 3]};
                }
                asm volatile("s_waitcnt lgkmcnt(0)\n\ts_barrier" ::: "memory");
                const f32x2 ml = *(const LAS f32x2*)(lds + (kh ^ 1) * 65536 + so + 8192 + rg * 512 + lane * 8);
                f32x16 p0, p1;
#pragma unroll
                for (int qd = 0; qd < 4; ++qd) {
                    const f32x4 a = *(const LAS f32x4*)(lds + sb_par + qd * 1024 + lane * 16), b = *(const LAS f32x4*)(lds + sb_par + 4096 + qd * 1024 + lane * 16);
#pragma unroll
                    for (int e = 0; e < 4; ++e) { p0[4 * qd + e] = a[e]; p1[4 * qd + e] = b[e]; }
                }
                const float mf = __builtin_fmaxf(mown, ml[0]);
                const float fo = EX2(mown - mf), fp = EX2(ml[0] - mf);
                const float inv = 1.0f / swap_add(fo * lown + fp * ml[1]);
                const float ca = fo * inv, cb = fp * inv;
#pragma unroll
                for (int e = 0; e < 16; ++e) { p0[e] = ca * k0[e] + cb * p0[e]; p1[e] = ca * k1[e] + cb * p1[e]; }
                const unsigned dst = WS_AF + ((head * 8 + 4 * kh) * 64 + 4 * ib + rg) * 1024 + lane * 16;
                st16(rws, dst, pack8(p0, 0, 1.0f)); st16(rws, dst + 65536, pack8(p0, 1, 1.0f));
                st16(rws, dst + 2 * 65536, pack8(p1, 0, 1.0f)); st16(rws, dst + 3 * 65536, pack8(p1, 1, 1.0f));
                pf = F_C + ib * 32 + head;
            }
            so ^= 16384;
        }
#undef A_RD
#undef A_P1
#undef A_P2A
#undef A_P2B
#undef A_P2K
#undef A_CHECK
#undef XE
#undef PWF
    }

    {
        int lane;
        asm volatile("v_mbcnt_lo_u32_b32 %0, -1, 0\n\tv_mbcnt_hi_u32_b32 %0, -1, %0" : "=v"(lane));
        const int r = lane & 31, hh = lane >> 5, blk = (lane >> 4) & 1, qq = (lane & 15) >> 2, p = lane & 3;
        const unsigned brow = 4 * wid + (lane >> 4), bchk = ((lane & 15) ^ (((lane >> 4) & 3) << 2)) << 4;
        const int sq = x8 >> 1, tm = (x8 & 1) * 8 + (kk >> 2), tn = kk & 3, rb = wid & 3, kp = wid >> 2;
        OutArgs oa;
        oa.ra = __builtin_amdgcn_make_buffer_rsrc((void*)(ws + WS_AF), (short)0, NKS * 65536, 0x00020000);
        oa.wo = (const char*)wo + (size_t)sq * 1048576 + tn * 256; oa.wu = (const char*)wout + tn * 256;
        oa.wv = brow * 1024 + bchk;
#pragma unroll
        for (int j = 0; j < 6; ++j) { const unsigned ur = 192 * sq + 32 * j + brow; oa.uv[j] = (ur > FH - 1 ? FH - 1 : ur) * 1024 + bchk; }
        oa.va = kp * 131072 + (tm * 4 + rb) * 1024 + lane * 16; oa.sc = sq * 64 * 65536; oa.su = (256 + 12 * sq) * 65536; oa.ldsw = ldsw;
#pragma unroll
        for (int c = 0; c < 4; ++c) oa.rd[c] = lds + (kp * 32 + 4 * hh + qq) * 256 + ((c ^ qq) << 6) + blk * 32 + p * 8;
        WAIT_L(0); BAR();
        out_w<0>(oa); out_w<1>(oa); out_w<2>(oa); out_w<3>(oa);
        WAIT_V(8); BAR();
        if (wid == 0 && lane == 0) set_flag(ws, pf, tag);
        if (wid == 1) {
            if (bid == 0) {
#pragma unroll 1
                for (int i = 0; i < 4; ++i) poll_flags(ws, F_S + i * 64 + lane, true, tag);
                if (lane == 0) set_flag(ws, F_E, tag);
            }
            poll_flags(ws, lane < 8 ? F_C + tm * 32 + 8 * sq + lane : F_U + (tm >> 1) * 8 + ((3 * sq) >> 1) + (lane & 1), lane < 10, tag);
        }
        out_w<4>(oa); out_w<5>(oa); out_w<6>(oa);
        WAIT_V(10); BAR();
        u32x4 af[6][2];
        bf16x8 wf[2][4];
        f32x16 acc[4] = {};
        out_a<0>(oa, af); out_a<1>(oa, af); out_a<2>(oa, af); out_a<3>(oa, af); out_a<4>(oa, af);
#pragma unroll
        for (int c = 0; c < 4; ++c) wf[0][c] = ldtr2(oa.rd[c], 2048);
#define OS(t) out_step<t>(oa, af, wf, acc)
        OS(0); OS(1); OS(2); OS(3); OS(4); OS(5); OS(6); OS(7); OS(8); OS(9); OS(10); OS(11); OS(12); OS(13); OS(14); OS(15); OS(16); OS(17); OS(18);
#undef OS
        const f32x16 s0 = kp ? acc[0] : acc[2], s1 = kp ? acc[1] : acc[3];
        f32x16 k0 = kp ? acc[2] : acc[0], k1 = kp ? acc[3] : acc[1];
#pragma unroll
        for (int g = 0; g < 4; ++g) {
            *(LAS f32x4*)(lds + 16384 + wid * 8192 + g * 1024 + lane * 16) = (f32x4){s0[4 * g], s0[4 * g + 1], s0[4 * g + 2], s0[4 * g + 3]};
            *(LAS f32x4*)(lds + 16384 + wid * 8192 + 4096 + g * 1024 + lane * 16) = (f32x4){s1[4 * g], s1[4 * g + 1], s1[4 * g + 2], s1[4 * g + 3]};
        }
        const size_t orow = (size_t)(tm * 128 + rb * 32 + r) * DM + tn * 128 + kp * 64 + 4 * hh;
        u32x2 xv[8];
        if (rb == sq) {
#pragma unroll
            for (int g = 0; g < 8; ++g) xv[g] = *(const u32x2*)(x + orow + 8 * g);
        }
        WAIT_L(0); BAR();
#pragma unroll
        for (int g = 0; g < 4; ++g) {
            const f32x4 a = *(const LAS f32x4*)(lds + 16384 + (wid ^ 4) * 8192 + g * 1024 + lane * 16), b = *(const LAS f32x4*)(lds + 16384 + (wid ^ 4) * 8192 + 4096 + g * 1024 + lane * 16);
#pragma unroll
            for (int e = 0; e < 4; ++e) { k0[4 * g + e] += a[e]; k1[4 * g + e] += b[e]; }
        }
        const unsigned pb = WS_P + ((tm * 4 + tn) * 4 + rb) * 65536 + kp * 8192 + lane * 16;
        const int fp = F_P + (((tm * 4 + tn) * 4 + rb) * 2 + kp) * 4;
        if (rb != sq) {
#pragma unroll
            for (int g = 0; g < 4; ++g) {
                st16(rws, pb + sq * 16384 + g * 1024, __builtin_bit_cast(u32x4, (f32x4){k0[4 * g], k0[4 * g + 1], k0[4 * g + 2], k0[4 * g + 3]}));
                st16(rws, pb + sq * 16384 + 4096 + g * 1024, __builtin_bit_cast(u32x4, (f32x4){k1[4 * g], k1[4 * g + 1], k1[4 * g + 2], k1[4 * g + 3]}));
            }
            WAIT_V(0);
            if (lane == 0) set_flag(ws, fp + sq, tag);
        }
        if (rb == sq) {
            poll_flags(ws, fp + (lane & 3), (lane & 3) != sq, tag);
            u32x4 pp[3][8];
#pragma unroll
            for (int j = 0; j < 3; ++j)
#pragma unroll
                for (int g = 0; g < 8; ++g) pp[j][g] = ld16(rws, pb + ((sq + 1 + j) & 3) * 16384 + g * 1024);
#pragma unroll
            for (int g = 0; g < 8; ++g) {
                const f32x16& k = g < 4 ? k0 : k1; const int e0 = 4 * (g & 3);
                const f32x4 a = __builtin_bit_cast(f32x4, pp[0][g]), b = __builtin_bit_cast(f32x4, pp[1][g]), c = __builtin_bit_cast(f32x4, pp[2][g]);
                const f32x4 t = ((f32x4){k[e0], k[e0 + 1], k[e0 + 2], k[e0 + 3]} + a) + (b + c);
                *(u32x2*)(out + orow + 8 * g) = (u32x2){cvtpk(bflo(xv[g][0]) + t[0], bfhi(xv[g][0]) + t[1]), cvtpk(bflo(xv[g][1]) + t[2], bfhi(xv[g][1]) + t[3])};
            }
        }
    }
}

extern "C" void kernel_launch(void* const* d_in, const int* in_sizes, int n_in, void* d_out, int out_size, void* d_ws, size_t ws_size, hipStream_t stream) {
    (void)in_sizes; (void)n_in; (void)out_size; (void)ws_size;
    fused_fwd<<<256, 512, 0, stream>>>((const bf16_t*)d_in[0], (const bf16_t*)d_in[1], (const bf16_t*)d_in[2], (const bf16_t*)d_in[3], (const bf16_t*)d_in[4], (const bf16_t*)d_in[5],
                                       (const bf16_t*)d_in[7], (const bf16_t*)d_in[8], (const bf16_t*)d_in[9], (const bf16_t*)d_in[10], (bf16_t*)d_out, (unsigned char*)d_ws);
}
```
